```python
import torch

def _softmax(x, axis=-1):
    return torch.softmax(x, dim=axis)
def _gelu(x):
    return torch.nn.functional.gelu(x, approximate="tanh")
def _silu(x):
    return torch.nn.functional.silu(x)
def _sigmoid(x):
    return torch.sigmoid(x)
def _layernorm(x, axis=-1, eps=1e-5):
    m = x.mean(dim=axis, keepdim=True)
    v = x.var(dim=axis, keepdim=True, unbiased=False)
    return (x - m) / torch.sqrt(v + eps)
def _scatter_add(tgt, idx, src, axis=0):
    return tgt.clone().index_add_(axis, idx, src)
def _segment_sum(src, seg, n):
    out = torch.zeros((n,) + src.shape[1:], dtype=src.dtype)
    return out.index_add_(0, seg, src)
def _pad(x, pw):
    flat = tuple(v for pair in reversed(pw) for v in pair)
    return torch.nn.functional.pad(x, flat)

kernel_name = 'concat_heads__compressed_kv_cla__ssm_conv_h3__prompt'

def setup_inputs():

    _w = (lambda a: (lambda t: t.to(torch.bfloat16) if t.is_floating_point() else t)(torch.as_tensor(a)))
    def _A_setup():
        B, H, Sq, Sk, D, M = 1, 16, 2048, 2048, 256, 448
        Dc = 4 * D
        return {
            "x": _w((lambda *s: torch.randn(*s, dtype=torch.bfloat16) * 3.2)(B, Sq, M)),
            "c_kv": _w((lambda *s: torch.randn(*s, dtype=torch.bfloat16) * 0.05)(B, Sk, Dc)),
            "w_q": _w((lambda *s: torch.randn(*s, dtype=torch.bfloat16) * 3.2)(M, H, D) * (M ** -0.5)),
            "w_uk": _w((lambda *s: torch.randn(*s, dtype=torch.bfloat16) * 3.2)(Dc, H, D) * (Dc ** -0.5)),
            "w_uv": _w((lambda *s: torch.randn(*s, dtype=torch.bfloat16) * 0.05)(Dc, H, D) * (Dc ** -0.5)),
            "w_o": _w((lambda *s: torch.randn(*s, dtype=torch.bfloat16) * 0.05)(H, D, M) * ((H * D) ** -0.5)),
        }
    _A_in = _A_setup()

    def _B_setup():
        B, S, D = 1, 2048, 448
        ks = 4
        return {
            "q": _w((lambda *s: torch.randn(*s, dtype=torch.bfloat16) * 0.05)(B, S, D)),
            "k": _w((lambda *s: torch.randn(*s, dtype=torch.bfloat16) * 0.1)(B, S, D)),
            "v": _w((lambda *s: torch.randn(*s, dtype=torch.bfloat16) * 0.1)(B, S, D)),
            "shift_k": _w((lambda *s: torch.randn(*s, dtype=torch.bfloat16) * 0.1)(D, ks) * 0.5),
            "ssm_k": _w((lambda *s: torch.randn(*s, dtype=torch.bfloat16) * 0.1)(D, S) * (1.0 / S ** 0.5)),
            "ssm_D": _w((lambda *s: torch.randn(*s, dtype=torch.bfloat16) * 0.1)(D)),
        }
    _B_in = _B_setup()

    return {
        "A_x": _A_in["x"],
        "A_c_kv": _A_in["c_kv"],
        "A_w_q": _A_in["w_q"],
        "A_w_uk": _A_in["w_uk"],
        "A_w_uv": _A_in["w_uv"],
        "A_w_o": _A_in["w_o"],
        "B_q": _B_in["q"],
        "B_k": _B_in["k"],
        "B_v": _B_in["v"],
        "B_shift_k": _B_in["shift_k"],
        "B_ssm_k": _B_in["ssm_k"],
        "B_ssm_D": _B_in["ssm_D"],
    }

def reference(A_x, A_c_kv, A_w_q, A_w_uk, A_w_uv, A_w_o, B_q, B_k, B_v, B_shift_k, B_ssm_k, B_ssm_D):
    def _A_ref(x, c_kv, w_q, w_uk, w_uv, w_o):
        B, Sq, M = x.shape[0], x.shape[1], x.shape[2]
        Sk = c_kv.shape[1]
        H, D = w_q.shape[1], w_q.shape[2]
        q = torch.einsum("bqm,mhd->bhqd", x, w_q)
        k = torch.einsum("bkc,chd->bhkd", c_kv, w_uk)
        v = torch.einsum("bkc,chd->bhkd", c_kv, w_uv)
        scores = torch.einsum("bhqd,bhkd->bhqk", q, k) * (D ** -0.5)
        off = Sk - Sq
        qi = torch.arange(Sq)[:, None]
        kj = torch.arange(Sk)[None, :]
        cmask = kj <= (qi + off)
        scores = torch.where(cmask[None, None], scores, -1e9)
        p = _softmax(scores, axis=-1)
        ctx = torch.einsum("bhqk,bhkd->bhqd", p, v)
        return torch.einsum("bhqd,hdm->bqm", ctx, w_o)

    def _B_ref(q, k, v, shift_k, ssm_k, ssm_D):
        B, S, D = q.shape
        ks = shift_k.shape[-1]
        k_pad = _pad(k, ((0, 0), (ks - 1, 0), (0, 0)))
        k_shift = torch.zeros_like(k)
        for t in range(ks):
            k_shift = k_shift + k_pad[:, t:t + S, :] * shift_k[:, ks - 1 - t][None, None, :]
        kv = k_shift * v
        L = 2 * S
        kv_pad = _pad(kv, ((0, 0), (0, S), (0, 0)))
        kf_pad = _pad(ssm_k, ((0, 0), (0, S)))
        kv_f = (lambda x, axes=(-2,-1): torch.fft.rfft2(x.float(), dim=axes))(kv_pad[:, None, :, :], axes=(1, 2))
        kf_f = (lambda x, axes=(-2,-1): torch.fft.rfft2(x.float(), dim=axes))(kf_pad[:, None, :], axes=(1, 2))
        kf_f = (lambda x, *d: x.permute(*d))(kf_f, 1, 2, 0)
        y_f = kv_f * kf_f[None, :, :, :]
        y = (lambda x, s, axes=(-2,-1): torch.fft.irfft2(x, s=s, dim=axes).to(torch.bfloat16))(y_f, (1, L), axes=(1, 2))[:, 0, :S, :]
        y = y + kv * ssm_D[None, None, :]
        return q * y

    a = _A_ref(A_x, A_c_kv, A_w_q, A_w_uk, A_w_uv, A_w_o)
    b = _B_ref(B_q, B_k, B_v, B_shift_k, B_ssm_k, B_ssm_D)
    return torch.concatenate([a, b], axis=-1)
```

```cpp
#include <hip/hip_runtime.h>
#include <hip/hip_bf16.h>
#include <utility>

typedef short    bf16x8 __attribute__((ext_vector_type(8)));
typedef short    s16x4  __attribute__((ext_vector_type(4)));
typedef float    f32x16 __attribute__((ext_vector_type(16)));
typedef float    f32x4  __attribute__((ext_vector_type(4)));
typedef float    f32x2  __attribute__((ext_vector_type(2)));
typedef __bf16   bf16x2 __attribute__((ext_vector_type(2)));
typedef unsigned u32x4  __attribute__((ext_vector_type(4)));
#define DI __device__ __forceinline__
#define LAS __attribute__((address_space(3)))
#define SBAR() __builtin_amdgcn_sched_barrier(0)
#define MFMA32(a, b, c) __builtin_amdgcn_mfma_f32_32x32x16_bf16((a), (b), (c), 0, 0, 0)
#define MFMA16(a, b, c) __builtin_amdgcn_mfma_f32_16x16x32_bf16((a), (b), (c), 0, 0, 0)
DI void mfma16a(f32x4& c, const bf16x8& a, const bf16x8& b) { asm volatile("v_mfma_f32_16x16x32_bf16 %0, %1, %2, %0" : "+a"(c) : "v"(a), "v"(b)); }
#define MFMA_DRAIN() asm volatile("s_nop 7\n\ts_nop 7" ::: "memory")
typedef LAS const char* lds_cptr;
typedef __amdgpu_buffer_rsrc_t srd_t;

DI unsigned rfl(unsigned x) { return __builtin_amdgcn_readfirstlane(x); }
DI unsigned cvtpk(float lo, float hi) { f32x2 v = {lo, hi}; bf16x2 b = __builtin_convertvector(v, bf16x2); return __builtin_bit_cast(unsigned, b); }
DI bf16x8 lds_rd128(lds_cptr p) { return *(const LAS bf16x8*)p; }
DI s16x4 lds_tr(lds_cptr p) { return __builtin_amdgcn_ds_read_tr16_b64_v4i16((LAS s16x4*)p); }
DI srd_t mk_srd(const void* p, unsigned bytes) { return __builtin_amdgcn_make_buffer_rsrc((void*)p, (short)0, (int)bytes, 0x00020000); }
DI void dma16(unsigned m0v, unsigned voff, srd_t srd, unsigned soff) {
  asm volatile("s_mov_b32 m0, %0\n\ts_nop 0\n\tbuffer_load_dwordx4 %1, %2, %3 offen lds" :: "s"(m0v), "v"(voff), "s"(srd), "s"(soff) : "memory");
}
DI void st16(u32x4 v, srd_t srd, unsigned voff, unsigned soff) { asm volatile("buffer_store_dwordx4 %0, %1, %2, %3 offen\n\ts_nop 1" :: "v"(v), "v"(voff), "s"(srd), "s"(soff) : "memory"); }
DI void st16_sc1_a(f32x4 v, srd_t srd, unsigned voff, unsigned soff) { asm volatile("buffer_store_dwordx4 %0, %1, %2, %3 offen sc1\n\ts_nop 1" :: "a"(v), "v"(voff), "s"(srd), "s"(soff) : "memory"); }
DI void st16_sc1(f32x4 v, srd_t srd, unsigned voff, unsigned soff) { asm volatile("buffer_store_dwordx4 %0, %1, %2, %3 offen sc1\n\ts_nop 1" :: "v"(v), "v"(voff), "s"(srd), "s"(soff) : "memory"); }
constexpr int SEQ = 2048, NH = 16, DH = 256, DM = 448, DC = 1024;
constexpr unsigned WRS = NH * DH * 2;
constexpr float QSCALE = 1.4426950408889634f / 16.0f;

struct K1C {
  unsigned ldsb, wid;
  unsigned vx_kv[2], vx_q[2], vw[4];
  srd_t sX, sW, sXq, sWq;
  unsigned sx_kv, sw_kv, sx_q, sw_q;
  lds_cptr xr_kv[2], xr_q[2], wr[8];
};
template <int KIND> DI void k1_piece(const K1C& c, int st, int buf, int j) {
  const unsigned lb = c.ldsb + buf * 32768u;
  const int p = j >> 1;
  if constexpr (KIND == 0) {
    if (!(j & 1)) dma16(lb + 65536u + (8u * c.wid + p) * 1024u, c.vw[(p & 1) + 2 * (p >> 2)], c.sW, c.sw_kv + (unsigned)(st * 64 + 2 * p) * WRS);
    else          dma16(lb + (8u * c.wid + p) * 1024u, c.vx_kv[(p >> 1) & 1], c.sX, c.sx_kv + (unsigned)(8 * p) * (DC * 2) + st * 128u);
  } else if constexpr (KIND == 1) {
    if (!(j & 1)) dma16(lb + 65536u + (8u * c.wid + p) * 1024u, c.vw[(p & 1) + 2 * (p >> 2)], c.sWq, c.sw_q + (unsigned)(st * 64 + 2 * p) * WRS);
    else if (p < 4) dma16(lb + (4u * c.wid + p) * 1024u, c.vx_q[(p >> 1) & 1], c.sXq, c.sx_q + (unsigned)(8 * p) * (DM * 2) + st * 128u);
  }
}
struct Frags { bf16x8 x[8]; s16x4 wl[8], wh[8]; };
template <int NX> DI void k1_read(const K1C& c, Frags& f, int buf, int ks, int i) {
  const int so = buf * 32768;
  if (i < 2 || i >= 2 + NX) { const int k = i < 2 ? i : i - NX, nb = k >> 1, t = k & 1;
    const s16x4 v = lds_tr(c.wr[nb] + so + ks * 16384 + t * 2048); if (t) f.wh[nb] = v; else f.wl[nb] = v; }
  else { const int mb = i - 2; f.x[mb] = lds_rd128((NX == 8 ? c.xr_kv[ks] : c.xr_q[ks]) + so + (mb >> 1) * 4096 + (mb & 1) * 1024); }
}
DI bf16x8 cat(s16x4 l, s16x4 h) { return __builtin_shufflevector(l, h, 0, 1, 2, 3, 4, 5, 6, 7); }
template <bool WA, int NX, int NXN, int KB> DI void k1_step(const K1C& c, f32x4 (&acc)[8][8], Frags& f0, Frags& f1, int buf, int sb) {
  constexpr int G = 8 * NX;
#pragma unroll
  for (int ks = 0; ks < 2; ++ks) {
    Frags& fc = ks ? f1 : f0; Frags& fn = ks ? f0 : f1;
    if (ks == 1) { asm volatile("s_waitcnt vmcnt(0) lgkmcnt(0)\n\ts_barrier" ::: "memory"); SBAR(); }
#pragma unroll
    for (int g = 0; g < G; ++g) {
      const int nb = g / NX, mb = g % NX;
      const bf16x8 w = cat(fc.wl[nb], fc.wh[nb]);
      if (ks == 0 && g == 0) asm volatile("s_nop 1" :: "v"(w), "v"(fc.x[0]));
      if (WA) mfma16a(acc[mb][nb], w, fc.x[mb]); else mfma16a(acc[mb][nb], fc.x[mb], w);
      if (NX == 8) {
        if (!(g & 1)) {
          if (ks == 0) { if (g / 2 < 24) k1_read<8>(c, fn, buf, 1, g / 2); }
          else if (NXN == 8) { if (g / 2 < 24) k1_read<8>(c, fn, buf ^ 1, 0, g / 2); }
          else if (NXN == 4) { if (g / 2 < 20) k1_read<4>(c, fn, buf ^ 1, 0, g / 2); }
        }
        if (ks == 1 && (g & 3) == 1) k1_piece<KB>(c, sb, buf, g >> 2);
      } else {
        if (g < 20) { if (ks == 0) k1_read<4>(c, fn, buf, 1, g); else if (NXN == 4) k1_read<4>(c, fn, buf ^ 1, 0, g); }
        if (ks == 1 && (g & 1) == 1) k1_piece<KB>(c, sb, buf, g >> 1);
      }
      SBAR();
    }
  }
}
DI u32x4 pack8(const f32x4& a, const f32x4& b, float sc) { u32x4 w = {cvtpk(a[0] * sc, a[1] * sc), cvtpk(a[2] * sc, a[3] * sc), cvtpk(b[0] * sc, b[1] * sc), cvtpk(b[2] * sc, b[3] * sc)}; return w; }
DI u32x4 pack8(const f32x4& a, const f32x4& b) { u32x4 w = {cvtpk(a[0], a[1]), cvtpk(a[2], a[3]), cvtpk(b[0], b[1]), cvtpk(b[2], b[3])}; return w; }
template <bool WA> DI void k1_kv(const K1C& c, Frags& f0, Frags& f1, srd_t so, unsigned lane, unsigned kb0, unsigned wc) {
  f32x4 acc[8][8];
#pragma unroll
  for (int i = 0; i < 8; ++i)
#pragma unroll
    for (int j = 0; j < 8; ++j) { acc[i][j] = f32x4{}; asm volatile("" : "+a"(acc[i][j])); }
  asm volatile("s_nop 1"); SBAR();
  for (int t = 0; t < 14; t += 2) {
    k1_step<WA, 8, 8, 0>(c, acc, f0, f1, 0, t + 2);
    k1_step<WA, 8, 8, 0>(c, acc, f0, f1, 1, t + 3);

  }
  k1_step<WA, 8, 8, 1>(c, acc, f0, f1, 0, 0);
  k1_step<WA, 8, 4, 1>(c, acc, f0, f1, 1, 1);
  MFMA_DRAIN(); SBAR();
#pragma unroll
  for (int i = 0; i < 8; ++i)
#pragma unroll
    for (int j = 0; j < 8; ++j) asm volatile("" : "+a"(acc[i][j]));

  const unsigned g = lane >> 4, rho = lane & 15u;
  if (WA) {
    const unsigned vo = (g >> 1) * 1024u + (16u * (rho >> 3) + (rho & 7u) + 32u * (g & 1u)) * 16u;
    const unsigned b0 = kb0 * 16384u + 4u * wc * 2048u;
#pragma unroll
    for (int mb = 0; mb < 8; ++mb)
#pragma unroll
      for (int db = 0; db < 4; ++db) {
        st16(pack8(acc[mb][2 * db], acc[mb][2 * db + 1]), so, vo, b0 + (mb >> 1) * 16384u + (mb & 1) * 128u + db * 2048u);
        SBAR();
      }
  } else {
    const unsigned vo = (g >> 1) * 8192u + (rho + 32u * (g & 1u)) * 16u;
    const unsigned b0 = kb0 * 16384u + 4u * wc * 1024u;
#pragma unroll
    for (int mp = 0; mp < 4; ++mp)
#pragma unroll
      for (int nb = 0; nb < 8; ++nb) {
        st16(pack8(acc[2 * mp][nb], acc[2 * mp + 1][nb]), so, vo, b0 + mp * 16384u + (nb >> 1) * 1024u + (nb & 1) * 256u);
        SBAR();
      }
  }
}

__global__ __launch_bounds__(256, 1) void k1_proj(const unsigned short* __restrict__ x, const unsigned short* __restrict__ ckv, const unsigned short* __restrict__ wq,
                                                  const unsigned short* __restrict__ wuk, const unsigned short* __restrict__ wuv,
                                                  char* __restrict__ Qf, char* __restrict__ Kf, char* __restrict__ Vf, unsigned* __restrict__ flags) {
  extern __shared__ __attribute__((aligned(16))) char lds[];
  const unsigned tid = threadIdx.x, lane = tid & 63u, wid = rfl(tid >> 6), wr = wid >> 1, wc = wid & 1;
  const unsigned b = blockIdx.x, xcd = b & 7u, idx = b >> 3;
  const unsigned pm = idx & 7u, which = idx >> 3, h = 2u * xcd + (which & 1u), isV = which >> 1;
  const unsigned qm = idx & 15u, hq = 2u * xcd + (idx >> 4);

  if (b == 0) flags[tid] = 0u;
  K1C c;
  c.ldsb = (unsigned)(size_t)lds; c.wid = wid;
  c.sX = mk_srd(ckv, SEQ * DC * 2); c.sW = mk_srd(isV ? wuv : wuk, DC * WRS); c.sXq = mk_srd(x, SEQ * DM * 2); c.sWq = mk_srd(wq, DM * WRS);
#pragma unroll
  for (int par = 0; par < 2; ++par) {
    const unsigned ch = 16u * ((lane & 7u) ^ (4u * par + (lane >> 4)));
    c.vx_kv[par] = (lane >> 3) * (DC * 2) + ch; c.vx_q[par] = (lane >> 3) * (DM * 2) + ch;
  }
#pragma unroll
  for (int v = 0; v < 4; ++v) {
    const unsigned f = (2u * (v & 1) + (lane >> 5)) | (4u * (v >> 1));
    c.vw[v] = (lane >> 5) * WRS + 16u * ((lane & 31u) ^ (f << 1));
  }
  c.sx_kv = (256u * pm + 64u * wid) * (DC * 2); c.sw_kv = 16u * wid * WRS + h * (DH * 2);
  c.sx_q = (128u * qm + 32u * wid) * (DM * 2);  c.sw_q = 16u * wid * WRS + hq * (DH * 2);
  const unsigned rho = lane & 15u, g = lane >> 4;
#pragma unroll
  for (int ks = 0; ks < 2; ++ks) {
    const unsigned row = 16u * (rho >> 3) + (rho & 7u), ch = 16u * ((4u * ks + g) ^ (rho >> 1));
    c.xr_kv[ks] = (lds_cptr)lds + (wr * 128u + row) * 128u + ch; c.xr_q[ks] = (lds_cptr)lds + (wr * 64u + row) * 128u + ch;
  }
  { const unsigned q = (lane & 15u) >> 2, p = lane & 3u, f = q | ((g & 1u) << 2);
#pragma unroll
    for (int nb = 0; nb < 8; ++nb) { c.wr[nb] = (lds_cptr)lds + 65536u + (8u * g + q) * 512u + (8u * wc + ((unsigned)nb ^ f)) * 32u + 8u * p; asm volatile("" : "+v"(c.wr[nb])); } }
#pragma unroll
  for (int j = 0; j < 16; ++j) k1_piece<0>(c, 0, 0, j);
#pragma unroll
  for (int j = 0; j < 16; ++j) k1_piece<0>(c, 1, 1, j);
  Frags f0, f1;
  asm volatile("s_waitcnt vmcnt(16)\n\ts_barrier" ::: "memory");
#pragma unroll
  for (int i = 0; i < 24; ++i) k1_read<8>(c, f0, 0, 0, i);
  SBAR();
  { const srd_t so = mk_srd((isV ? Vf : Kf) + (size_t)h * (SEQ * DH * 2), SEQ * DH * 2);
    if (isV) k1_kv<false>(c, f0, f1, so, lane, 8u * pm + 4u * wr, wc); else k1_kv<true>(c, f0, f1, so, lane, 8u * pm + 4u * wr, wc); }
  f32x4 acc[8][8];
#pragma unroll
  for (int i = 0; i < 4; ++i)
#pragma unroll
    for (int j = 0; j < 8; ++j) { acc[i][j] = f32x4{}; asm volatile("" : "+a"(acc[i][j])); }
  asm volatile("s_nop 1"); SBAR();
  k1_step<true, 4, 4, 1>(c, acc, f0, f1, 0, 2);
  k1_step<true, 4, 4, 1>(c, acc, f0, f1, 1, 3);
  k1_step<true, 4, 4, 1>(c, acc, f0, f1, 0, 4);
  k1_step<true, 4, 4, 1>(c, acc, f0, f1, 1, 5);
  k1_step<true, 4, 4, 1>(c, acc, f0, f1, 0, 6);
  k1_step<true, 4, 4, 2>(c, acc, f0, f1, 1, 0);
  k1_step<true, 4, 0, 2>(c, acc, f0, f1, 0, 0); MFMA_DRAIN(); SBAR();
#pragma unroll
  for (int i = 0; i < 4; ++i)
#pragma unroll
    for (int j = 0; j < 8; ++j) asm volatile("" : "+a"(acc[i][j]));

  {
    const srd_t so = mk_srd(Qf + (size_t)hq * (SEQ * DH * 2), SEQ * DH * 2);
    const unsigned vo = (g >> 1) * 1024u + (16u * (rho >> 3) + (rho & 7u) + 32u * (g & 1u)) * 16u;
    const unsigned b0 = (4u * qm + 2u * wr) * 16384u + 4u * wc * 2048u;
#pragma unroll
    for (int mb = 0; mb < 4; ++mb)
#pragma unroll
      for (int db = 0; db < 4; ++db)
        st16(pack8(acc[mb][2 * db], acc[mb][2 * db + 1], QSCALE), so, vo, b0 + (mb >> 1) * 16384u + (mb & 1) * 128u + db * 2048u);
  }

}

DI u32x4 pack8w(const f32x16& a, int s, float sc) {
  u32x4 w = {cvtpk(a[8 * s] * sc, a[8 * s + 1] * sc), cvtpk(a[8 * s + 2] * sc, a[8 * s + 3] * sc), cvtpk(a[8 * s + 4] * sc, a[8 * s + 5] * sc), cvtpk(a[8 * s + 6] * sc, a[8 * s + 7] * sc)};
  return w;
}
constexpr float THR = 8.0f;
constexpr int NA = 17;
DI void mfma32_acc_a(f32x16& c, const bf16x8& a, const u32x4& b) { asm volatile("v_mfma_f32_32x32x16_bf16 %0, %1, %2, %0" : "+a"(c) : "v"(a), "v"(b)); }
DI void mfma32_acc_v(f32x16& c, const bf16x8& a, const bf16x8& b) { asm volatile("v_mfma_f32_32x32x16_bf16 %0, %1, %2, %0" : "+v"(c) : "v"(a), "a"(b)); }
DI void mfma32_init_v(f32x16& d, const bf16x8& a, const bf16x8& b, const f32x16& c) { asm volatile("v_mfma_f32_32x32x16_bf16 %0, %1, %2, %3" : "=&v"(d) : "v"(a), "a"(b), "v"(c)); }
DI float max3f(float a, float b, float c) { float r; asm("v_max3_f32 %0, %1, %2, %3" : "=v"(r) : "v"(a), "v"(b), "v"(c)); return r; }
DI float max2f(float a, float b) { float r; asm("v_max_f32_e32 %0, %1, %2" : "=v"(r) : "v"(a), "v"(b)); return r; }
DI float fadd_s(float a, float b) { float r; asm volatile("v_add_f32_e32 %0, %1, %2" : "=v"(r) : "v"(a), "v"(b)); return r; }
DI float fsub_s(float a, float b) { float r; asm("v_sub_f32_e32 %0, %1, %2" : "=v"(r) : "v"(a), "v"(b)); return r; }
#define PINV(x) asm volatile("" : "+v"(x))
struct K2C {
  unsigned ldsb, vlane;
  srd_t sK, sV;
  lds_cptr kr, vr;
  unsigned kslot_w, vslot_w, vslot_r;
  unsigned ksoff, vsoff;
  bf16x8 q[16]; f32x16 o[8]; f32x16 s0, s1, negm; u32x4 p[4]; bf16x8 fr[16];
  float m, l0, l1, mx0, mx1, fct, e14, e15; bool pend;
  int dq;
  bool domask;
};
template <int J> DI void k2_dma(unsigned m0v, unsigned voff, srd_t srd, unsigned soff) {
  if constexpr ((J & 3) == 0) asm volatile("s_mov_b32 m0, %0\n\ts_nop 0\n\tbuffer_load_dwordx4 %1, %2, %3 offen lds" :: "s"(m0v + (J >> 2) * 4096u), "v"(voff), "s"(srd), "s"(soff + (J >> 2) * 4096u) : "memory");
  else asm volatile("buffer_load_dwordx4 %0, %1, %2 offen offset:%3 lds" :: "v"(voff), "s"(srd), "s"(soff + (J >> 2) * 4096u), "i"((J & 3) * 1024) : "memory");
}
DI void k2_dma8(unsigned m0v, unsigned voff, srd_t srd, unsigned soff) {
  k2_dma<0>(m0v, voff, srd, soff); k2_dma<1>(m0v, voff, srd, soff); k2_dma<2>(m0v, voff, srd, soff); k2_dma<3>(m0v, voff, srd, soff);
  k2_dma<4>(m0v, voff, srd, soff); k2_dma<5>(m0v, voff, srd, soff); k2_dma<6>(m0v, voff, srd, soff); k2_dma<7>(m0v, voff, srd, soff);
}
DI void k2_mask(f32x16& s, int dq) {
#pragma unroll
  for (int r = 0; r < 16; ++r) if ((r & 3) + 8 * (r >> 2) > dq) s[r] = -__builtin_inff();
}
template <int... Is> using iseq = std::integer_sequence<int, Is...>;
DI void mfma32_acc_a4(f32x16& c, const bf16x8& a, const u32x4& b, const bf16x8& x, const bf16x8& y, const bf16x8& z) { asm volatile("v_mfma_f32_32x32x16_bf16 %0, %1, %2, %0" : "+a"(c) : "v"(a), "v"(b), "v"(x), "v"(y), "v"(z)); }
DI void mfma32_acc_v4(f32x16& c, const bf16x8& a, const bf16x8& b, const bf16x8& x, const bf16x8& y, const bf16x8& z) { asm volatile("v_mfma_f32_32x32x16_bf16 %0, %1, %2, %0" : "+v"(c) : "v"(a), "a"(b), "v"(x), "v"(y), "v"(z)); }
DI void mfma32_init_v4(f32x16& d, const bf16x8& a, const bf16x8& b, const f32x16& c, const bf16x8& x, const bf16x8& y, const bf16x8& z) { asm volatile("v_mfma_f32_32x32x16_bf16 %0, %1, %2, %3" : "=&v"(d) : "v"(a), "a"(b), "v"(c), "v"(x), "v"(y), "v"(z)); }
DI void max4(float& m, float a, float b, float c, float d) { asm volatile("v_max3_f32 %0, %0, %1, %2\n\tv_max3_f32 %0, %0, %3, %4" : "+v"(m) : "v"(a), "v"(b), "v"(c), "v"(d)); }
DI void max6(float& m, float a, float b, float c, float d, float e, float f) { asm volatile("v_max3_f32 %0, %0, %1, %2\n\tv_max3_f32 %0, %0, %3, %4\n\tv_max3_f32 %0, %0, %5, %6" : "+v"(m) : "v"(a), "v"(b), "v"(c), "v"(d), "v"(e), "v"(f)); }
DI float max4i(float a, float b, float c, float d) { float m; asm volatile("v_max_f32_e32 %0, %1, %2\n\tv_max3_f32 %0, %0, %3, %4" : "=&v"(m) : "v"(a), "v"(b), "v"(c), "v"(d)); return m; }
template <int e> DI void k2_lsum(K2C& c, float v) { if constexpr (e & 1) c.l1 = fadd_s(c.l1, v); else c.l0 = fadd_s(c.l0, v); }
template <bool FIN, bool QK, int g> DI void k2_gapA(K2C& c) {
  if constexpr (QK) {
    if constexpr (g == 0) mfma32_init_v4(c.s0, c.fr[0], c.q[0], c.negm, c.fr[1], c.fr[2], c.fr[3]);
    else if constexpr (g == 16) mfma32_init_v4(c.s1, c.fr[0], c.q[0], c.negm, c.fr[1], c.fr[2], c.fr[3]);
    else if constexpr ((g & 3) == 0) mfma32_acc_v4(g < 16 ? c.s0 : c.s1, c.fr[g & 15], c.q[g & 15], c.fr[(g + 1) & 15], c.fr[(g + 2) & 15], c.fr[(g + 3) & 15]);
    else mfma32_acc_v(g < 16 ? c.s0 : c.s1, c.fr[g & 15], c.q[g & 15]);
    SBAR();
    if constexpr (g < 24) c.fr[(g + 8) & 15] = lds_rd128(c.kr + (g + 8) * 1024);
  }
  if constexpr (!QK && g <= 9) { asm volatile("s_nop 0"); SBAR(); }
  if constexpr (FIN && (QK ? g >= 24 : g < 8)) c.fr[g & 7] = lds_rd128(c.vr + (g & 7) * 1024);
  if constexpr (FIN && g <= 8) {
    if constexpr (g < 8) c.s1[8 + g] = __builtin_amdgcn_exp2f(c.s1[8 + g]);
    k2_lsum<g>(c, c.s1[7 + g]);
    if constexpr (g < 4) c.p[2][g] = cvtpk(c.s1[2 * g], c.s1[2 * g + 1]);
    else if constexpr (g < 7) c.p[3][g - 4] = cvtpk(c.s1[2 * g], c.s1[2 * g + 1]);
    else if constexpr (g == 8) c.p[3][3] = cvtpk(c.s1[14], c.s1[15]);
    if constexpr (g < 8) PINV(c.s1);
    if constexpr (g < 4) PINV(c.p[2]); else PINV(c.p[3]);
  }
  if constexpr (QK) {
    if constexpr (g >= 10 && g < 18) k2_dma<g - 10>(c.ldsb + 65536u + c.vslot_w, c.vlane, c.sV, c.vsoff);
    if constexpr (g == 18) { if (c.domask) { SBAR(); k2_mask(c.s0, c.dq); } }
    if constexpr (g == 20) { c.mx0 = max4i(c.s0[0], c.s0[1], c.s0[2], c.s0[3]); }
    if constexpr (g >= 21 && g < 24) { constexpr int r = 4 * (g - 20); max4(c.mx0, c.s0[r], c.s0[r + 1], c.s0[r + 2], c.s0[r + 3]); }
  }
  SBAR();
}
template <bool FIN, bool QK, int... Gs> DI void k2_phaseA_(K2C& c, iseq<Gs...>) { (k2_gapA<FIN, QK, Gs>(c), ...); }
template <bool FIN, bool QK> DI void k2_phaseA(K2C& c) {
  if constexpr (QK && !FIN) {
#pragma unroll
    for (int i = 0; i < 8; ++i) c.fr[i] = lds_rd128(c.kr + i * 1024);
  }
  k2_phaseA_<FIN, QK>(c, std::make_integer_sequence<int, 32>{});
}
template <bool PV, bool SM, int g> DI void k2_gapB(K2C& c) {
  if constexpr (SM && g == 24) {
    asm volatile("s_waitcnt vmcnt(8) lgkmcnt(0)\n\ts_barrier" ::: "memory");
    c.kr = (lds_cptr)((unsigned)(size_t)c.kr ^ 32768u);
    SBAR();
  }
  if constexpr (PV) {
    if constexpr ((g & 3) == 0) mfma32_acc_a4(c.o[g & 7], c.fr[g & 15], c.p[g >> 3], c.fr[(g + 1) & 15], c.fr[(g + 2) & 15], c.fr[(g + 3) & 15]);
    else mfma32_acc_a(c.o[g & 7], c.fr[g & 15], c.p[g >> 3]);
    SBAR();
    if constexpr (g < 2) { c.fr[(8 + 2 * g) & 15] = lds_rd128(c.vr + (8 + 2 * g) * 1024); c.fr[(9 + 2 * g) & 15] = lds_rd128(c.vr + (9 + 2 * g) * 1024); }
    else if constexpr (g < 8) c.fr[(g + 10) & 15] = lds_rd128(c.vr + (g + 10) * 1024);
    else if constexpr (g < 10) { c.fr[(2 * g + 2) & 15] = lds_rd128(c.vr + (2 * g + 2) * 1024); c.fr[(2 * g + 3) & 15] = lds_rd128(c.vr + (2 * g + 3) * 1024); }
    else if constexpr (g < 20) c.fr[(g + 12) & 15] = lds_rd128(c.vr + (g + 12) * 1024);
  }
  if constexpr (!PV && g >= 7) { asm volatile("s_nop 0"); SBAR(); }
  if constexpr (SM) {
    if constexpr (g >= 24) { c.fr[(g + 8) & 15] = lds_rd128(c.kr + (g - 24) * 1024); k2_dma<g - 24>(c.ldsb + c.kslot_w, c.vlane, c.sK, c.ksoff); }
    if constexpr (g == 2) { if (c.domask) { SBAR(); k2_mask(c.s1, c.dq - 32); } }
    if constexpr (g == 3) { max6(c.mx0, c.s1[0], c.s1[1], c.s1[2], c.s1[3], c.s1[4], c.s1[5]); }
    if constexpr (g == 4) { max6(c.mx0, c.s1[6], c.s1[7], c.s1[8], c.s1[9], c.s1[10], c.s1[11]); }
    if constexpr (g == 5) { max4(c.mx0, c.s1[12], c.s1[13], c.s1[14], c.s1[15]); c.mx1 = c.mx0; PINV(c.mx1); }
    if constexpr (g == 6) {
      if constexpr (!PV) asm volatile("s_nop 1");
      auto pr = __builtin_amdgcn_permlane32_swap(__float_as_uint(c.mx0), __float_as_uint(c.mx1), false, false);
      const float rm = max2f(__uint_as_float(pr[0]), __uint_as_float(pr[1]));
      if constexpr (!PV) {
        const float dl = max2f(rm, -1e30f); c.m = dl;
#pragma unroll
        for (int r = 0; r < 16; ++r) { c.s0[r] = fsub_s(c.s0[r], dl); c.s1[r] = fsub_s(c.s1[r], dl); c.negm[r] = -dl; }
        PINV(c.negm);
      } else if (__builtin_expect(__any(rm > THR), 0)) {
        const float dl = max2f(rm, 0.f); c.m = fadd_s(c.m, dl); c.fct = __builtin_amdgcn_exp2f(-dl);
#pragma unroll
        for (int r = 0; r < 16; ++r) { c.s0[r] = fsub_s(c.s0[r], dl); c.s1[r] = fsub_s(c.s1[r], dl); c.negm[r] = -c.m; }
        c.l0 *= c.fct; c.l1 *= c.fct; c.pend = true;
      }
    }
    if constexpr (g >= 7 && g < 23) c.s0[g - 7] = __builtin_amdgcn_exp2f(c.s0[g - 7]);
    if constexpr (g >= 8 && g < 24) k2_lsum<g>(c, c.s0[g - 8]);
    if constexpr (g >= 24) c.s1[g - 24] = __builtin_amdgcn_exp2f(c.s1[g - 24]);
    if constexpr (g >= 25) k2_lsum<g>(c, c.s1[g - 25]);
    if constexpr (g >= 16 && g < 24) { constexpr int w = g - 16; c.p[w >> 2][w & 3] = cvtpk(c.s0[2 * w], c.s0[2 * w + 1]); PINV(c.p[w >> 2]); }
    if constexpr (g >= 7 && g < 23) PINV(c.s0);
    if constexpr (g >= 24) PINV(c.s1);
  }
  SBAR();
}
template <bool PV, bool SM, int... Gs> DI void k2_phaseB_(K2C& c, iseq<Gs...>) { (k2_gapB<PV, SM, Gs>(c), ...); }
template <bool PV, bool SM> DI void k2_phaseB(K2C& c) {
  if constexpr (!PV) { MFMA_DRAIN(); SBAR(); }
  k2_phaseB_<PV, SM>(c, std::make_integer_sequence<int, 32>{});
  if constexpr (PV && SM) {
    if (__builtin_expect(c.pend, 0)) {
      MFMA_DRAIN(); SBAR();
#pragma unroll
      for (int d = 0; d < 8; ++d) { asm volatile("" : "+a"(c.o[d])); c.o[d] *= c.fct; asm volatile("" : "+a"(c.o[d])); SBAR(); }
      c.pend = false;
    }
    SBAR();
  }
}

__global__ __launch_bounds__(256, 1) void k2_attn(const char* __restrict__ Qf, const char* __restrict__ Kf, const char* __restrict__ Vf, char* __restrict__ Cf,
                                                  char* __restrict__ part, unsigned* __restrict__ flags) {
  extern __shared__ __attribute__((aligned(16))) char lds[];
  const unsigned tid = threadIdx.x, lane = tid & 63u, wid = rfl(tid >> 6), hi = lane >> 5, r32 = lane & 31u;
  const unsigned b = blockIdx.x, xcd = b & 7u, idx = b >> 3, h = 2u * xcd + (idx >> 4), pi = (idx >> 1) & 7u, roleB = idx & 1u;
  const unsigned pair = h * 8u + pi;

  const int n1 = roleB ? 32 - 2 * (int)pi - NA : NA, tb1 = roleB ? NA : 0, ntot = roleB ? 34 - NA : NA;
  auto tile_off = [&](int p) -> unsigned { return p >= ntot ? 0x40000000u : (unsigned)(p < n1 ? tb1 + p : p - n1) * 32768u + wid * 8192u; };
  K2C c;
  c.ldsb = (unsigned)(size_t)lds + wid * 8192u; c.vlane = lane * 16u;
  c.sK = mk_srd(Kf + (size_t)h * (SEQ * DH * 2), SEQ * DH * 2); c.sV = mk_srd(Vf + (size_t)h * (SEQ * DH * 2), SEQ * DH * 2);
  unsigned qb = 15u - pi;
  { const char* qp = Qf + ((size_t)h * 64u + qb * 4u + wid) * 16384u + lane * 16u;
#pragma unroll
    for (int i = 0; i < 16; ++i) c.q[i] = *(const bf16x8*)(qp + i * 1024); }
  SBAR();
  k2_dma8(c.ldsb, c.vlane, c.sK, tile_off(0)); k2_dma8(c.ldsb + 32768u, c.vlane, c.sK, tile_off(1)); k2_dma8(c.ldsb + 65536u, c.vlane, c.sV, tile_off(0));
  c.kr = (lds_cptr)lds + lane * 16u; c.kslot_w = 0u; c.vslot_w = 32768u; c.vslot_r = 0u;
  asm volatile("s_waitcnt vmcnt(16)\n\ts_barrier" ::: "memory");

  int p = 0;
  const int nseg = roleB ? 2 : 1;
  for (int seg = 0; seg < nseg; ++seg) {
    const int pend_ = seg == 0 ? n1 : ntot;
    int t = seg == 0 ? tb1 : 0;
    const int row0 = (int)(qb * 128u + wid * 32u);
    auto setup = [&]() {
      c.vsoff = tile_off(p + 1); c.ksoff = tile_off(p + 2);
      c.domask = 64 * t + 63 > row0; c.dq = row0 + (int)r32 - 64 * t - 4 * (int)hi;
    };
    auto advance = [&]() {
      c.kslot_w ^= 32768u; c.vslot_w = c.vslot_w == 65536u ? 0u : c.vslot_w + 32768u; ++p; ++t;
      c.vr = (lds_cptr)lds + 65536u + c.vslot_r + lane * 16u; c.vslot_r = c.vslot_r == 65536u ? 0u : c.vslot_r + 32768u;
    };
#pragma unroll
    for (int d = 0; d < 8; ++d) { c.o[d] = f32x16{}; asm volatile("" : "+a"(c.o[d])); }
    c.negm = f32x16{}; PINV(c.negm); c.l0 = 0.f; c.l1 = 0.f; c.pend = false; c.fct = 1.f;
#pragma unroll
    for (int i = 0; i < 16; ++i) asm volatile("" : "+a"(c.q[i]));
    asm volatile("s_nop 1"); SBAR();
    setup();
    k2_phaseA<false, true>(c); k2_phaseB<false, true>(c);
    if (seg == 1 && lane == 0) __hip_atomic_fetch_add(flags + pair * 2u, 1u, __ATOMIC_RELAXED, __HIP_MEMORY_SCOPE_AGENT);
    advance();

    while (p < pend_) {
      setup();
      k2_phaseA<true, true>(c); k2_phaseB<true, true>(c);
      advance();
    }

    if (seg + 1 < nseg) {
      const char* qp = Qf + ((size_t)h * 64u + pi * 4u + wid) * 16384u + lane * 16u;
#pragma unroll
      for (int i = 0; i < 16; ++i) c.q[i] = *(const bf16x8*)(qp + i * 1024);
    }
    SBAR();
    k2_phaseA<true, false>(c); k2_phaseB<true, false>(c);
    MFMA_DRAIN(); SBAR();
#pragma unroll
    for (int d = 0; d < 8; ++d) asm volatile("" : "+a"(c.o[d]));

    float l = c.l0 + c.l1;
    { auto pr = __builtin_amdgcn_permlane32_swap(__float_as_uint(l), __float_as_uint(l), false, false); l = __uint_as_float(pr[0]) + __uint_as_float(pr[1]); }
    const srd_t sP = mk_srd(part + ((size_t)pair * 4u + wid) * 33792u, 33792u);
    if (roleB && seg == 0) {
#pragma unroll
      for (int d = 0; d < 8; ++d) {
#pragma unroll
        for (int k = 0; k < 4; ++k) {
          f32x4 v = {c.o[d][4 * k], c.o[d][4 * k + 1], c.o[d][4 * k + 2], c.o[d][4 * k + 3]};
          st16_sc1_a(v, sP, c.vlane, (4 * d + k) * 1024);
        }
        SBAR();
      }
      f32x4 v = {c.m, l, 0.f, 0.f};
      st16_sc1(v, sP, c.vlane, 32 * 1024);
    } else {
      const srd_t sC = mk_srd(Cf + ((size_t)(qb * 4u + wid) * NH + h) * 16384u, 16384u);
      if (roleB) {
        const float rl = 1.0f / l;
#pragma unroll
        for (int d = 0; d < 8; ++d) {
#pragma unroll
          for (int s = 0; s < 2; ++s) st16(pack8w(c.o[d], s, rl), sC, c.vlane, (2 * d + s) * 1024);
          SBAR();
        }
      } else {
        for (int spin = 0; spin < (1 << 22); ++spin) {
          if (rfl(__hip_atomic_load(flags + pair * 2u, __ATOMIC_RELAXED, __HIP_MEMORY_SCOPE_AGENT)) >= 4u) break;
          __builtin_amdgcn_s_sleep(2);
        }
        asm volatile("s_waitcnt vmcnt(0)\n\ts_barrier" ::: "memory");

        const f32x4 ml = __builtin_bit_cast(f32x4, __builtin_amdgcn_raw_buffer_load_b128(sP, c.vlane, 32 * 1024, 16));
        const float mm = fmaxf(c.m, ml[0]), wa = __builtin_amdgcn_exp2f(c.m - mm), wb = __builtin_amdgcn_exp2f(ml[0] - mm);
        const float rl = 1.0f / (wa * l + wb * ml[1]), fa = wa * rl, fb = wb * rl;
#pragma unroll
        for (int dd = 0; dd < 2; ++dd) {
          f32x4 ob[16];
#pragma unroll
          for (int k = 0; k < 16; ++k) ob[k] = __builtin_bit_cast(f32x4, __builtin_amdgcn_raw_buffer_load_b128(sP, c.vlane, (16 * dd + k) * 1024, 16));
#pragma unroll
          for (int d4 = 0; d4 < 4; ++d4) {
            const int d = 4 * dd + d4;
#pragma unroll
            for (int s = 0; s < 2; ++s) {
              u32x4 w;
#pragma unroll
              for (int e = 0; e < 4; ++e) { const int r = 8 * s + 2 * e;
                w[e] = cvtpk(c.o[d][r] * fa + ob[4 * d4 + (r >> 2)][r & 3] * fb, c.o[d][r + 1] * fa + ob[4 * d4 + ((r + 1) >> 2)][(r + 1) & 3] * fb); }
              st16(w, sC, c.vlane, (2 * d + s) * 1024);
            }
          }
          SBAR();
        }
      }
    }

    qb = pi;
  }

}
constexpr int OUTW = 2 * DM;
DI float bf2f(unsigned h) { return __uint_as_float(h << 16); }
DI float rbf(float x) { return __uint_as_float(cvtpk(x, x) << 16); }
DI void mfma32_b(f32x16& c, const bf16x8& a, const bf16x8& b) { c = MFMA32(a, b, c); }

template <bool LAST> DI void k3_block(f32x16 (&acc)[2][2], bf16x8 (&cf)[8][2], lds_cptr wr0, lds_cptr wr1, unsigned ldsw, unsigned vc, unsigned vw, srd_t sC, srd_t sW, unsigned sc, unsigned sw) {
#pragma unroll
  for (int j = 0; j < 8; ++j) {
    if (LAST) {
      switch (j) {
        case 0: asm volatile("s_waitcnt vmcnt(28)" : "+v"(cf[j][0]), "+v"(cf[j][1]) :: "memory"); break;
        case 1: asm volatile("s_waitcnt vmcnt(24)" : "+v"(cf[j][0]), "+v"(cf[j][1]) :: "memory"); break;
        case 2: asm volatile("s_waitcnt vmcnt(20)" : "+v"(cf[j][0]), "+v"(cf[j][1]) :: "memory"); break;
        case 3: asm volatile("s_waitcnt vmcnt(16)" : "+v"(cf[j][0]), "+v"(cf[j][1]) :: "memory"); break;
        case 4: asm volatile("s_waitcnt vmcnt(12)" : "+v"(cf[j][0]), "+v"(cf[j][1]) :: "memory"); break;
        case 5: asm volatile("s_waitcnt vmcnt(8)" : "+v"(cf[j][0]), "+v"(cf[j][1]) :: "memory"); break;
        case 6: asm volatile("s_waitcnt vmcnt(4)" : "+v"(cf[j][0]), "+v"(cf[j][1]) :: "memory"); break;
        default: asm volatile("s_waitcnt vmcnt(0)" : "+v"(cf[j][0]), "+v"(cf[j][1]) :: "memory"); break;
      }
    } else asm volatile("s_waitcnt vmcnt(28)" : "+v"(cf[j][0]), "+v"(cf[j][1]) :: "memory");
    const bf16x8 w0 = cat(lds_tr(wr0 + j * 2048), lds_tr(wr0 + j * 2048 + 1024));
    const bf16x8 w1 = cat(lds_tr(wr1 + j * 2048), lds_tr(wr1 + j * 2048 + 1024));
    mfma32_b(acc[0][0], w0, cf[j][0]); mfma32_b(acc[0][1], w0, cf[j][1]);
    mfma32_b(acc[1][0], w1, cf[j][0]); mfma32_b(acc[1][1], w1, cf[j][1]);
    SBAR();
    if (!LAST) {
      asm volatile("buffer_load_dwordx4 %0, %1, %2, %3 offen" : "=v"(cf[j][0]) : "v"(vc), "s"(sC), "s"(sc + j * 1024u) : "memory");
      asm volatile("buffer_load_dwordx4 %0, %1, %2, %3 offen" : "=v"(cf[j][1]) : "v"(vc), "s"(sC), "s"(sc + j * 1024u + 262144u) : "memory");
      dma16(ldsw + j * 2048u, vw, sW, sw + (16u * j) * (DM * 2)); dma16(ldsw + j * 2048u + 1024u, vw, sW, sw + (16u * j + 8u) * (DM * 2));
      SBAR();
    }
  }
}
DI void k3_outproj(char* lds, unsigned blk, const char* Cf, const unsigned short* wo, unsigned short* out) {
  const unsigned tid = threadIdx.x, lane = tid & 63u, wid = rfl(tid >> 6), hi = lane >> 5;
  const unsigned xcd = blk & 7u, i = blk >> 3, tm = 4u * xcd + i / 7u, tn = i % 7u;
  const srd_t sC = mk_srd(Cf, SEQ * NH * DH * 2), sW = mk_srd(wo, NH * DH * DM * 2);
  const unsigned ldsw = (unsigned)(size_t)lds + wid * 16384u;
  const unsigned vc = lane * 16u, vw = (lane >> 3) * (DM * 2) + 16u * ((lane & 7u) ^ (((lane >> 4) & 1u) << 2));
  unsigned sc = 2u * tm * 262144u + wid * 65536u, sw = wid * 1024u * (DM * 2) + tn * 128u;
  const unsigned q = (lane & 15u) >> 2, p = lane & 3u, blk16 = (lane >> 4) & 1u;
  const unsigned ro = (4u * hi + q) * 128u + 32u * blk16 + 16u * (p & 1u) + 8u * (p >> 1), sx = ((q >> 1) & 1u) * 64u;
  const lds_cptr wr0 = (lds_cptr)lds + wid * 16384u + ro + sx, wr1 = (lds_cptr)lds + wid * 16384u + ro + (64u ^ sx);
  f32x16 acc[2][2] = {}; bf16x8 cf[8][2];
#pragma unroll
  for (int j = 0; j < 8; ++j) {
    asm volatile("buffer_load_dwordx4 %0, %1, %2, %3 offen" : "=v"(cf[j][0]) : "v"(vc), "s"(sC), "s"(sc + j * 1024u) : "memory");
    asm volatile("buffer_load_dwordx4 %0, %1, %2, %3 offen" : "=v"(cf[j][1]) : "v"(vc), "s"(sC), "s"(sc + j * 1024u + 262144u) : "memory");
    dma16(ldsw + j * 2048u, vw, sW, sw + (16u * j) * (DM * 2)); dma16(ldsw + j * 2048u + 1024u, vw, sW, sw + (16u * j + 8u) * (DM * 2));
  }
  for (int it = 0; it < 7; ++it) {
    sc += 8192u; sw += 128u * (DM * 2);
    k3_block<false>(acc, cf, wr0, wr1, ldsw, vc, vw, sC, sW, sc, sw);
  }
  k3_block<true>(acc, cf, wr0, wr1, ldsw, vc, vw, sC, sW, sc, sw);
  LAS f32x4* red = (LAS f32x4*)lds;
#pragma unroll
  for (int t = 0; t < 4; ++t)
#pragma unroll
    for (int k = 0; k < 4; ++k) { const f32x16& a = acc[t >> 1][t & 1]; red[(wid * 16u + t * 4 + k) * 64u + lane] = (f32x4){a[4 * k], a[4 * k + 1], a[4 * k + 2], a[4 * k + 3]}; }
  __syncthreads();
  f32x4 s[4];
#pragma unroll
  for (int k = 0; k < 4; ++k) s[k] = (red[(wid * 4u + k) * 64u + lane] + red[(16u + wid * 4u + k) * 64u + lane]) + (red[(32u + wid * 4u + k) * 64u + lane] + red[(48u + wid * 4u + k) * 64u + lane]);
  unsigned short* op = out + (size_t)(64u * tm + 32u * (wid & 1u) + (lane & 31u)) * OUTW + 64u * tn + 32u * (wid >> 1) + 8u * hi;
  *(u32x4*)op = pack8(s[0], s[1]); *(u32x4*)(op + 16) = pack8(s[2], s[3]);
}

constexpr int HCOPY = 4160, HCH = 4 * HCOPY, XROW = 80, XCH = 96 * XROW;
DI void k3_ssm(char* lds, unsigned blk, const unsigned short* bq, const unsigned short* bk, const unsigned short* bv, const unsigned short* shk,
               const unsigned short* ssmk, const unsigned short* ssmd, unsigned short* out) {
  const unsigned tid = threadIdx.x, lane = tid & 63u, wid = rfl(tid >> 6), hi = lane >> 5, r = lane & 31u;
  const unsigned c0 = 2u * (28u * (blk & 7u) + (blk >> 3));
  unsigned kw[11], vw[8], qw[8];
#pragma unroll
  for (int i = 0; i < 11; ++i) { const int t = 8 * (int)tid - 3 + i; kw[i] = t >= 0 ? *(const unsigned*)(bk + (size_t)t * DM + c0) : 0u; }
#pragma unroll
  for (int i = 0; i < 8; ++i) vw[i] = *(const unsigned*)(bv + (size_t)(8u * tid + i) * DM + c0);
#pragma unroll
  for (int i = 0; i < 8; ++i) qw[i] = *(const unsigned*)(bq + (size_t)(8u * tid + i) * DM + c0);
#pragma unroll
  for (int ch = 0; ch < 2; ++ch) {
    const u32x4* hp = (const u32x4*)(ssmk + (size_t)(c0 + ch) * SEQ);
#pragma unroll
    for (int pass = 0; pass < 2; ++pass) {
      const int n = (int)tid + 256 * pass;
      if (pass == 0 || tid < 4u) {
        const int m = n - 4;
        const u32x4 z = {0u, 0u, 0u, 0u};
        const u32x4 A = m >= 0 ? hp[m] : z, B = (m + 1 >= 0 && m + 1 < 256) ? hp[m + 1] : z;
        const unsigned w[6] = {A[0], A[1], A[2], A[3], B[0], B[1]};
        unsigned o[5];
#pragma unroll
        for (int k = 0; k < 5; ++k) o[k] = __builtin_amdgcn_alignbyte(w[k + 1], w[k], 2);
        LAS char* dst = (LAS char*)lds + ch * HCH + n * 16;
        *(LAS u32x4*)(dst) = (u32x4){o[0], o[1], o[2], o[3]};
        *(LAS u32x4*)(dst + HCOPY) = (u32x4){w[1], w[2], w[3], w[4]};
        *(LAS u32x4*)(dst + 2 * HCOPY) = (u32x4){o[1], o[2], o[3], o[4]};
        *(LAS u32x4*)(dst + 3 * HCOPY) = (u32x4){w[2], w[3], w[4], w[5]};
      }
    }
  }
  float kv[2][8];
  const u32x4 sk = *(const u32x4*)(shk + (size_t)c0 * 4);
#pragma unroll
  for (int ch = 0; ch < 2; ++ch) {
    const float s0 = bf2f(sk[2 * ch] & 0xffffu), s1 = bf2f(sk[2 * ch] >> 16), s2 = bf2f(sk[2 * ch + 1] & 0xffffu), s3 = bf2f(sk[2 * ch + 1] >> 16);
    float kf[11];
#pragma unroll
    for (int i = 0; i < 11; ++i) kf[i] = bf2f(ch ? kw[i] >> 16 : kw[i] & 0xffffu);
#pragma unroll
    for (int i = 0; i < 8; ++i) {
      float a = rbf(kf[i] * s3); a = rbf(a + rbf(kf[i + 1] * s2)); a = rbf(a + rbf(kf[i + 2] * s1)); a = rbf(a + rbf(kf[i + 3] * s0));
      kv[ch][i] = rbf(a * bf2f(ch ? vw[i] >> 16 : vw[i] & 0xffffu));
    }
    LAS char* xb = (LAS char*)lds + 2 * HCH + ch * XCH;
    *(LAS u32x4*)(xb + (32u + (tid >> 2)) * XROW + (tid & 3u) * 16u) = (u32x4){cvtpk(kv[ch][7], kv[ch][6]), cvtpk(kv[ch][5], kv[ch][4]), cvtpk(kv[ch][3], kv[ch][2]), cvtpk(kv[ch][1], kv[ch][0])};
    if (tid < 160u) *(LAS u32x4*)(xb + tid * 16u) = (u32x4){0u, 0u, 0u, 0u};
  }
  __syncthreads();
  const unsigned ch = wid >> 1, dh = wid & 1u;
  typedef short s16x4v __attribute__((ext_vector_type(4)));
  const lds_cptr ha = (lds_cptr)lds + ch * HCH + (r & 3u) * HCOPY + (24u - 16u * dh - 8u * hi + 4u * (r >> 2)) * 2u;
  const lds_cptr xa = (lds_cptr)lds + 2 * HCH + ch * XCH + (r + 1u) * XROW + (2u * dh + hi) * 16u;
  f32x16 y0 = {}, y1 = {};
#pragma unroll 8
  for (int e = 0; e < 32; ++e) {
    const bf16x8 A = cat(*(const LAS s16x4v*)(ha + e * 64), *(const LAS s16x4v*)(ha + e * 64 + 8));
    y1 = MFMA32(A, lds_rd128(xa + (63 - e) * XROW), y1);
    y0 = MFMA32(A, lds_rd128(xa + (31 - e) * XROW), y0);
  }
#pragma unroll 8
  for (int e = 32; e < 64; ++e) {
    const bf16x8 A = cat(*(const LAS s16x4v*)(ha + e * 64), *(const LAS s16x4v*)(ha + e * 64 + 8));
    y1 = MFMA32(A, lds_rd128(xa + (63 - e) * XROW), y1);
  }
  __syncthreads();
  LAS char* ys = (LAS char*)lds + wid * 9216u;
#pragma unroll
  for (int k = 0; k < 4; ++k) {
    *(LAS f32x4*)(ys + r * 144u + (8 * k + 4u * hi) * 4u) = (f32x4){y0[4 * k], y0[4 * k + 1], y0[4 * k + 2], y0[4 * k + 3]};
    *(LAS f32x4*)(ys + (32u + r) * 144u + (8 * k + 4u * hi) * 4u) = (f32x4){y1[4 * k], y1[4 * k + 1], y1[4 * k + 2], y1[4 * k + 3]};
  }
  __syncthreads();
  const unsigned dw = *(const unsigned*)(ssmd + c0);
  unsigned res[2][8];
#pragma unroll
  for (int c = 0; c < 2; ++c) {
    const LAS char* yp = (const LAS char*)lds + (2 * c) * 9216u + (tid >> 2) * 144u + (tid & 3u) * 32u;
    const f32x4 a0 = *(const LAS f32x4*)yp + *(const LAS f32x4*)(yp + 9216), a1 = *(const LAS f32x4*)(yp + 16) + *(const LAS f32x4*)(yp + 9216 + 16);
    const float dd = bf2f(c ? dw >> 16 : dw & 0xffffu);
#pragma unroll
    for (int i = 0; i < 8; ++i) {
      const float y = rbf(rbf(i < 4 ? a0[i & 3] : a1[i & 3]) + rbf(kv[c][i] * dd));
      res[c][i] = cvtpk(bf2f(c ? qw[i] >> 16 : qw[i] & 0xffffu) * y, 0.f) & 0xffffu;
    }
  }
#pragma unroll
  for (int i = 0; i < 8; ++i) *(unsigned*)(out + (size_t)(8u * tid + i) * OUTW + DM + c0) = res[0][i] | (res[1][i] << 16);
}

__global__ __launch_bounds__(256, 2) void k3_out(const char* __restrict__ Cf, const unsigned short* __restrict__ wo, const unsigned short* __restrict__ bq,
                                                 const unsigned short* __restrict__ bk, const unsigned short* __restrict__ bv, const unsigned short* __restrict__ shk,
                                                 const unsigned short* __restrict__ ssmk, const unsigned short* __restrict__ ssmd, unsigned short* __restrict__ out) {
  extern __shared__ __attribute__((aligned(16))) char lds[];
#define K3_ORDER 0
  const unsigned b = blockIdx.x;
  if (K3_ORDER == 0) { if (b < 224u) k3_outproj(lds, b, Cf, wo, out); else k3_ssm(lds, b - 224u, bq, bk, bv, shk, ssmk, ssmd, out); }
  else { if (b >= 224u) k3_outproj(lds, b - 224u, Cf, wo, out); else k3_ssm(lds, b, bq, bk, bv, shk, ssmk, ssmd, out); }
}

extern "C" void kernel_launch(void* const* d_in, const int* in_sizes, int n_in, void* d_out, int out_size, void* d_ws, size_t ws_size, hipStream_t stream) {
  typedef const unsigned short* cu16;
  char* ws = (char*)d_ws;
  constexpr size_t MB16 = (size_t)SEQ * NH * DH * 2;
  (void)hipFuncSetAttribute((const void*)k1_proj, hipFuncAttributeMaxDynamicSharedMemorySize, 131072);
  (void)hipFuncSetAttribute((const void*)k2_attn, hipFuncAttributeMaxDynamicSharedMemorySize, 163840);
  (void)hipFuncSetAttribute((const void*)k3_out, hipFuncAttributeMaxDynamicSharedMemorySize, 65536);
  k1_proj<<<256, 256, 131072, stream>>>((cu16)d_in[0], (cu16)d_in[1], (cu16)d_in[2], (cu16)d_in[3], (cu16)d_in[4], ws, ws + MB16, ws + 2 * MB16, (unsigned*)(ws + 8 * MB16));
  k2_attn<<<256, 256, 163840, stream>>>(ws, ws + MB16, ws + 2 * MB16, ws + 3 * MB16, ws + 4 * MB16, (unsigned*)(ws + 8 * MB16));
  k3_out<<<448, 256, 65536, stream>>>(ws + 3 * MB16, (cu16)d_in[5], (cu16)d_in[6], (cu16)d_in[7], (cu16)d_in[8], (cu16)d_in[9], (cu16)d_in[10], (cu16)d_in[11], (unsigned short*)d_out);
}
```

```cpp
#include <hip/hip_runtime.h>
#include <hip/hip_bf16.h>
#include <utility>

typedef short    bf16x8 __attribute__((ext_vector_type(8)));
typedef short    s16x4  __attribute__((ext_vector_type(4)));
typedef float    f32x16 __attribute__((ext_vector_type(16)));
typedef float    f32x4  __attribute__((ext_vector_type(4)));
typedef float    f32x2  __attribute__((ext_vector_type(2)));
typedef __bf16   bf16x2 __attribute__((ext_vector_type(2)));
typedef unsigned u32x4  __attribute__((ext_vector_type(4)));
typedef unsigned u32x2  __attribute__((ext_vector_type(2)));
#define DI __device__ __forceinline__
#define LAS __attribute__((address_space(3)))
#define SBAR() __builtin_amdgcn_sched_barrier(0)
#define PINV(x) asm volatile("" : "+v"(x))
#define PINA(x) asm volatile("" : "+a"(x))
#define MFMA32(a, b, c) __builtin_amdgcn_mfma_f32_32x32x16_bf16((a), (b), (c), 0, 0, 0)
#define MFMA_DRAIN() asm volatile("s_nop 7\n\ts_nop 7" ::: "memory")
typedef LAS const char* lds_cptr;
typedef __amdgpu_buffer_rsrc_t srd_t;
template <int... Is> using iseq = std::integer_sequence<int, Is...>;
template <int N> using mkseq = std::make_integer_sequence<int, N>;

DI unsigned rfl(unsigned x) { return __builtin_amdgcn_readfirstlane(x); }
DI unsigned cvtpk(float lo, float hi) { f32x2 v = {lo, hi}; bf16x2 b = __builtin_convertvector(v, bf16x2); return __builtin_bit_cast(unsigned, b); }
DI float bf2f(unsigned h) { return __uint_as_float(h << 16); }
DI float rbf(float x) { return __uint_as_float(cvtpk(x, x) << 16); }
DI bf16x8 lds_rd128(lds_cptr p) { return *(const LAS bf16x8*)p; }
DI s16x4 lds_tr(lds_cptr p) { return __builtin_amdgcn_ds_read_tr16_b64_v4i16((LAS s16x4*)p); }
DI bf16x8 cat(s16x4 l, s16x4 h) { return __builtin_shufflevector(l, h, 0, 1, 2, 3, 4, 5, 6, 7); }
DI srd_t mk_srd(const void* p, unsigned bytes) { return __builtin_amdgcn_make_buffer_rsrc((void*)p, (short)0, (int)bytes, 0x00020000); }
DI void dma16(unsigned m0v, unsigned voff, srd_t srd, unsigned soff) {
  asm volatile("s_mov_b32 m0, %0\n\ts_nop 0\n\tbuffer_load_dwordx4 %1, %2, %3 offen lds" :: "s"(m0v), "v"(voff), "s"(srd), "s"(soff) : "memory");
}
DI void st16(u32x4 v, srd_t srd, unsigned voff, unsigned soff) { asm volatile("buffer_store_dwordx4 %0, %1, %2, %3 offen\n\ts_nop 1" :: "v"(v), "v"(voff), "s"(srd), "s"(soff) : "memory"); }
DI void st16_sc1_a(f32x4 v, srd_t srd, unsigned voff, unsigned soff) { asm volatile("buffer_store_dwordx4 %0, %1, %2, %3 offen sc1\n\ts_nop 1" :: "a"(v), "v"(voff), "s"(srd), "s"(soff) : "memory"); }
DI void st16_sc1(f32x4 v, srd_t srd, unsigned voff, unsigned soff) { asm volatile("buffer_store_dwordx4 %0, %1, %2, %3 offen sc1\n\ts_nop 1" :: "v"(v), "v"(voff), "s"(srd), "s"(soff) : "memory"); }
DI u32x4 pack8(const f32x4& a, const f32x4& b, float sc) { u32x4 w = {cvtpk(a[0] * sc, a[1] * sc), cvtpk(a[2] * sc, a[3] * sc), cvtpk(b[0] * sc, b[1] * sc), cvtpk(b[2] * sc, b[3] * sc)}; return w; }
DI u32x4 pack8w(const f32x16& a, int s, float sc) {
  u32x4 w = {cvtpk(a[8 * s] * sc, a[8 * s + 1] * sc), cvtpk(a[8 * s + 2] * sc, a[8 * s + 3] * sc), cvtpk(a[8 * s + 4] * sc, a[8 * s + 5] * sc), cvtpk(a[8 * s + 6] * sc, a[8 * s + 7] * sc)};
  return w;
}
constexpr int SEQ = 2048, NH = 16, DH = 256, DM = 448, DC = 1024;
constexpr unsigned WRS = NH * DH * 2;
constexpr unsigned MB16 = SEQ * NH * DH * 2;
constexpr float QSCALE = 1.4426950408889634f / 16.0f;

DI void mfma16a(f32x4& c, const bf16x8& a, const bf16x8& b) { asm volatile("v_mfma_f32_16x16x32_bf16 %0, %1, %2, %0" : "+a"(c) : "v"(a), "v"(b)); }
DI void mfma16v(f32x4& c, const bf16x8& a, const bf16x8& b) { asm volatile("v_mfma_f32_16x16x32_bf16 %0, %1, %2, %0" : "+v"(c) : "v"(a), "v"(b)); }
DI void mfma16z(f32x4& c, const bf16x8& a, const bf16x8& b) { asm volatile("v_mfma_f32_16x16x32_bf16 %0, %1, %2, 0" : "=&v"(c) : "v"(a), "v"(b)); }
struct Frags { bf16x8 x[8]; s16x4 wl[8], wh[8]; };
struct K1C {
  unsigned ldsb, wid, vlane;
  unsigned vx_kv[2], vx_q[2], vw[4];
  srd_t sX, sW, sXq, sWq, sO;
  unsigned sx_kv, sw_kv, sx_q, sw_q, so;
  lds_cptr xr_kv[2], xr_q[2], wr[8];
  const char* pf; unsigned pfstep;
  f32x4 acc[8][8], qacc[4][8]; Frags f0, f1; f32x4 ta, tb; u32x4 pk;
  srd_t sBk, sBv, sBs, sWo; unsigned b, kw[11], vv[8]; u32x4 sk;
};
DI void k1_touch(const K1C& c) {
  asm volatile("s_mov_b32 m0, %0\n\ts_nop 0\n\tglobal_load_lds_dword %1, off" :: "s"(c.ldsb + 147456u), "v"(c.pf) : "memory"); }
template <int KIND, int j> DI void k1_piece(const K1C& c, int st, int buf) {
  const unsigned lb = c.ldsb + buf * 32768u;
  if constexpr (KIND == 0) {
    constexpr int p = j >> 1;
    if constexpr (!(j & 1)) dma16(lb + 65536u + (8u * c.wid + p) * 1024u, c.vw[(p & 1) + 2 * (p >> 2)], c.sW, c.sw_kv + (unsigned)(st * 64 + 2 * p) * WRS);
    else          dma16(lb + (8u * c.wid + p) * 1024u, c.vx_kv[(p >> 1) & 1], c.sX, c.sx_kv + (unsigned)(8 * p) * (DC * 2) + st * 128u);
  } else if constexpr (KIND == 1 && j < 12) {
    if constexpr (j % 3 != 1) { constexpr int p = 2 * (j / 3) + (j % 3 == 2); dma16(lb + 65536u + (8u * c.wid + p) * 1024u, c.vw[(p & 1) + 2 * (p >> 2)], c.sWq, c.sw_q + (unsigned)(st * 64 + 2 * p) * WRS); }
    else { constexpr int p = j / 3; dma16(lb + (4u * c.wid + p) * 1024u, c.vx_q[(p >> 1) & 1], c.sXq, c.sx_q + (unsigned)(8 * p) * (DM * 2) + st * 128u); }
  }
}
template <int KIND, int... Js> DI void k1_stage(const K1C& c, int st, int buf, iseq<Js...>) { (k1_piece<KIND, Js>(c, st, buf), ...); }
template <int NX, int buf, int ks, int i> DI void k1_read(const K1C& c, Frags& f) {
  constexpr int so = buf * 32768;
  if constexpr (i < 2 || i >= 2 + NX) { constexpr int k = i < 2 ? i : i - NX, nb = k >> 1, t = k & 1;
    const s16x4 v = lds_tr(c.wr[nb] + so + ks * 16384 + t * 2048); if constexpr (t) f.wh[nb] = v; else f.wl[nb] = v; }
  else { constexpr int mb = i - 2; f.x[mb] = lds_rd128((NX == 8 ? c.xr_kv[ks] : c.xr_q[ks]) + so + (mb >> 1) * 4096 + (mb & 1) * 1024); }
}
template <int... Is> DI void k1_read0(K1C& c, iseq<Is...>) { (k1_read<8, 0, 0, Is>(c, c.f0), ...); }
template <bool WA, int NXN, int KB, int buf, bool PF, int G> DI void k1_gap(K1C& c, int sb) {
  constexpr int ks = G >> 6, g = G & 63, nb = g >> 3, mb = g & 7;
  Frags& fc = ks ? c.f1 : c.f0; Frags& fn = ks ? c.f0 : c.f1;
  if constexpr (G == 64) { asm volatile("s_waitcnt vmcnt(1) lgkmcnt(0)\n\ts_barrier" ::: "memory"); SBAR(); }
  const bf16x8 w = cat(fc.wl[nb], fc.wh[nb]);
  if constexpr (G == 0) asm volatile("s_nop 1" :: "v"(w), "v"(fc.x[0]));
  if constexpr (WA) mfma16a(c.acc[mb][nb], w, fc.x[mb]); else mfma16a(c.acc[nb][mb], fc.x[mb], w);
  if constexpr (!(g & 1)) {
    if constexpr (ks == 0) { if constexpr (g / 2 < 24) k1_read<8, buf, 1, g / 2>(c, fn); }
    else if constexpr (g / 2 < 16 + NXN) k1_read<NXN, buf ^ 1, 0, g / 2>(c, fn);
  }
  if constexpr (ks == 1 && (g & 3) == 1) k1_piece<KB, (g >> 2)>(c, sb, buf);
  if constexpr (G == 127) {
    k1_touch(c);
    if constexpr (PF) c.pf += c.pfstep;
  }
  SBAR();
}
template <bool WA, int NXN, int KB, int buf, bool PF, int... Gs> DI void k1_step_(K1C& c, int sb, iseq<Gs...>) { (k1_gap<WA, NXN, KB, buf, PF, Gs>(c, sb), ...); }
template <bool WA, int NXN, int KB, int buf, bool PF> DI void k1_step(K1C& c, int sb) { k1_step_<WA, NXN, KB, buf, PF>(c, sb, mkseq<128>{}); }
template <bool WA, int T, int G> DI void k1_qgap(K1C& c) {
  constexpr int buf = T & 1, ks = G >> 5, g = G & 31, nb = g >> 2, mb = g & 3;
  Frags& fc = ks ? c.f1 : c.f0; Frags& fn = ks ? c.f0 : c.f1;
  if constexpr (G == 32 && T < 6) {
    asm volatile("s_waitcnt vmcnt(3) lgkmcnt(0)\n\ts_barrier" ::: "memory"); SBAR(); }
  const bf16x8 w = cat(fc.wl[nb], fc.wh[nb]);
  if constexpr (T == 0 && ks == 0) mfma16z(c.qacc[mb][nb], w, fc.x[mb]); else mfma16v(c.qacc[mb][nb], w, fc.x[mb]);
  SBAR();
  if constexpr (g < 25 && g % 5 != 4) { constexpr int i = g - g / 5;
    if constexpr (ks == 0) k1_read<4, buf, 1, i>(c, fn); else if constexpr (T < 6) k1_read<4, buf ^ 1, 0, i>(c, fn); }
  if constexpr (ks == 1 && T + 2 < 7 && (g & 1) == 0 && g < 24) k1_piece<1, (g >> 1)>(c, T + 2, buf);
  if constexpr (T == 6 && G >= 26 && G < 30) {
    constexpr int j = G - 26; const unsigned ln = c.vlane >> 4;
    dma16(c.ldsb + 131072u + (4u * c.wid + j) * 1024u, (ln >> 2) * (DM * 2) + (ln & 3u) * 16u, c.sWo, c.b * (16u * DM * 2) + (4u * j + c.wid) * 64u);
  }
  if constexpr (T == 6 && G >= 36 && G < 56) {
    constexpr int i = G - 36; const unsigned tid = threadIdx.x, bc0 = 64u * (c.b % 7u) + 2u * (tid & 31u), vo = ((64u * (c.b / 7u) + 8u * (tid >> 5)) * DM + bc0) * 2u;
    if constexpr (i < 11) asm volatile("buffer_load_dword %0, %1, %2, 0 offen" : "=v"(c.kw[i]) : "v"(vo + (unsigned)((i - 3) * DM * 2)), "s"(c.sBk) : "memory");
    else if constexpr (i < 19) asm volatile("buffer_load_dword %0, %1, %2, 0 offen" : "=v"(c.vv[i - 11]) : "v"(vo + (unsigned)((i - 11) * DM * 2)), "s"(c.sBv) : "memory");
    else asm volatile("buffer_load_dwordx4 %0, %1, %2, 0 offen" : "=v"(c.sk) : "v"(bc0 * 8u), "s"(c.sBs) : "memory");
  }
  constexpr int u = 5 * T + G / 12, ph = G % 12;
  if constexpr (G < 60 && u < 32) {
    constexpr int i = u >> 2, j = u & 3;
    if constexpr (ph == 2) { c.ta = c.acc[i][2 * j]; PINV(c.ta); }
    if constexpr (ph == 4) { c.tb = c.acc[i][2 * j + 1]; PINV(c.tb); }
    if constexpr (ph == 6) { c.pk[0] = cvtpk(c.ta[0], c.ta[1]); c.pk[1] = cvtpk(c.ta[2], c.ta[3]); PINV(c.pk); }
    if constexpr (ph == 8) { c.pk[2] = cvtpk(c.tb[0], c.tb[1]); c.pk[3] = cvtpk(c.tb[2], c.tb[3]); PINV(c.pk); }
    if constexpr (ph == 11) st16(c.pk, c.sO, c.vlane, c.so + (WA ? (i >> 1) * 16384u + j * 2048u : (i >> 1) * 2048u + j * 16384u) + (i & 1) * 1024u);
  }
  SBAR();
}
template <bool WA, int T, int... Gs> DI void k1_qstep_(K1C& c, iseq<Gs...>) { (k1_qgap<WA, T, Gs>(c), ...); }
template <bool WA, int... Ts> DI void k1_qloop(K1C& c, iseq<Ts...>) { (k1_qstep_<WA, Ts>(c, mkseq<64>{}), ...); }

DI f32x2 up2(unsigned w) { f32x2 r = {__uint_as_float(w << 16), __uint_as_float(w & 0xffff0000u)}; return r; }
DI f32x2 rb2(f32x2 v) { return up2(cvtpk(v[0], v[1])); }
template <bool WA> DI void k1_main(K1C& c, char* lds, unsigned b, unsigned lane, char* Wf, char* Qdst, const char* pf2, unsigned pfstep2, unsigned short* xT
) {
#pragma unroll
  for (int i = 0; i < 8; ++i)
#pragma unroll
    for (int j = 0; j < 8; ++j) { c.acc[i][j] = f32x4{}; PINA(c.acc[i][j]); }
  SBAR();
  k1_stage<0>(c, 1, 1, mkseq<16>{});
  k1_touch(c); c.pf += c.pfstep;
  asm volatile("s_waitcnt vmcnt(17)\n\ts_barrier" ::: "memory");
  k1_read0(c, mkseq<24>{});
  SBAR();
  for (int t = 0; t < 10; t += 2) {
    k1_step<WA, 8, 0, 0, true>(c, t + 2);
    k1_step<WA, 8, 0, 1, true>(c, t + 3);
  }
  c.pf = pf2; c.pfstep = pfstep2;
  for (int t = 10; t < 14; t += 2) {
    k1_step<WA, 8, 0, 0, true>(c, t + 2);
    k1_step<WA, 8, 0, 1, true>(c, t + 3);
  }
  k1_step<WA, 8, 1, 0, false>(c, 0);
  k1_step<WA, 4, 1, 1, false>(c, 1);
  k1_qloop<WA>(c, mkseq<7>{});
  MFMA_DRAIN(); SBAR();
  const srd_t sq = mk_srd(Qdst, 32768u);
#pragma unroll
  for (int mb = 0; mb < 4; ++mb)
#pragma unroll
    for (int db = 0; db < 4; ++db) { PINV(c.qacc[mb][2 * db]); PINV(c.qacc[mb][2 * db + 1]);
      st16(pack8(c.qacc[mb][2 * db], c.qacc[mb][2 * db + 1], QSCALE), sq, c.vlane, (mb >> 1) * 16384u + db * 2048u + (mb & 1) * 1024u); }
  asm volatile("s_waitcnt vmcnt(16)" : "+v"(c.kw[0]), "+v"(c.kw[1]), "+v"(c.kw[2]), "+v"(c.kw[3]), "+v"(c.kw[4]), "+v"(c.kw[5]), "+v"(c.kw[6]), "+v"(c.kw[7]), "+v"(c.kw[8]), "+v"(c.kw[9]), "+v"(c.kw[10]),
               "+v"(c.vv[0]), "+v"(c.vv[1]), "+v"(c.vv[2]), "+v"(c.vv[3]), "+v"(c.vv[4]), "+v"(c.vv[5]), "+v"(c.vv[6]), "+v"(c.vv[7]), "+v"(c.sk) :: "memory");
  {
    const unsigned q = (lane & 15u) >> 2, p = lane & 3u;
    const lds_cptr rp = (lds_cptr)lds + 131072u + c.wid * 4096u + (4u * (lane >> 5) + q) * 64u + 32u * ((lane >> 4) & 1u) + 16u * (p & 1u) + 8u * (p >> 1);
    const srd_t sw_ = mk_srd(Wf + (size_t)(b >> 4) * (DH * DM * 2), DH * DM * 2);
#pragma unroll
    for (int j = 0; j < 4; ++j) {
      const bf16x8 w = cat(lds_tr(rp + j * 1024), lds_tr(rp + j * 1024 + 512));
      st16(__builtin_bit_cast(u32x4, w), sw_, c.vlane, ((4u * j + c.wid) * 16u + (b & 15u)) * 1024u);
    }
  }
  {
    const srd_t sx_ = mk_srd(xT, SEQ * DM * 2);
    const unsigned tid = threadIdx.x, bc0 = 64u * (b % 7u) + 2u * (tid & 31u);
    const f32x2 s0 = {bf2f(c.sk[0] & 0xffffu), bf2f(c.sk[2] & 0xffffu)}, s1 = {bf2f(c.sk[0] >> 16), bf2f(c.sk[2] >> 16)};
    const f32x2 s2 = {bf2f(c.sk[1] & 0xffffu), bf2f(c.sk[3] & 0xffffu)}, s3 = {bf2f(c.sk[1] >> 16), bf2f(c.sk[3] >> 16)};
    f32x2 kf[11]; unsigned kv[8];
#pragma unroll
    for (int i = 0; i < 11; ++i) kf[i] = up2(c.kw[i]);
#pragma unroll
    for (int i = 0; i < 8; ++i) {
      f32x2 a = rb2(kf[i] * s3); a = rb2(a + rb2(kf[i + 1] * s2)); a = rb2(a + rb2(kf[i + 2] * s1)); a = rb2(a + rb2(kf[i + 3] * s0));
      a = a * up2(c.vv[i]); kv[i] = cvtpk(a[0], a[1]);
    }
    const unsigned xo = (((b / 7u) * DM + bc0) * 64u + 8u * (tid >> 5)) * 2u;
    st16((u32x4){__builtin_amdgcn_perm(kv[6], kv[7], 0x05040100u), __builtin_amdgcn_perm(kv[4], kv[5], 0x05040100u), __builtin_amdgcn_perm(kv[2], kv[3], 0x05040100u), __builtin_amdgcn_perm(kv[0], kv[1], 0x05040100u)}, sx_, xo, 0u);
    st16((u32x4){__builtin_amdgcn_perm(kv[6], kv[7], 0x07060302u), __builtin_amdgcn_perm(kv[4], kv[5], 0x07060302u), __builtin_amdgcn_perm(kv[2], kv[3], 0x07060302u), __builtin_amdgcn_perm(kv[0], kv[1], 0x07060302u)}, sx_, xo + 128u, 0u);
  }
}

__global__ __launch_bounds__(256, 1) void k1_proj(const unsigned short* __restrict__ x, const unsigned short* __restrict__ ckv, const unsigned short* __restrict__ wq,
                                                  const unsigned short* __restrict__ wuk, const unsigned short* __restrict__ wuv,
                                                  char* __restrict__ Qf, char* __restrict__ Kf, char* __restrict__ Vf, unsigned* __restrict__ flags,
                                                  const unsigned short* __restrict__ wo, char* __restrict__ Wf,
                                                  const unsigned short* __restrict__ bk, const unsigned short* __restrict__ bv, const unsigned short* __restrict__ shk,
                                                  unsigned short* __restrict__ xT) {
  extern __shared__ __attribute__((aligned(16))) char lds[];
  const unsigned tid = threadIdx.x, lane = tid & 63u, wid = rfl(tid >> 6), wr = wid >> 1, wc = wid & 1;
  const unsigned b = blockIdx.x, xcd = b & 7u, idx = b >> 3;
  const unsigned pm = idx & 7u, which = idx >> 3, h = 2u * xcd + (which & 1u), isV = which >> 1;
  const unsigned qm = idx & 15u, hq = 2u * xcd + (idx >> 4);
  K1C c;
  c.ldsb = (unsigned)(size_t)lds; c.wid = wid; c.vlane = lane * 16u;
  const unsigned short* wkv = isV ? wuv : wuk;
  c.sX = mk_srd(ckv, SEQ * DC * 2); c.sW = mk_srd(wkv, DC * WRS); c.sXq = mk_srd(x, SEQ * DM * 2); c.sWq = mk_srd(wq, DM * WRS);
#pragma unroll
  for (int par = 0; par < 2; ++par) {
    const unsigned ch = 16u * ((lane & 7u) ^ (4u * par + (lane >> 4)));
    c.vx_kv[par] = (lane >> 3) * (DC * 2) + ch; c.vx_q[par] = (lane >> 3) * (DM * 2) + ch;
  }
#pragma unroll
  for (int v = 0; v < 4; ++v) {
    const unsigned f = (2u * (v & 1) + (lane >> 5)) | (4u * (v >> 1));
    c.vw[v] = (lane >> 5) * WRS + 16u * ((lane & 31u) ^ (f << 1));
  }
  c.sx_kv = (256u * pm + 64u * wid) * (DC * 2); c.sw_kv = 16u * wid * WRS + h * (DH * 2);
  c.sx_q = (128u * qm + 32u * wid) * (DM * 2);  c.sw_q = 16u * wid * WRS + hq * (DH * 2);
  k1_stage<0>(c, 0, 0, mkseq<16>{});
  if (b == 0) flags[tid] = 0u;
  const unsigned rho = lane & 15u, g = lane >> 4;
#pragma unroll
  for (int ks = 0; ks < 2; ++ks) {
    const unsigned row = 16u * (rho >> 3) + (rho & 7u), ch = 16u * ((4u * ks + g) ^ (rho >> 1));
    c.xr_kv[ks] = (lds_cptr)lds + (wr * 128u + row) * 128u + ch; c.xr_q[ks] = (lds_cptr)lds + (wr * 64u + row) * 128u + ch;
    PINV(c.xr_kv[ks]); PINV(c.xr_q[ks]);
  }
  { const unsigned q = (lane & 15u) >> 2, p = lane & 3u, f = q | ((g & 1u) << 2);
#pragma unroll
    for (int nb = 0; nb < 8; ++nb) { c.wr[nb] = (lds_cptr)lds + 65536u + (8u * g + q) * 512u + (8u * wc + ((unsigned)nb ^ f)) * 32u + 8u * p; PINV(c.wr[nb]); } }
  c.sO = mk_srd((isV ? Vf : Kf) + (size_t)h * (SEQ * DH * 2), SEQ * DH * 2);
  c.so = (8u * pm + 4u * wr) * 16384u + wc * 8192u;
  constexpr unsigned PFD = 5;
  const bool pw = lane >= 32u;
  c.pf = pw ? (const char*)wkv + (size_t)(PFD * 64u + 8u * pm + 2u * wid + ((lane >> 2) & 1u)) * WRS + h * (DH * 2) + (lane & 3u) * 128u
            : (const char*)ckv + (size_t)(256u * pm + 64u * which + 16u * wid + (lane & 15u)) * (DC * 2) + PFD * 128u;
  c.pfstep = pw ? 64u * WRS : 128u;
  const char* pf2 = pw ? (const char*)wq + (size_t)(128u + 4u * qm + wid) * WRS + hq * (DH * 2) + (lane & 3u) * 128u
                       : (const char*)x + (size_t)(128u * qm + 64u * (idx >> 4) + 16u * wid + (lane & 15u)) * (DM * 2) + 256u;
  c.sBk = mk_srd(bk, SEQ * DM * 2); c.sBv = mk_srd(bv, SEQ * DM * 2); c.sBs = mk_srd(shk, DM * 8); c.sWo = mk_srd(wo, NH * DH * DM * 2); c.b = b;
  char* Qdst = Qf + (size_t)hq * (SEQ * DH * 2) + (4u * qm + 2u * wr) * 16384u + wc * 8192u;
  if (isV) k1_main<false>(c, lds, b, lane, Wf, Qdst, pf2, c.pfstep, xT); else k1_main<true>(c, lds, b, lane, Wf, Qdst, pf2, c.pfstep, xT);
}

constexpr float THR = 8.0f;
constexpr int NA = 17;
DI void mfma32_acc_a(f32x16& c, const bf16x8& a, const u32x4& b) { asm volatile("v_mfma_f32_32x32x16_bf16 %0, %1, %2, %0" : "+a"(c) : "v"(a), "v"(b)); }
DI void mfma32_acc_v(f32x16& c, const bf16x8& a, const bf16x8& b) { asm volatile("v_mfma_f32_32x32x16_bf16 %0, %1, %2, %0" : "+v"(c) : "v"(a), "a"(b)); }
DI void mfma32_init_v(f32x16& d, const bf16x8& a, const bf16x8& b, const f32x16& c) { asm volatile("v_mfma_f32_32x32x16_bf16 %0, %1, %2, %3" : "=&v"(d) : "v"(a), "a"(b), "v"(c)); }
DI float max3f(float a, float b, float c) { float r; asm("v_max3_f32 %0, %1, %2, %3" : "=v"(r) : "v"(a), "v"(b), "v"(c)); return r; }
DI float max2f(float a, float b) { float r; asm("v_max_f32_e32 %0, %1, %2" : "=v"(r) : "v"(a), "v"(b)); return r; }
DI float fadd_s(float a, float b) { float r; asm volatile("v_add_f32_e32 %0, %1, %2" : "=v"(r) : "v"(a), "v"(b)); return r; }
DI float fsub_s(float a, float b) { float r; asm("v_sub_f32_e32 %0, %1, %2" : "=v"(r) : "v"(a), "v"(b)); return r; }
constexpr int koff(int f) { return (f >> 1) * 2048 + (f & 1) * 512; }
constexpr int voff(int f) { return (f >> 4) * 16384 + (f & 7) * 2048 + ((f >> 3) & 1) * 512; }
struct K2C {
  unsigned ldsb, vlane;
  srd_t sK;
  lds_cptr kr, vr;
  unsigned kslot_w, vslot_w, vslot_r;
  unsigned ksoff, vsoff;
  bf16x8 q[16]; f32x16 o[8]; f32x16 s0, s1, negm; u32x4 p[4]; bf16x8 fr[16];
  float m, l0, l1, mx0, mx1, fct, e14, e15; bool pend;
  int dq;
  bool domask;
};
template <int J> DI void k2_dma(unsigned m0v, unsigned voff, srd_t srd, unsigned soff) {
  if constexpr ((J & 3) == 0) asm volatile("s_mov_b32 m0, %0\n\ts_nop 0\n\tbuffer_load_dwordx4 %1, %2, %3 offen lds" :: "s"(m0v + (J >> 2) * 4096u), "v"(voff), "s"(srd), "s"(soff + (J >> 2) * 4096u) : "memory");
  else asm volatile("buffer_load_dwordx4 %0, %1, %2 offen offset:%3 lds" :: "v"(voff), "s"(srd), "s"(soff + (J >> 2) * 4096u), "i"((J & 3) * 1024) : "memory");
}
DI void k2_dma8(unsigned m0v, unsigned voff, srd_t srd, unsigned soff) {
  k2_dma<0>(m0v, voff, srd, soff); k2_dma<1>(m0v, voff, srd, soff); k2_dma<2>(m0v, voff, srd, soff); k2_dma<3>(m0v, voff, srd, soff);
  k2_dma<4>(m0v, voff, srd, soff); k2_dma<5>(m0v, voff, srd, soff); k2_dma<6>(m0v, voff, srd, soff); k2_dma<7>(m0v, voff, srd, soff);
}
DI void k2_mask(f32x16& s, int dq) {
#pragma unroll
  for (int r = 0; r < 16; ++r) if ((r & 3) + 8 * (r >> 2) > dq) s[r] = -__builtin_inff();
}
DI void mfma32_acc_a4(f32x16& c, const bf16x8& a, const u32x4& b, const bf16x8& x, const bf16x8& y, const bf16x8& z) { asm volatile("v_mfma_f32_32x32x16_bf16 %0, %1, %2, %0" : "+a"(c) : "v"(a), "v"(b), "v"(x), "v"(y), "v"(z)); }
DI void mfma32_acc_v4(f32x16& c, const bf16x8& a, const bf16x8& b, const bf16x8& x, const bf16x8& y, const bf16x8& z) { asm volatile("v_mfma_f32_32x32x16_bf16 %0, %1, %2, %0" : "+v"(c) : "v"(a), "a"(b), "v"(x), "v"(y), "v"(z)); }
DI void mfma32_init_v4(f32x16& d, const bf16x8& a, const bf16x8& b, const f32x16& c, const bf16x8& x, const bf16x8& y, const bf16x8& z) { asm volatile("v_mfma_f32_32x32x16_bf16 %0, %1, %2, %3" : "=&v"(d) : "v"(a), "a"(b), "v"(c), "v"(x), "v"(y), "v"(z)); }
DI void max4(float& m, float a, float b, float c, float d) { asm volatile("v_max3_f32 %0, %0, %1, %2\n\tv_max3_f32 %0, %0, %3, %4" : "+v"(m) : "v"(a), "v"(b), "v"(c), "v"(d)); }
DI void max6(float& m, float a, float b, float c, float d, float e, float f) { asm volatile("v_max3_f32 %0, %0, %1, %2\n\tv_max3_f32 %0, %0, %3, %4\n\tv_max3_f32 %0, %0, %5, %6" : "+v"(m) : "v"(a), "v"(b), "v"(c), "v"(d), "v"(e), "v"(f)); }
DI float max4i(float a, float b, float c, float d) { float m; asm volatile("v_max_f32_e32 %0, %1, %2\n\tv_max3_f32 %0, %0, %3, %4" : "=&v"(m) : "v"(a), "v"(b), "v"(c), "v"(d)); return m; }
template <int e> DI void k2_lsum(K2C& c, float v) { if constexpr (e & 1) c.l1 = fadd_s(c.l1, v); else c.l0 = fadd_s(c.l0, v); }
template <bool FIN, bool QK, int g> DI void k2_gapA(K2C& c) {
  if constexpr (QK) {
    if constexpr (g == 0) mfma32_init_v4(c.s0, c.fr[0], c.q[0], c.negm, c.fr[1], c.fr[2], c.fr[3]);
    else if constexpr (g == 16) mfma32_init_v4(c.s1, c.fr[0], c.q[0], c.negm, c.fr[1], c.fr[2], c.fr[3]);
    else if constexpr ((g & 3) == 0) mfma32_acc_v4(g < 16 ? c.s0 : c.s1, c.fr[g & 15], c.q[g & 15], c.fr[(g + 1) & 15], c.fr[(g + 2) & 15], c.fr[(g + 3) & 15]);
    else mfma32_acc_v(g < 16 ? c.s0 : c.s1, c.fr[g & 15], c.q[g & 15]);
    SBAR();
    if constexpr (g < 24) c.fr[(g + 8) & 15] = lds_rd128(c.kr + koff((g + 8)));
  }
  if constexpr (!QK && g <= 9) { asm volatile("s_nop 0"); SBAR(); }
  if constexpr (FIN && (QK ? g >= 24 : g < 8)) c.fr[g & 7] = lds_rd128(c.vr + voff((g & 7)));
  if constexpr (FIN && g <= 8) {
    if constexpr (g < 8) c.s1[8 + g] = __builtin_amdgcn_exp2f(c.s1[8 + g]);
    k2_lsum<g>(c, c.s1[7 + g]);
    if constexpr (g < 4) c.p[2][g] = cvtpk(c.s1[2 * g], c.s1[2 * g + 1]);
    else if constexpr (g < 7) c.p[3][g - 4] = cvtpk(c.s1[2 * g], c.s1[2 * g + 1]);
    else if constexpr (g == 8) c.p[3][3] = cvtpk(c.s1[14], c.s1[15]);
    if constexpr (g < 8) PINV(c.s1);
    if constexpr (g < 4) PINV(c.p[2]); else PINV(c.p[3]);
  }
  if constexpr (QK) {
    if constexpr (g >= 10 && g < 18) k2_dma<g - 10>(c.ldsb + 65536u + c.vslot_w, c.vlane, c.sK, c.vsoff);
    if constexpr (g == 18) { if (c.domask) { SBAR(); k2_mask(c.s0, c.dq); } }
    if constexpr (g == 20) { c.mx0 = max4i(c.s0[0], c.s0[1], c.s0[2], c.s0[3]); }
    if constexpr (g >= 21 && g < 24) { constexpr int r = 4 * (g - 20); max4(c.mx0, c.s0[r], c.s0[r + 1], c.s0[r + 2], c.s0[r + 3]); }
  }
  SBAR();
}
template <bool FIN, bool QK, int... Gs> DI void k2_phaseA_(K2C& c, iseq<Gs...>) { (k2_gapA<FIN, QK, Gs>(c), ...); }
template <bool FIN, bool QK> DI void k2_phaseA(K2C& c) {
  if constexpr (QK && !FIN) {
#pragma unroll
    for (int i = 0; i < 8; ++i) c.fr[i] = lds_rd128(c.kr + koff(i));
  }
  k2_phaseA_<FIN, QK>(c, std::make_integer_sequence<int, 32>{});
}
template <bool PV, bool SM, int g> DI void k2_gapB(K2C& c) {
  if constexpr (SM && g == 24) {
    asm volatile("s_waitcnt vmcnt(8) lgkmcnt(0)\n\ts_barrier" ::: "memory");
    c.kr = (lds_cptr)(size_t)((unsigned)(size_t)c.kr ^ 32768u);
    SBAR();
  }
  if constexpr (PV) {
    if constexpr ((g & 3) == 0) mfma32_acc_a4(c.o[g & 7], c.fr[g & 15], c.p[g >> 3], c.fr[(g + 1) & 15], c.fr[(g + 2) & 15], c.fr[(g + 3) & 15]);
    else mfma32_acc_a(c.o[g & 7], c.fr[g & 15], c.p[g >> 3]);
    SBAR();
    if constexpr (g < 2) { c.fr[(8 + 2 * g) & 15] = lds_rd128(c.vr + voff((8 + 2 * g))); c.fr[(9 + 2 * g) & 15] = lds_rd128(c.vr + voff((9 + 2 * g))); }
    else if constexpr (g < 8) c.fr[(g + 10) & 15] = lds_rd128(c.vr + voff((g + 10)));
    else if constexpr (g < 10) { c.fr[(2 * g + 2) & 15] = lds_rd128(c.vr + voff((2 * g + 2))); c.fr[(2 * g + 3) & 15] = lds_rd128(c.vr + voff((2 * g + 3))); }
    else if constexpr (g < 20) c.fr[(g + 12) & 15] = lds_rd128(c.vr + voff((g + 12)));
  }
  if constexpr (!PV && g >= 7) { asm volatile("s_nop 0"); SBAR(); }
  if constexpr (SM) {
    if constexpr (g >= 24) { c.fr[(g + 8) & 15] = lds_rd128(c.kr + koff((g - 24))); k2_dma<g - 24>(c.ldsb + c.kslot_w, c.vlane, c.sK, c.ksoff); }
    if constexpr (g == 2) { if (c.domask) { SBAR(); k2_mask(c.s1, c.dq - 32); } }
    if constexpr (g == 3) { max6(c.mx0, c.s1[0], c.s1[1], c.s1[2], c.s1[3], c.s1[4], c.s1[5]); }
    if constexpr (g == 4) { max6(c.mx0, c.s1[6], c.s1[7], c.s1[8], c.s1[9], c.s1[10], c.s1[11]); }
    if constexpr (g == 5) { max4(c.mx0, c.s1[12], c.s1[13], c.s1[14], c.s1[15]); c.mx1 = c.mx0; PINV(c.mx1); }
    if constexpr (g == 6) {
      if constexpr (!PV) asm volatile("s_nop 1");
      auto pr = __builtin_amdgcn_permlane32_swap(__float_as_uint(c.mx0), __float_as_uint(c.mx1), false, false);
      const float rm = max2f(__uint_as_float(pr[0]), __uint_as_float(pr[1]));
      if constexpr (!PV) {
        const float dl = max2f(rm, -1e30f); c.m = dl;
#pragma unroll
        for (int r = 0; r < 16; ++r) { c.s0[r] = fsub_s(c.s0[r], dl); c.s1[r] = fsub_s(c.s1[r], dl); c.negm[r] = -dl; }
        PINV(c.negm);
      } else if (__builtin_expect(__any(rm > THR), 0)) {
        const float dl = max2f(rm, 0.f); c.m = fadd_s(c.m, dl); c.fct = __builtin_amdgcn_exp2f(-dl);
#pragma unroll
        for (int r = 0; r < 16; ++r) { c.s0[r] = fsub_s(c.s0[r], dl); c.s1[r] = fsub_s(c.s1[r], dl); c.negm[r] = -c.m; }
        c.l0 *= c.fct; c.l1 *= c.fct; c.pend = true;
      }
    }
    if constexpr (g >= 7 && g < 23) c.s0[g - 7] = __builtin_amdgcn_exp2f(c.s0[g - 7]);
    if constexpr (g >= 8 && g < 24) k2_lsum<g>(c, c.s0[g - 8]);
    if constexpr (g >= 24) c.s1[g - 24] = __builtin_amdgcn_exp2f(c.s1[g - 24]);
    if constexpr (g >= 25) k2_lsum<g>(c, c.s1[g - 25]);
    if constexpr (g >= 16 && g < 24) { constexpr int w = g - 16; c.p[w >> 2][w & 3] = cvtpk(c.s0[2 * w], c.s0[2 * w + 1]); PINV(c.p[w >> 2]); }
    if constexpr (g >= 7 && g < 23) PINV(c.s0);
    if constexpr (g >= 24) PINV(c.s1);
  }
  SBAR();
}
template <bool PV, bool SM, int... Gs> DI void k2_phaseB_(K2C& c, iseq<Gs...>) { (k2_gapB<PV, SM, Gs>(c), ...); }
template <bool PV, bool SM> DI void k2_phaseB(K2C& c) {
  if constexpr (!PV) { MFMA_DRAIN(); SBAR(); }
  k2_phaseB_<PV, SM>(c, std::make_integer_sequence<int, 32>{});
  if constexpr (PV && SM) {
    if (__builtin_expect(c.pend, 0)) {
      MFMA_DRAIN(); SBAR();
#pragma unroll
      for (int d = 0; d < 8; ++d) { asm volatile("" : "+a"(c.o[d])); c.o[d] *= c.fct; asm volatile("" : "+a"(c.o[d])); SBAR(); }
      c.pend = false;
    }
    SBAR();
  }
}

DI void mfma32_z4(f32x16& d, const bf16x8& a, const u32x4& b, const bf16x8& x, const bf16x8& y, const bf16x8& z) { asm volatile("v_mfma_f32_32x32x16_bf16 %0, %1, %2, 0" : "=&v"(d) : "v"(a), "v"(b), "v"(x), "v"(y), "v"(z)); }
DI void mfma32_vv4(f32x16& c, const bf16x8& a, const u32x4& b, const bf16x8& x, const bf16x8& y, const bf16x8& z) { asm volatile("v_mfma_f32_32x32x16_bf16 %0, %1, %2, %0" : "+v"(c) : "v"(a), "v"(b), "v"(x), "v"(y), "v"(z)); }
DI void mfma32_vv(f32x16& c, const bf16x8& a, const u32x4& b) { asm volatile("v_mfma_f32_32x32x16_bf16 %0, %1, %2, %0" : "+v"(c) : "v"(a), "v"(b)); }
struct K2E { u32x4 cx[16]; lds_cptr rc, rn; unsigned slotw, wsoff; srd_t sP; u32x4 pk; };
template <int CH, int g> DI void k2_gapC(K2C& c, K2E& e) {
  if constexpr (CH == 3 && g == 24) { asm volatile("s_waitcnt vmcnt(6)\n\ts_barrier" ::: "memory"); SBAR(); }
  if constexpr ((g & 15) == 0) mfma32_z4(g < 16 ? c.s0 : c.s1, c.fr[0], e.cx[0], c.fr[1], c.fr[2], c.fr[3]);
  else if constexpr ((g & 3) == 0) mfma32_vv4(g < 16 ? c.s0 : c.s1, c.fr[g & 15], e.cx[g & 15], c.fr[(g + 1) & 15], c.fr[(g + 2) & 15], c.fr[(g + 3) & 15]);
  else mfma32_vv(g < 16 ? c.s0 : c.s1, c.fr[g & 15], e.cx[g & 15]);
  SBAR();
  if constexpr (CH < 2 && g == 28) { asm volatile("s_barrier" ::: "memory"); SBAR(); }
  if constexpr (g < 24) c.fr[(g + 8) & 15] = lds_rd128(e.rc + (g + 8) * 1024);
  else if constexpr (CH < 6) c.fr[(g + 8) & 15] = lds_rd128(e.rn + (g - 24) * 1024);
  if constexpr (CH < 3 && g >= 8 && g < 16) k2_dma<g - 8>(c.ldsb + e.slotw, c.vlane, c.sK, e.wsoff);
  constexpr int u = (g & 15) - 2;
  if constexpr (u >= 0 && u < 6 && (g >= 16 || CH > 0)) {
    const f32x16& t = g < 16 ? c.s1 : c.s0; constexpr int n = 2 * CH + (g < 16 ? -1 : 0), a = u / 3, i = u % 3;
    if constexpr (i < 2) { e.pk[2 * i] = cvtpk(t[8 * a + 4 * i], t[8 * a + 4 * i + 1]); e.pk[2 * i + 1] = cvtpk(t[8 * a + 4 * i + 2], t[8 * a + 4 * i + 3]); PINV(e.pk); }
    else st16(e.pk, e.sP, c.vlane, (2 * n + a) * 1024);
  }
  SBAR();
}
template <int CH, int... Gs> DI void k2_phaseC_(K2C& c, K2E& e, iseq<Gs...>) { (k2_gapC<CH, Gs>(c, e), ...); }
template <int CH> DI void k2_phaseC(K2C& c, K2E& e) { k2_phaseC_<CH>(c, e, std::make_integer_sequence<int, 32>{}); }

__global__ __launch_bounds__(256, 1) void k2_attn(const char* __restrict__ Qf, const char* __restrict__ KVW, char* __restrict__ Pf,
                                                  char* __restrict__ part, unsigned* __restrict__ flags) {
  extern __shared__ __attribute__((aligned(16))) char lds[];
  const unsigned tid = threadIdx.x, lane = tid & 63u, wid = rfl(tid >> 6), hi = lane >> 5, r32 = lane & 31u;
  const unsigned b = blockIdx.x, xcd = b & 7u, idx = b >> 3, h = 2u * xcd + (idx >> 4), pi = (idx >> 1) & 7u, roleB = idx & 1u;
  const unsigned pair = h * 8u + pi;
  const unsigned klane = ((r32 >> 3) & 1u) * 1024u + hi * 256u + ((r32 & 7u) + 8u * (r32 >> 4)) * 16u, vlane_r = (r32 >> 4) * 1024u + hi * 256u + (r32 & 15u) * 16u;
  const int n1 = roleB ? 32 - 2 * (int)pi - NA : NA, tb1 = roleB ? NA : 0, ntot = roleB ? 34 - NA : NA;
  const unsigned wbase = 2u * MB16 + h * (DH * DM * 2) + wid * 8192u;
  auto tile_off = [&](int p) -> unsigned { return h * (SEQ * DH * 2) + (unsigned)(p < n1 ? tb1 + p : p - n1) * 32768u + wid * 8192u; };
  auto k_off = [&](int p) -> unsigned { return p >= ntot ? wbase + (unsigned)(p - ntot) * 32768u : tile_off(p); };
  auto v_off = [&](int p) -> unsigned { return p >= ntot ? wbase + 2u * 32768u : MB16 + tile_off(p); };
  K2C c;
  c.ldsb = (unsigned)(size_t)lds + wid * 8192u; c.vlane = lane * 16u;
  c.sK = mk_srd(KVW, 2u * MB16 + NH * DH * DM * 2);
  unsigned qb = 15u - pi;
  { const char* qp = Qf + ((size_t)h * 64u + qb * 4u + wid) * 16384u + klane;
#pragma unroll
    for (int i = 0; i < 16; ++i) c.q[i] = *(const bf16x8*)(qp + koff(i)); }
  SBAR();
  k2_dma8(c.ldsb, c.vlane, c.sK, k_off(0)); k2_dma8(c.ldsb + 32768u, c.vlane, c.sK, k_off(1)); k2_dma8(c.ldsb + 65536u, c.vlane, c.sK, v_off(0));
  c.kr = (lds_cptr)lds + klane; c.kslot_w = 0u; c.vslot_w = 32768u; c.vslot_r = 0u;
  asm volatile("s_waitcnt vmcnt(16)\n\ts_barrier" ::: "memory");
  int p = 0;
  const int nseg = roleB ? 2 : 1;
  for (int seg = 0; seg < nseg; ++seg) {
    const int pend_ = seg == 0 ? n1 : ntot;
    int t = seg == 0 ? tb1 : 0;
    const int row0 = (int)(qb * 128u + wid * 32u);
    auto setup = [&]() {
      c.vsoff = v_off(p + 1); c.ksoff = k_off(p + 2);
      c.domask = 64 * t + 63 > row0; c.dq = row0 + (int)r32 - 64 * t - 4 * (int)hi;
    };
    auto advance = [&]() {
      c.kslot_w ^= 32768u; c.vslot_w = c.vslot_w == 65536u ? 0u : c.vslot_w + 32768u; ++p; ++t;
      c.vr = (lds_cptr)lds + 65536u + c.vslot_r + vlane_r; c.vslot_r = c.vslot_r == 65536u ? 0u : c.vslot_r + 32768u;
    };
#pragma unroll
    for (int d = 0; d < 8; ++d) { c.o[d] = f32x16{}; asm volatile("" : "+a"(c.o[d])); }
    c.negm = f32x16{}; PINV(c.negm); c.l0 = 0.f; c.l1 = 0.f; c.pend = false; c.fct = 1.f;
#pragma unroll
    for (int i = 0; i < 16; ++i) asm volatile("" : "+a"(c.q[i]));
    asm volatile("s_nop 1"); SBAR();
    setup();
    k2_phaseA<false, true>(c); k2_phaseB<false, true>(c);
    if (seg == 1 && lane == 0) __hip_atomic_fetch_add(flags + pair * 2u, 1u, __ATOMIC_RELAXED, __HIP_MEMORY_SCOPE_AGENT);
    advance();
    while (p < pend_) {
      setup();
      k2_phaseA<true, true>(c); k2_phaseB<true, true>(c);
      advance();
    }
    if (seg + 1 < nseg) {
      const char* qp = Qf + ((size_t)h * 64u + pi * 4u + wid) * 16384u + klane;
#pragma unroll
      for (int i = 0; i < 16; ++i) c.q[i] = *(const bf16x8*)(qp + koff(i));
    } else k2_dma8(c.ldsb + 65536u + c.vslot_w, c.vlane, c.sK, wbase + 3u * 32768u);
    SBAR();
    k2_phaseA<true, false>(c); k2_phaseB<true, false>(c);
    MFMA_DRAIN(); SBAR();
#pragma unroll
    for (int d = 0; d < 8; ++d) asm volatile("" : "+a"(c.o[d]));
    float l = c.l0 + c.l1;
    { auto pr = __builtin_amdgcn_permlane32_swap(__float_as_uint(l), __float_as_uint(l), false, false); l = __uint_as_float(pr[0]) + __uint_as_float(pr[1]); }
    const srd_t sP = mk_srd(part + ((size_t)pair * 4u + wid) * 33792u, 33792u);
    if (roleB && seg == 0) {
#pragma unroll
      for (int d = 0; d < 8; ++d) {
#pragma unroll
        for (int k = 0; k < 4; ++k) {
          f32x4 v = {c.o[d][4 * k], c.o[d][4 * k + 1], c.o[d][4 * k + 2], c.o[d][4 * k + 3]};
          st16_sc1_a(v, sP, c.vlane, (4 * d + k) * 1024);
        }
        SBAR();
      }
      f32x4 v = {c.m, l, 0.f, 0.f};
      st16_sc1(v, sP, c.vlane, 32 * 1024);
    } else {
      K2E e;
      if (roleB) {
        const float rl = 1.0f / l;
#pragma unroll
        for (int d = 0; d < 8; ++d) {
#pragma unroll
          for (int s = 0; s < 2; ++s) { e.cx[2 * d + s] = pack8w(c.o[d], s, rl); PINV(e.cx[2 * d + s]); }
          SBAR();
        }
      } else {
        for (int spin = 0; spin < (1 << 22); ++spin) {
          if (rfl(__hip_atomic_load(flags + pair * 2u, __ATOMIC_RELAXED, __HIP_MEMORY_SCOPE_AGENT)) >= 4u) break;
          __builtin_amdgcn_s_sleep(2);
        }
        asm volatile("s_waitcnt vmcnt(0)\n\ts_barrier" ::: "memory");
        const f32x4 ml = __builtin_bit_cast(f32x4, __builtin_amdgcn_raw_buffer_load_b128(sP, c.vlane, 32 * 1024, 16));
        f32x4 ob[32];
#pragma unroll
        for (int k = 0; k < 32; ++k) ob[k] = __builtin_bit_cast(f32x4, __builtin_amdgcn_raw_buffer_load_b128(sP, c.vlane, k * 1024, 16));
        SBAR();
        const float mm = fmaxf(c.m, ml[0]), wa = __builtin_amdgcn_exp2f(c.m - mm), wb = __builtin_amdgcn_exp2f(ml[0] - mm);
        const float rl = 1.0f / (wa * l + wb * ml[1]), fa = wa * rl, fb = wb * rl;
#pragma unroll
        for (int d = 0; d < 8; ++d) {
#pragma unroll
          for (int s = 0; s < 2; ++s) {
#pragma unroll
            for (int i = 0; i < 4; ++i) { const int r = 8 * s + 2 * i;
              e.cx[2 * d + s][i] = cvtpk(c.o[d][r] * fa + ob[4 * d + (r >> 2)][r & 3] * fb, c.o[d][r + 1] * fa + ob[4 * d + ((r + 1) >> 2)][(r + 1) & 3] * fb); }
            PINV(e.cx[2 * d + s]);
          }
          SBAR();
        }
      }
      e.sP = mk_srd(Pf + ((size_t)h * 64u + qb * 4u + wid) * 28672u, 28672u);
      const unsigned kA = c.kslot_w, kB = c.kslot_w ^ 32768u;
      const unsigned v2 = 65536u + c.vslot_r, v3 = 65536u + c.vslot_w, v4 = 65536u + (98304u - c.vslot_r - c.vslot_w);
      const lds_cptr lb = (lds_cptr)lds + lane * 16u;
      asm volatile("s_waitcnt vmcnt(0)\n\ts_barrier" ::: "memory");
      e.rc = lb + kA;
#pragma unroll
      for (int i = 0; i < 8; ++i) c.fr[i] = lds_rd128(e.rc + i * 1024);
      e.rn = lb + kB; e.slotw = v4; e.wsoff = wbase + 4u * 32768u; SBAR(); k2_phaseC<0>(c, e);
      e.rc = e.rn; e.rn = lb + v2; e.slotw = kA; e.wsoff = wbase + 5u * 32768u; SBAR(); k2_phaseC<1>(c, e);
      e.rc = e.rn; e.rn = lb + v3; e.slotw = kB; e.wsoff = wbase + 6u * 32768u; SBAR(); k2_phaseC<2>(c, e);
      e.rc = e.rn; e.rn = lb + v4; SBAR(); k2_phaseC<3>(c, e);
      e.rc = e.rn; e.rn = lb + kA; SBAR(); k2_phaseC<4>(c, e);
      e.rc = e.rn; e.rn = lb + kB; SBAR(); k2_phaseC<5>(c, e);
      e.rc = e.rn; SBAR(); k2_phaseC<6>(c, e);
      MFMA_DRAIN(); PINV(c.s1); SBAR();
      st16(pack8w(c.s1, 0, 1.0f), e.sP, c.vlane, 26 * 1024); st16(pack8w(c.s1, 1, 1.0f), e.sP, c.vlane, 27 * 1024);
    }
    qb = pi;
  }
}
constexpr int OUTW = 2 * DM;
DI void k3_reduce(unsigned blk, const char* Pf, unsigned short* out) {
  const unsigned lane = threadIdx.x & 63u, task = blk * 8u + (threadIdx.x >> 6), rb = task / 28u, f = task % 28u;
  const char* p = Pf + (size_t)task * 1024u + lane * 16u;
  u32x4 v[16];
#pragma unroll
  for (int h = 0; h < 16; ++h) v[h] = *(const u32x4*)(p + (size_t)h * (64u * 28672u));
  SBAR();
  float s[8];
#pragma unroll
  for (int i = 0; i < 8; ++i) {
    float a[16];
#pragma unroll
    for (int h = 0; h < 16; ++h) a[h] = (i & 1) ? __uint_as_float(v[h][i >> 1] & 0xffff0000u) : __uint_as_float(v[h][i >> 1] << 16);
    s[i] = (((a[0] + a[1]) + (a[2] + a[3])) + ((a[4] + a[5]) + (a[6] + a[7]))) + (((a[8] + a[9]) + (a[10] + a[11])) + ((a[12] + a[13]) + (a[14] + a[15])));
  }
  *(u32x4*)(out + (size_t)(32u * rb + (lane & 31u)) * OUTW + 16u * f + 8u * (lane >> 5)) = (u32x4){cvtpk(s[0], s[1]), cvtpk(s[2], s[3]), cvtpk(s[4], s[5]), cvtpk(s[6], s[7])};
}

constexpr int HCOPY = 4160, HCH = 4 * HCOPY, XROW = 80, XCH = 96 * XROW;
constexpr int XOFF = 65536, K3_LDS = XOFF + 2 * XCH;
struct K3S { lds_cptr ha, xa; f32x16 y0, y1; s16x4 al[4][2], ah[4][2]; bf16x8 bx[4][2]; };
template <int e> DI void k3_ssm_rd(K3S& c) {
  constexpr int s = e & 3;
  c.al[s][0] = *(const volatile LAS s16x4*)(c.ha + e * 64); c.ah[s][0] = *(const volatile LAS s16x4*)(c.ha + e * 64 + 8);
  c.bx[s][0] = lds_rd128(c.xa + (47 - e) * XROW); c.bx[s][1] = lds_rd128(c.xa + (15 - e) * XROW);
  c.al[s][1] = *(const volatile LAS s16x4*)(c.ha + (e + 32) * 64); c.ah[s][1] = *(const volatile LAS s16x4*)(c.ha + (e + 32) * 64 + 8);
}
template <int e> DI void k3_ssm_step(K3S& c) {
  constexpr int s = e & 3;
  if constexpr (e + 2 < 16) k3_ssm_rd<e + 2>(c);
  SBAR();
  const bf16x8 a0 = cat(c.al[s][0], c.ah[s][0]), a1 = cat(c.al[s][1], c.ah[s][1]);
  c.y1 = MFMA32(a0, c.bx[s][0], c.y1); c.y0 = MFMA32(a0, c.bx[s][1], c.y0); c.y1 = MFMA32(a1, c.bx[s][1], c.y1);
  SBAR();
}
template <int... Es> DI void k3_ssm_loop(K3S& c, iseq<Es...>) { (k3_ssm_step<Es>(c), ...); }
DI void k3_ssm(char* lds, unsigned blk, const unsigned short* bq, const unsigned short* xT, const unsigned short* ssmk, const unsigned short* ssmd, unsigned short* out) {
  const unsigned tid = threadIdx.x, lane = tid & 63u, wid = rfl(tid >> 6), hi = lane >> 5, r = lane & 31u;
  const unsigned c0 = 2u * (28u * (blk & 7u) + (blk >> 3));
  const unsigned ch = wid >> 2, n = tid & 255u;
  const srd_t sh = mk_srd(ssmk + (size_t)(c0 + ch) * SEQ, SEQ * 2);
  const u32x4 hA = __builtin_amdgcn_raw_buffer_load_b128(sh, n * 16u, 0, 0); const u32x2 hB = __builtin_amdgcn_raw_buffer_load_b64(sh, n * 16u + 16u, 0, 0);
  const u32x4 xw = *(const u32x4*)(xT + ((size_t)(n >> 3) * DM + c0 + ch) * 64u + 8u * (n & 7u));
  const unsigned dw = *(const unsigned*)(ssmd + c0);
  SBAR();
  LAS char* xb = (LAS char*)lds + XOFF + ch * XCH;
  if (n < 160u) *(LAS u32x4*)(xb + n * 16u) = (u32x4){0u, 0u, 0u, 0u};
  {
    const unsigned w[6] = {hA[0], hA[1], hA[2], hA[3], hB[0], hB[1]};
    unsigned o[5];
#pragma unroll
    for (int k = 0; k < 5; ++k) o[k] = __builtin_amdgcn_alignbyte(w[k + 1], w[k], 2);
    LAS char* dst = (LAS char*)lds + ch * HCH + n * 16u + 64u;
    *(LAS u32x4*)(dst) = (u32x4){o[0], o[1], o[2], o[3]};
    *(LAS u32x4*)(dst + HCOPY) = (u32x4){w[1], w[2], w[3], w[4]};
    *(LAS u32x4*)(dst + 2 * HCOPY) = (u32x4){o[1], o[2], o[3], o[4]};
    *(LAS u32x4*)(dst + 3 * HCOPY) = (u32x4){w[2], w[3], w[4], w[5]};
    if (n < 4u) {
      const unsigned z0 = n ? 0u : w[0], z1 = n ? 0u : w[1], zo = n ? 0u : o[0];
      LAS char* d2 = (LAS char*)lds + ch * HCH + (3u - n) * 16u;
      *(LAS u32x4*)(d2) = (u32x4){0u, 0u, 0u, z0 << 16};
      *(LAS u32x4*)(d2 + HCOPY) = (u32x4){0u, 0u, 0u, z0};
      *(LAS u32x4*)(d2 + 2 * HCOPY) = (u32x4){0u, 0u, z0 << 16, zo};
      *(LAS u32x4*)(d2 + 3 * HCOPY) = (u32x4){0u, 0u, z0, z1};
    }
    *(LAS u32x4*)(xb + (32u + (n >> 2)) * XROW + (n & 3u) * 16u) = xw;
  }
  SBAR();
  unsigned qw[4];
#pragma unroll
  for (int i = 0; i < 4; ++i) qw[i] = *(const unsigned*)(bq + (size_t)(tid + 512u * i) * DM + c0);
  asm volatile("s_waitcnt lgkmcnt(0)\n\ts_barrier" ::: "memory");
  const unsigned dh = wid & 1u, eh = (wid >> 1) & 1u;
  K3S c;
  c.ha = (lds_cptr)lds + ch * HCH + (r & 3u) * HCOPY + (24u - 16u * dh - 8u * hi + 4u * (r >> 2)) * 2u + eh * 1024u;
  c.xa = (lds_cptr)lds + XOFF + ch * XCH + (r + 17u - 16u * eh) * XROW + (2u * dh + hi) * 16u;
  c.y0 = f32x16{}; c.y1 = f32x16{};
  k3_ssm_rd<0>(c); k3_ssm_rd<1>(c);
  k3_ssm_loop(c, mkseq<16>{});
  const f32x16 y0 = c.y0, y1 = c.y1;
  asm volatile("s_barrier" ::: "memory");
  LAS char* ys = (LAS char*)lds + wid * 8192u;
#pragma unroll
  for (int k = 0; k < 4; ++k) {
    const unsigned o = r * 128u + (((2u * k + hi) ^ (r & 7u)) << 4);
    *(LAS f32x4*)(ys + o) = (f32x4){y0[4 * k], y0[4 * k + 1], y0[4 * k + 2], y0[4 * k + 3]};
    *(LAS f32x4*)(ys + 4096u + o) = (f32x4){y1[4 * k], y1[4 * k + 1], y1[4 * k + 2], y1[4 * k + 3]};
  }
  asm volatile("s_waitcnt lgkmcnt(0)\n\ts_barrier" ::: "memory");
#pragma unroll
  for (int i = 0; i < 4; ++i) {
    const unsigned t = tid + 512u * i, row = t >> 5, bb = t & 31u;
    const unsigned yo = row * 128u + (((bb >> 2) ^ (row & 7u)) << 4) + (bb & 3u) * 4u, xo = (32u + row) * XROW + (bb & 24u) * 2u + (7u - (bb & 7u)) * 2u;
    unsigned res = 0;
#pragma unroll
    for (int cc = 0; cc < 2; ++cc) {
      const LAS char* yp = (const LAS char*)lds + cc * 32768 + yo;
      const float y = (*(const LAS float*)yp + *(const LAS float*)(yp + 8192)) + (*(const LAS float*)(yp + 16384) + *(const LAS float*)(yp + 24576));
      const float kv = bf2f(*(const LAS unsigned short*)((LAS char*)lds + XOFF + cc * XCH + xo));
      const float z = rbf(rbf(y) + rbf(kv * bf2f(cc ? dw >> 16 : dw & 0xffffu)));
      res |= (cvtpk(bf2f(cc ? qw[i] >> 16 : qw[i] & 0xffffu) * z, 0.f) & 0xffffu) << (16 * cc);
    }
    *(unsigned*)(out + (size_t)t * OUTW + DM + c0) = res;
  }
}

__global__ __launch_bounds__(512, 4) void k3_out(const char* __restrict__ Pf, const unsigned short* __restrict__ bq, const unsigned short* __restrict__ xT,
                                                 const unsigned short* __restrict__ ssmk, const unsigned short* __restrict__ ssmd, unsigned short* __restrict__ out) {
  extern __shared__ __attribute__((aligned(16))) char lds[];
  const unsigned b = blockIdx.x;
  if (b < 224u) k3_ssm(lds, b, bq, xT, ssmk, ssmd, out); else k3_reduce(b - 224u, Pf, out);
}

extern "C" void kernel_launch(void* const* d_in, const int* in_sizes, int n_in, void* d_out, int out_size, void* d_ws, size_t ws_size, hipStream_t stream) {
  typedef const unsigned short* cu16;
  char* ws = (char*)d_ws;
  char* Qf = ws; char* Kf = ws + MB16; char* Vf = ws + 2 * (size_t)MB16; char* Wf = ws + 3 * (size_t)MB16;
  char* xT = Wf + (4u << 20); char* fl = xT + (2u << 20); char* part = fl + (1u << 20); char* Pf = part + (17u << 20);
  (void)hipFuncSetAttribute((const void*)k1_proj, hipFuncAttributeMaxDynamicSharedMemorySize, 147712);
  (void)hipFuncSetAttribute((const void*)k2_attn, hipFuncAttributeMaxDynamicSharedMemorySize, 163840);
  k1_proj<<<256, 256, 147712, stream>>>((cu16)d_in[0], (cu16)d_in[1], (cu16)d_in[2], (cu16)d_in[3], (cu16)d_in[4], Qf, Kf, Vf, (unsigned*)fl,
                                        (cu16)d_in[5], Wf, (cu16)d_in[7], (cu16)d_in[8], (cu16)d_in[9], (unsigned short*)xT);
  k2_attn<<<256, 256, 163840, stream>>>(Qf, Kf, Pf, part, (unsigned*)fl);
  (void)hipFuncSetAttribute((const void*)k3_out, hipFuncAttributeMaxDynamicSharedMemorySize, K3_LDS);
  k3_out<<<448, 512, K3_LDS, stream>>>(Pf, (cu16)d_in[6], (cu16)xT, (cu16)d_in[10], (cu16)d_in[11], (unsigned short*)d_out);
}
```

```cpp
#include <hip/hip_runtime.h>
#include <hip/hip_bf16.h>
#include <utility>

typedef short    bf16x8 __attribute__((ext_vector_type(8)));
typedef short    s16x4  __attribute__((ext_vector_type(4)));
typedef float    f32x16 __attribute__((ext_vector_type(16)));
typedef float    f32x4  __attribute__((ext_vector_type(4)));
typedef float    f32x2  __attribute__((ext_vector_type(2)));
typedef __bf16   bf16x2 __attribute__((ext_vector_type(2)));
typedef unsigned u32x4  __attribute__((ext_vector_type(4)));
typedef unsigned u32x2  __attribute__((ext_vector_type(2)));
#define DI __device__ __forceinline__
#define LAS __attribute__((address_space(3)))
#define SBAR() __builtin_amdgcn_sched_barrier(0)
#define PINV(x) asm volatile("" : "+v"(x))
#define PINA(x) asm volatile("" : "+a"(x))
#define MFMA32(a, b, c) __builtin_amdgcn_mfma_f32_32x32x16_bf16((a), (b), (c), 0, 0, 0)
#define MFMA_DRAIN() asm volatile("s_nop 7\n\ts_nop 7" ::: "memory")
typedef LAS const char* lds_cptr;
typedef __amdgpu_buffer_rsrc_t srd_t;
template <int... Is> using iseq = std::integer_sequence<int, Is...>;
template <int N> using mkseq = std::make_integer_sequence<int, N>;

DI unsigned rfl(unsigned x) { return __builtin_amdgcn_readfirstlane(x); }
DI unsigned lane_id() { unsigned l; asm volatile("v_mbcnt_lo_u32_b32 %0, -1, 0\n\tv_mbcnt_hi_u32_b32 %0, -1, %0" : "=v"(l)); return l; }
DI unsigned cvtpk(float lo, float hi) { f32x2 v = {lo, hi}; bf16x2 b = __builtin_convertvector(v, bf16x2); return __builtin_bit_cast(unsigned, b); }
DI float bf2f(unsigned h) { return __uint_as_float(h << 16); }
DI float rbf(float x) { return __uint_as_float(cvtpk(x, x) << 16); }
DI bf16x8 lds_rd128(lds_cptr p) { return *(const LAS bf16x8*)p; }
DI s16x4 lds_tr(lds_cptr p) { return __builtin_amdgcn_ds_read_tr16_b64_v4i16((LAS s16x4*)p); }
DI bf16x8 cat(s16x4 l, s16x4 h) { return __builtin_shufflevector(l, h, 0, 1, 2, 3, 4, 5, 6, 7); }
DI srd_t mk_srd(const void* p, unsigned bytes) { return __builtin_amdgcn_make_buffer_rsrc((void*)p, (short)0, (int)bytes, 0x00020000); }
template <unsigned MI, unsigned SI> DI void dma16(unsigned mb, unsigned voff, srd_t srd, unsigned sb) {
  unsigned t;
  asm volatile("s_add_u32 m0, %1, %5\n\ts_add_u32 %0, %4, %6\n\tbuffer_load_dwordx4 %2, %3, %0 offen lds" : "=&s"(t) : "s"(mb), "v"(voff), "s"(srd), "s"(sb), "n"(MI), "n"(SI) : "memory", "scc");
}
template <unsigned SI> DI void st16w(u32x4 v, srd_t srd, unsigned voff, unsigned sb) {
  unsigned t; asm volatile("s_add_u32 %0, %4, %5\n\tbuffer_store_dwordx4 %1, %2, %3, %0 offen sc1\n\ts_nop 1" : "=&s"(t) : "v"(v), "v"(voff), "s"(srd), "s"(sb), "n"(SI) : "memory", "scc"); }
template <unsigned SI> DI void st16wa(f32x4 v, srd_t srd, unsigned voff, unsigned sb) {
  unsigned t; asm volatile("s_add_u32 %0, %4, %5\n\tbuffer_store_dwordx4 %1, %2, %3, %0 offen sc1\n\ts_nop 1" : "=&s"(t) : "a"(v), "v"(voff), "s"(srd), "s"(sb), "n"(SI) : "memory", "scc"); }
template <int N, class F> DI void sfor_(F&& f, iseq<>) {}
template <int N, class F, int I, int... Is> DI void sfor_(F&& f, iseq<I, Is...>) { f(std::integral_constant<int, I>{}); sfor_<N>(f, iseq<Is...>{}); }
template <int N, class F> DI void sfor(F&& f) { sfor_<N>(f, mkseq<N>{}); }
DI u32x4 pack8(const f32x4& a, const f32x4& b, float sc) { u32x4 w = {cvtpk(a[0] * sc, a[1] * sc), cvtpk(a[2] * sc, a[3] * sc), cvtpk(b[0] * sc, b[1] * sc), cvtpk(b[2] * sc, b[3] * sc)}; return w; }
DI u32x4 pack8w(const f32x16& a, int s, float sc) {
  u32x4 w = {cvtpk(a[8 * s] * sc, a[8 * s + 1] * sc), cvtpk(a[8 * s + 2] * sc, a[8 * s + 3] * sc), cvtpk(a[8 * s + 4] * sc, a[8 * s + 5] * sc), cvtpk(a[8 * s + 6] * sc, a[8 * s + 7] * sc)};
  return w;
}
constexpr int SEQ = 2048, NH = 16, DH = 256, DM = 448, DC = 1024;
constexpr unsigned WRS = NH * DH * 2;
constexpr unsigned MB16 = SEQ * NH * DH * 2;
constexpr float QSCALE = 1.4426950408889634f / 16.0f;

typedef __attribute__((address_space(1))) unsigned long long gu64;
#define RLX_AGENT __ATOMIC_RELAXED, __HIP_MEMORY_SCOPE_AGENT
struct Params {
  const unsigned short *x, *ckv, *wq, *wuk, *wuv, *wo, *bq, *bk, *bv, *shk, *ssmk, *ssmd;
  unsigned short* out; char* ws;
};
constexpr size_t WS_K = MB16, WS_W = 3 * (size_t)MB16, WS_XT = WS_W + (4u << 20), WS_FL = WS_XT + (2u << 20), WS_PART = WS_FL + (1u << 20), WS_PF = WS_PART + (17u << 20);
constexpr int FL_EPOCH = 0, FL_S1 = 16, FL_S2 = FL_S1 + 256 * 16, FL_S3 = FL_S2 + 128 * 16;
DI void publish(gu64* flag, unsigned long long tag, unsigned wid) {
  asm volatile("s_waitcnt vmcnt(0)\n\ts_barrier" ::: "memory");
  if (wid == 0 && lane_id() == 0) __hip_atomic_store(flag, tag, RLX_AGENT);
}
DI void wait_flags(gu64* lane_flag, unsigned long long tag) {
  for (int spin = 0; spin < (1 << 20); ++spin) {
    if (__all(__hip_atomic_load(lane_flag, RLX_AGENT) == tag)) break;
    __builtin_amdgcn_s_sleep(1);
  }
}

DI void mfma16a(f32x4& c, const bf16x8& a, const bf16x8& b) { asm volatile("v_mfma_f32_16x16x32_bf16 %0, %1, %2, %0" : "+a"(c) : "v"(a), "v"(b)); }
DI void mfma16v(f32x4& c, const bf16x8& a, const bf16x8& b) { asm volatile("v_mfma_f32_16x16x32_bf16 %0, %1, %2, %0" : "+v"(c) : "v"(a), "v"(b)); }
DI void mfma16z(f32x4& c, const bf16x8& a, const bf16x8& b) { asm volatile("v_mfma_f32_16x16x32_bf16 %0, %1, %2, 0" : "=&v"(c) : "v"(a), "v"(b)); }
struct Frags { bf16x8 x[8]; s16x4 wl[8], wh[8]; };
struct K1C {
  unsigned ldsb, lds8, lds4, wid, vlane;
  unsigned vx_kv[2], vx_q[2], vw[4];
  srd_t sX, sW, sXq, sWq, sO;
  unsigned sx_kv, sw_kv, sx_q, sw_q, so;
  lds_cptr xr_kv[2], xr_q[2], wr[8];
  const char* pf; unsigned pfstep;
  f32x4 acc[8][8], qacc[4][8]; Frags f0, f1; f32x4 ta, tb; u32x4 pk;
  srd_t sBk, sBv, sBs, sWo; unsigned b, hb, kw[11], vv[8]; u32x4 sk;
};
DI void k1_touch(const K1C& c) {
  asm volatile("s_add_u32 m0, %0, 147456\n\ts_nop 0\n\tglobal_load_lds_dword %1, off" :: "s"(c.ldsb), "v"(c.pf) : "memory", "scc"); }
template <int KIND, int buf, int j> DI void k1_piece(const K1C& c, unsigned sw, unsigned sx) {
  if constexpr (KIND == 0) {
    constexpr int p = j >> 1;
    if constexpr (!(j & 1)) dma16<buf * 32768u + 65536u + p * 1024u, 2u * p * WRS>(c.lds8, c.vw[(p & 1) + 2 * (p >> 2)], c.sW, sw);
    else          dma16<buf * 32768u + p * 1024u, 8u * p * (DC * 2)>(c.lds8, c.vx_kv[(p >> 1) & 1], c.sX, sx);
  } else if constexpr (KIND == 1 && j < 12) {
    if constexpr (j % 3 != 1) { constexpr int p = 2 * (j / 3) + (j % 3 == 2); dma16<buf * 32768u + 65536u + p * 1024u, 2u * p * WRS>(c.lds8, c.vw[(p & 1) + 2 * (p >> 2)], c.sWq, sw); }
    else { constexpr int p = j / 3; dma16<buf * 32768u + p * 1024u, 8u * p * (DM * 2)>(c.lds4, c.vx_q[(p >> 1) & 1], c.sXq, sx); }
  }
}
template <int KIND> DI unsigned k1_sw(const K1C& c, int st) { return (KIND == 0 ? c.sw_kv : c.sw_q) + (unsigned)st * (64u * WRS); }
template <int KIND> DI unsigned k1_sx(const K1C& c, int st) { return (KIND == 0 ? c.sx_kv : c.sx_q) + (unsigned)st * 128u; }
template <int KIND, int buf, int... Js> DI void k1_stage(const K1C& c, int st, iseq<Js...>) { (k1_piece<KIND, buf, Js>(c, k1_sw<KIND>(c, st), k1_sx<KIND>(c, st)), ...); }
template <int NX, int buf, int ks, int i> DI void k1_read(const K1C& c, Frags& f) {
  constexpr int so = buf * 32768;
  if constexpr (i < 2 || i >= 2 + NX) { constexpr int k = i < 2 ? i : i - NX, nb = k >> 1, t = k & 1;
    const s16x4 v = lds_tr(c.wr[nb] + so + ks * 16384 + t * 2048); if constexpr (t) f.wh[nb] = v; else f.wl[nb] = v; }
  else { constexpr int mb = i - 2; f.x[mb] = lds_rd128((NX == 8 ? c.xr_kv[ks] : c.xr_q[ks]) + so + (mb >> 1) * 4096 + (mb & 1) * 1024); }
}
template <int... Is> DI void k1_read0(K1C& c, iseq<Is...>) { (k1_read<8, 0, 0, Is>(c, c.f0), ...); }
template <bool WA, int NXN, int KB, int buf, bool PF, int G> DI void k1_gap(K1C& c, int sb) {
  constexpr int ks = G >> 6, g = G & 63, nb = g >> 3, mb = g & 7;
  Frags& fc = ks ? c.f1 : c.f0; Frags& fn = ks ? c.f0 : c.f1;
  if constexpr (G == 64) { asm volatile("s_waitcnt vmcnt(1) lgkmcnt(0)\n\ts_barrier" ::: "memory"); SBAR(); }
  const bf16x8 w = cat(fc.wl[nb], fc.wh[nb]);
  if constexpr (G == 0) asm volatile("s_nop 1" :: "v"(w), "v"(fc.x[0]));
  if constexpr (WA) mfma16a(c.acc[mb][nb], w, fc.x[mb]); else mfma16a(c.acc[nb][mb], fc.x[mb], w);
  if constexpr (!(g & 1)) {
    if constexpr (ks == 0) { if constexpr (g / 2 < 24) k1_read<8, buf, 1, g / 2>(c, fn); }
    else if constexpr (g / 2 < 16 + NXN) k1_read<NXN, buf ^ 1, 0, g / 2>(c, fn);
  }
  if constexpr (ks == 1 && (g & 3) == 1) k1_piece<KB, buf, (g >> 2)>(c, k1_sw<KB>(c, sb), k1_sx<KB>(c, sb));
  if constexpr (G == 127) {
    k1_touch(c);
    if constexpr (PF) c.pf += c.pfstep;
  }
  SBAR();
}
template <bool WA, int NXN, int KB, int buf, bool PF, int... Gs> DI void k1_step_(K1C& c, int sb, iseq<Gs...>) { (k1_gap<WA, NXN, KB, buf, PF, Gs>(c, sb), ...); }
template <bool WA, int NXN, int KB, int buf, bool PF> DI void k1_step(K1C& c, int sb) { k1_step_<WA, NXN, KB, buf, PF>(c, sb, mkseq<128>{}); }
template <bool WA, int T, int G> DI void k1_qgap(K1C& c) {
  constexpr int buf = T & 1, ks = G >> 5, g = G & 31, nb = g >> 2, mb = g & 3;
  Frags& fc = ks ? c.f1 : c.f0; Frags& fn = ks ? c.f0 : c.f1;
  if constexpr (G == 32 && T < 6) {
    asm volatile("s_waitcnt vmcnt(3) lgkmcnt(0)\n\ts_barrier" ::: "memory"); SBAR(); }
  const bf16x8 w = cat(fc.wl[nb], fc.wh[nb]);
  if constexpr (T == 0 && ks == 0) mfma16z(c.qacc[mb][nb], w, fc.x[mb]); else mfma16v(c.qacc[mb][nb], w, fc.x[mb]);
  SBAR();
  if constexpr (g < 25 && g % 5 != 4) { constexpr int i = g - g / 5;
    if constexpr (ks == 0) k1_read<4, buf, 1, i>(c, fn); else if constexpr (T < 6) k1_read<4, buf ^ 1, 0, i>(c, fn); }
  if constexpr (ks == 1 && T + 2 < 7 && (g & 1) == 0 && g < 24) k1_piece<1, buf, (g >> 1)>(c, k1_sw<1>(c, T + 2), k1_sx<1>(c, T + 2));
  if constexpr (T == 6 && G >= 26 && G < 30) {
    constexpr int j = G - 26; const unsigned ln = c.vlane >> 4;
    dma16<131072u + j * 1024u, j * 256u>(c.lds4, (ln >> 2) * (DM * 2) + (ln & 3u) * 16u, c.sWo, c.hb * (16u * DM * 2) + c.wid * 64u);
  }
  if constexpr (T == 6 && G >= 36 && G < 56) {
    constexpr int i = G - 36; const unsigned tid = c.wid * 64u + (c.vlane >> 4), bc0 = 64u * (c.b % 7u) + 2u * (tid & 31u), vo = ((64u * (c.b / 7u) + 8u * (tid >> 5)) * DM + bc0) * 2u;
    if constexpr (i < 11) asm volatile("buffer_load_dword %0, %1, %2, 0 offen" : "=v"(c.kw[i]) : "v"(vo + (unsigned)((i - 3) * DM * 2)), "s"(c.sBk) : "memory");
    else if constexpr (i < 19) asm volatile("buffer_load_dword %0, %1, %2, 0 offen" : "=v"(c.vv[i - 11]) : "v"(vo + (unsigned)((i - 11) * DM * 2)), "s"(c.sBv) : "memory");
    else asm volatile("buffer_load_dwordx4 %0, %1, %2, 0 offen" : "=v"(c.sk) : "v"(bc0 * 8u), "s"(c.sBs) : "memory");
  }
  constexpr int u = 5 * T + G / 12, ph = G % 12;
  if constexpr (G < 60 && u < 32) {
    constexpr int i = u >> 2, j = u & 3;
    if constexpr (ph == 2) { c.ta = c.acc[i][2 * j]; PINV(c.ta); }
    if constexpr (ph == 4) { c.tb = c.acc[i][2 * j + 1]; PINV(c.tb); }
    if constexpr (ph == 6) { c.pk[0] = cvtpk(c.ta[0], c.ta[1]); c.pk[1] = cvtpk(c.ta[2], c.ta[3]); PINV(c.pk); }
    if constexpr (ph == 8) { c.pk[2] = cvtpk(c.tb[0], c.tb[1]); c.pk[3] = cvtpk(c.tb[2], c.tb[3]); PINV(c.pk); }
    if constexpr (ph == 11) st16w<(WA ? (i >> 1) * 16384u + j * 2048u : (i >> 1) * 2048u + j * 16384u) + (i & 1) * 1024u>(c.pk, c.sO, c.vlane, c.so);
  }
  SBAR();
}
template <bool WA, int T, int... Gs> DI void k1_qstep_(K1C& c, iseq<Gs...>) { (k1_qgap<WA, T, Gs>(c), ...); }
template <bool WA, int... Ts> DI void k1_qloop(K1C& c, iseq<Ts...>) { (k1_qstep_<WA, Ts>(c, mkseq<64>{}), ...); }

DI f32x2 up2(unsigned w) { f32x2 r = {__uint_as_float(w << 16), __uint_as_float(w & 0xffff0000u)}; return r; }
DI f32x2 rb2(f32x2 v) { return up2(cvtpk(v[0], v[1])); }
template <bool WA> DI void k1_main(K1C& c, char* lds, unsigned b, char* Wf, char* Qdst, const char* pf2, unsigned pfstep2, unsigned short* xT) {
#pragma unroll
  for (int i = 0; i < 8; ++i)
#pragma unroll
    for (int j = 0; j < 8; ++j) { c.acc[i][j] = f32x4{}; PINA(c.acc[i][j]); }
  SBAR();
  k1_stage<0, 1>(c, 1, mkseq<16>{});
  k1_touch(c); c.pf += c.pfstep;
  asm volatile("s_waitcnt vmcnt(17)\n\ts_barrier" ::: "memory");
  k1_read0(c, mkseq<24>{});
  SBAR();
#pragma nounroll
  for (int t = 0; t < 14; t += 2) {
    c.pf = t == 10 ? pf2 : c.pf;
    k1_step<WA, 8, 0, 0, true>(c, t + 2);
    k1_step<WA, 8, 0, 1, true>(c, t + 3);
  }
  k1_step<WA, 8, 1, 0, false>(c, 0);
  k1_step<WA, 4, 1, 1, false>(c, 1);
  k1_qloop<WA>(c, mkseq<7>{});
  MFMA_DRAIN(); SBAR();
  const srd_t sq = mk_srd(Qdst, 32768u);
  sfor<16>([&](auto I) { constexpr int mb = I >> 2, db = I & 3; PINV(c.qacc[mb][2 * db]); PINV(c.qacc[mb][2 * db + 1]);
    st16w<(mb >> 1) * 16384u + db * 2048u + (mb & 1) * 1024u>(pack8(c.qacc[mb][2 * db], c.qacc[mb][2 * db + 1], QSCALE), sq, c.vlane, 0u); });
  asm volatile("s_waitcnt vmcnt(16)" : "+v"(c.kw[0]), "+v"(c.kw[1]), "+v"(c.kw[2]), "+v"(c.kw[3]), "+v"(c.kw[4]), "+v"(c.kw[5]), "+v"(c.kw[6]), "+v"(c.kw[7]), "+v"(c.kw[8]), "+v"(c.kw[9]), "+v"(c.kw[10]),
               "+v"(c.vv[0]), "+v"(c.vv[1]), "+v"(c.vv[2]), "+v"(c.vv[3]), "+v"(c.vv[4]), "+v"(c.vv[5]), "+v"(c.vv[6]), "+v"(c.vv[7]), "+v"(c.sk) :: "memory");
  const unsigned lane = lane_id();
  {
    const unsigned q = (lane & 15u) >> 2, p = lane & 3u;
    const lds_cptr rp = (lds_cptr)lds + 131072u + c.wid * 4096u + (4u * (lane >> 5) + q) * 64u + 32u * ((lane >> 4) & 1u) + 16u * (p & 1u) + 8u * (p >> 1);
    const srd_t sw_ = mk_srd(Wf + (size_t)(c.hb >> 4) * (DH * DM * 2), DH * DM * 2);
    sfor<4>([&](auto J) { constexpr int j = J;
      const bf16x8 w = cat(lds_tr(rp + j * 1024), lds_tr(rp + j * 1024 + 512));
      st16w<j * 65536u>(__builtin_bit_cast(u32x4, w), sw_, c.vlane, (c.wid * 16u + (c.hb & 15u)) * 1024u); });
  }
  {
    const srd_t sx_ = mk_srd(xT, SEQ * DM * 2);
    const unsigned tid = c.wid * 64u + lane, bc0 = 64u * (b % 7u) + 2u * (tid & 31u);
    const f32x2 s0 = {bf2f(c.sk[0] & 0xffffu), bf2f(c.sk[2] & 0xffffu)}, s1 = {bf2f(c.sk[0] >> 16), bf2f(c.sk[2] >> 16)};
    const f32x2 s2 = {bf2f(c.sk[1] & 0xffffu), bf2f(c.sk[3] & 0xffffu)}, s3 = {bf2f(c.sk[1] >> 16), bf2f(c.sk[3] >> 16)};
    f32x2 kf[11]; unsigned kv[8];
#pragma unroll
    for (int i = 0; i < 11; ++i) kf[i] = up2(c.kw[i]);
#pragma unroll
    for (int i = 0; i < 8; ++i) {
      f32x2 a = rb2(kf[i] * s3); a = rb2(a + rb2(kf[i + 1] * s2)); a = rb2(a + rb2(kf[i + 2] * s1)); a = rb2(a + rb2(kf[i + 3] * s0));
      a = a * up2(c.vv[i]); kv[i] = cvtpk(a[0], a[1]);
    }
    const unsigned xo = (((b / 7u) * DM + bc0) * 64u + 8u * (tid >> 5)) * 2u;
    st16w<0>((u32x4){__builtin_amdgcn_perm(kv[6], kv[7], 0x05040100u), __builtin_amdgcn_perm(kv[4], kv[5], 0x05040100u), __builtin_amdgcn_perm(kv[2], kv[3], 0x05040100u), __builtin_amdgcn_perm(kv[0], kv[1], 0x05040100u)}, sx_, xo, 0u);
    st16w<128>((u32x4){__builtin_amdgcn_perm(kv[6], kv[7], 0x07060302u), __builtin_amdgcn_perm(kv[4], kv[5], 0x07060302u), __builtin_amdgcn_perm(kv[2], kv[3], 0x07060302u), __builtin_amdgcn_perm(kv[0], kv[1], 0x07060302u)}, sx_, xo, 0u);
  }
}

DI void phase_proj(const Params& P, char* lds, const unsigned wid) {
  const unsigned short *x = P.x, *ckv = P.ckv, *wq = P.wq, *wuk = P.wuk, *wuv = P.wuv, *wo = P.wo, *bk = P.bk, *bv = P.bv, *shk = P.shk;
  char *Qf = P.ws, *Kf = P.ws + WS_K, *Vf = P.ws + WS_K + MB16, *Wf = P.ws + WS_W; unsigned short* xT = (unsigned short*)(P.ws + WS_XT);
  const unsigned lane = lane_id(), wr = wid >> 1, wc = wid & 1;
  const unsigned b = blockIdx.x, xcd = b & 7u, idx = b >> 3;
  const unsigned pm = idx & 7u, which = idx >> 3, h = 2u * xcd + (which >> 1), isV = which & 1u;
  const unsigned qm = idx & 15u, hq = 2u * xcd + (idx >> 4);
  K1C c;
  c.ldsb = (unsigned)(size_t)lds; c.lds8 = c.ldsb + wid * 8192u; c.lds4 = c.ldsb + wid * 4096u; c.wid = wid; c.vlane = lane * 16u;
  const unsigned short* wkv = isV ? wuv : wuk;
  c.sX = mk_srd(ckv, SEQ * DC * 2); c.sW = mk_srd(wkv, DC * WRS); c.sXq = mk_srd(x, SEQ * DM * 2); c.sWq = mk_srd(wq, DM * WRS);
#pragma unroll
  for (int par = 0; par < 2; ++par) {
    const unsigned ch = 16u * ((lane & 7u) ^ (4u * par + (lane >> 4)));
    c.vx_kv[par] = (lane >> 3) * (DC * 2) + ch; c.vx_q[par] = (lane >> 3) * (DM * 2) + ch;
  }
#pragma unroll
  for (int v = 0; v < 4; ++v) {
    const unsigned f = (2u * (v & 1) + (lane >> 5)) | (4u * (v >> 1));
    c.vw[v] = (lane >> 5) * WRS + 16u * ((lane & 31u) ^ (f << 1));
  }
  c.sx_kv = (256u * pm + 64u * wid) * (DC * 2); c.sw_kv = 16u * wid * WRS + h * (DH * 2);
  c.sx_q = (128u * qm + 32u * wid) * (DM * 2);  c.sw_q = 16u * wid * WRS + hq * (DH * 2);
  k1_stage<0, 0>(c, 0, mkseq<16>{});
  const unsigned rho = lane & 15u, g = lane >> 4;
#pragma unroll
  for (int ks = 0; ks < 2; ++ks) {
    const unsigned row = 16u * (rho >> 3) + (rho & 7u), ch = 16u * ((4u * ks + g) ^ (rho >> 1));
    c.xr_kv[ks] = (lds_cptr)lds + (wr * 128u + row) * 128u + ch; c.xr_q[ks] = (lds_cptr)lds + (wr * 64u + row) * 128u + ch;
    PINV(c.xr_kv[ks]); PINV(c.xr_q[ks]);
  }
  { const unsigned q = (lane & 15u) >> 2, p = lane & 3u, f = q | ((g & 1u) << 2);
#pragma unroll
    for (int nb = 0; nb < 8; ++nb) { c.wr[nb] = (lds_cptr)lds + 65536u + (8u * g + q) * 512u + (8u * wc + ((unsigned)nb ^ f)) * 32u + 8u * p; PINV(c.wr[nb]); } }
  c.sO = mk_srd((isV ? Vf : Kf) + (size_t)h * (SEQ * DH * 2), SEQ * DH * 2);
  c.so = (8u * pm + 4u * wr) * 16384u + wc * 8192u;
  constexpr unsigned PFD = 5;
  const bool pw = lane >= 32u;
  c.pf = pw ? (const char*)wkv + (size_t)(PFD * 64u + 8u * pm + 2u * wid + ((lane >> 2) & 1u)) * WRS + h * (DH * 2) + (lane & 3u) * 128u
            : (const char*)ckv + (size_t)(256u * pm + 64u * which + 16u * wid + (lane & 15u)) * (DC * 2) + PFD * 128u;
  c.pfstep = pw ? 64u * WRS : 128u;
  const char* pf2 = pw ? (const char*)wq + (size_t)(128u + 4u * qm + wid) * WRS + hq * (DH * 2) + (lane & 3u) * 128u
                       : (const char*)x + (size_t)(128u * qm + 64u * (idx >> 4) + 16u * wid + (lane & 15u)) * (DM * 2) + 256u;
  c.sBk = mk_srd(bk, SEQ * DM * 2); c.sBv = mk_srd(bv, SEQ * DM * 2); c.sBs = mk_srd(shk, DM * 8); c.sWo = mk_srd(wo, NH * DH * DM * 2); c.b = b; c.hb = 16u * h + (idx & 15u);
  char* Qdst = Qf + (size_t)hq * (SEQ * DH * 2) + (4u * qm + 2u * wr) * 16384u + wc * 8192u;
  if (isV) k1_main<false>(c, lds, b, Wf, Qdst, pf2, c.pfstep, xT); else k1_main<true>(c, lds, b, Wf, Qdst, pf2, c.pfstep, xT);
}

constexpr float THR = 8.0f;
constexpr int NA = 17;
DI void mfma32_acc_a(f32x16& c, const bf16x8& a, const u32x4& b) { asm volatile("v_mfma_f32_32x32x16_bf16 %0, %1, %2, %0" : "+a"(c) : "v"(a), "v"(b)); }
DI void mfma32_acc_v(f32x16& c, const bf16x8& a, const bf16x8& b) { asm volatile("v_mfma_f32_32x32x16_bf16 %0, %1, %2, %0" : "+v"(c) : "v"(a), "a"(b)); }
DI void mfma32_init_v(f32x16& d, const bf16x8& a, const bf16x8& b, const f32x16& c) { asm volatile("v_mfma_f32_32x32x16_bf16 %0, %1, %2, %3" : "=&v"(d) : "v"(a), "a"(b), "v"(c)); }
DI float max3f(float a, float b, float c) { float r; asm("v_max3_f32 %0, %1, %2, %3" : "=v"(r) : "v"(a), "v"(b), "v"(c)); return r; }
DI float max2f(float a, float b) { float r; asm("v_max_f32_e32 %0, %1, %2" : "=v"(r) : "v"(a), "v"(b)); return r; }
DI float fadd_s(float a, float b) { float r; asm volatile("v_add_f32_e32 %0, %1, %2" : "=v"(r) : "v"(a), "v"(b)); return r; }
DI float fsub_s(float a, float b) { float r; asm("v_sub_f32_e32 %0, %1, %2" : "=v"(r) : "v"(a), "v"(b)); return r; }
constexpr int koff(int f) { return (f >> 1) * 2048 + (f & 1) * 512; }
constexpr int voff(int f) { return (f >> 4) * 16384 + (f & 7) * 2048 + ((f >> 3) & 1) * 512; }
struct K2C {
  unsigned ldsb, vlane;
  srd_t sK;
  lds_cptr kr, vr;
  unsigned kslot_w, vslot_w, vslot_r;
  unsigned ksoff, vsoff, dt;
  bf16x8 q[16]; f32x16 o[8]; f32x16 s0, s1, negm; u32x4 p[4]; bf16x8 fr[16];
  float m, l0, l1, mx0, mx1, fct, e14, e15; bool pend;
  int dq;
  bool domask;
};
template <int J> DI void k2_dma(unsigned& t, unsigned m0v, unsigned voff, srd_t srd, unsigned soff) {
  if constexpr ((J & 3) == 0) asm volatile("s_add_u32 m0, %1, %5\n\ts_add_u32 %0, %4, %5\n\tbuffer_load_dwordx4 %2, %3, %0 offen lds" : "=&s"(t) : "s"(m0v), "v"(voff), "s"(srd), "s"(soff), "n"((J >> 2) * 4096) : "memory", "scc");
  else asm volatile("buffer_load_dwordx4 %0, %1, %2 offen offset:%3 lds" :: "v"(voff), "s"(srd), "s"(t), "n"((J & 3) * 1024) : "memory");
}
DI void k2_dma8(unsigned m0v, unsigned voff, srd_t srd, unsigned soff) { unsigned t; sfor<8>([&](auto J) { k2_dma<J>(t, m0v, voff, srd, soff); }); }
DI void k2_mask(f32x16& s, int dq) {
#pragma unroll
  for (int r = 0; r < 16; ++r) if ((r & 3) + 8 * (r >> 2) > dq) s[r] = -__builtin_inff();
}
DI void mfma32_acc_a4(f32x16& c, const bf16x8& a, const u32x4& b, const bf16x8& x, const bf16x8& y, const bf16x8& z) { asm volatile("v_mfma_f32_32x32x16_bf16 %0, %1, %2, %0" : "+a"(c) : "v"(a), "v"(b), "v"(x), "v"(y), "v"(z)); }
DI void mfma32_acc_v4(f32x16& c, const bf16x8& a, const bf16x8& b, const bf16x8& x, const bf16x8& y, const bf16x8& z) { asm volatile("v_mfma_f32_32x32x16_bf16 %0, %1, %2, %0" : "+v"(c) : "v"(a), "a"(b), "v"(x), "v"(y), "v"(z)); }
DI void mfma32_init_v4(f32x16& d, const bf16x8& a, const bf16x8& b, const f32x16& c, const bf16x8& x, const bf16x8& y, const bf16x8& z) { asm volatile("v_mfma_f32_32x32x16_bf16 %0, %1, %2, %3" : "=&v"(d) : "v"(a), "a"(b), "v"(c), "v"(x), "v"(y), "v"(z)); }
DI void max4(float& m, float a, float b, float c, float d) { asm volatile("v_max3_f32 %0, %0, %1, %2\n\tv_max3_f32 %0, %0, %3, %4" : "+v"(m) : "v"(a), "v"(b), "v"(c), "v"(d)); }
DI void max6(float& m, float a, float b, float c, float d, float e, float f) { asm volatile("v_max3_f32 %0, %0, %1, %2\n\tv_max3_f32 %0, %0, %3, %4\n\tv_max3_f32 %0, %0, %5, %6" : "+v"(m) : "v"(a), "v"(b), "v"(c), "v"(d), "v"(e), "v"(f)); }
DI float max4i(float a, float b, float c, float d) { float m; asm volatile("v_max_f32_e32 %0, %1, %2\n\tv_max3_f32 %0, %0, %3, %4" : "=&v"(m) : "v"(a), "v"(b), "v"(c), "v"(d)); return m; }
template <int e> DI void k2_lsum(K2C& c, float v) { if constexpr (e & 1) c.l1 = fadd_s(c.l1, v); else c.l0 = fadd_s(c.l0, v); }
template <bool FIN, bool QK, int g> DI void k2_gapA(K2C& c) {
  if constexpr (QK) {
    if constexpr (g == 0) mfma32_init_v4(c.s0, c.fr[0], c.q[0], c.negm, c.fr[1], c.fr[2], c.fr[3]);
    else if constexpr (g == 16) mfma32_init_v4(c.s1, c.fr[0], c.q[0], c.negm, c.fr[1], c.fr[2], c.fr[3]);
    else if constexpr ((g & 3) == 0) mfma32_acc_v4(g < 16 ? c.s0 : c.s1, c.fr[g & 15], c.q[g & 15], c.fr[(g + 1) & 15], c.fr[(g + 2) & 15], c.fr[(g + 3) & 15]);
    else mfma32_acc_v(g < 16 ? c.s0 : c.s1, c.fr[g & 15], c.q[g & 15]);
    SBAR();
    if constexpr (g < 24) c.fr[(g + 8) & 15] = lds_rd128(c.kr + koff((g + 8)));
  }
  if constexpr (!QK && g <= 9) { asm volatile("s_nop 0"); SBAR(); }
  if constexpr (FIN && (QK ? g >= 24 : g < 8)) c.fr[g & 7] = lds_rd128(c.vr + voff((g & 7)));
  if constexpr (FIN && g <= 8) {
    if constexpr (g < 8) c.s1[8 + g] = __builtin_amdgcn_exp2f(c.s1[8 + g]);
    k2_lsum<g>(c, c.s1[7 + g]);
    if constexpr (g < 4) c.p[2][g] = cvtpk(c.s1[2 * g], c.s1[2 * g + 1]);
    else if constexpr (g < 7) c.p[3][g - 4] = cvtpk(c.s1[2 * g], c.s1[2 * g + 1]);
    else if constexpr (g == 8) c.p[3][3] = cvtpk(c.s1[14], c.s1[15]);
    if constexpr (g < 8) PINV(c.s1);
    if constexpr (g < 4) PINV(c.p[2]); else PINV(c.p[3]);
  }
  if constexpr (QK) {
    if constexpr (g >= 10 && g < 18) k2_dma<g - 10>(c.dt, c.ldsb + 65536u + c.vslot_w, c.vlane, c.sK, c.vsoff);
    if constexpr (g == 18) { if (c.domask) { SBAR(); k2_mask(c.s0, c.dq); } }
    if constexpr (g == 20) { c.mx0 = max4i(c.s0[0], c.s0[1], c.s0[2], c.s0[3]); }
    if constexpr (g >= 21 && g < 24) { constexpr int r = 4 * (g - 20); max4(c.mx0, c.s0[r], c.s0[r + 1], c.s0[r + 2], c.s0[r + 3]); }
  }
  SBAR();
}
template <bool FIN, bool QK, int... Gs> DI void k2_phaseA_(K2C& c, iseq<Gs...>) { (k2_gapA<FIN, QK, Gs>(c), ...); }
template <bool FIN, bool QK> DI void k2_phaseA(K2C& c) {
  if constexpr (QK && !FIN) {
#pragma unroll
    for (int i = 0; i < 8; ++i) c.fr[i] = lds_rd128(c.kr + koff(i));
  }
  k2_phaseA_<FIN, QK>(c, std::make_integer_sequence<int, 32>{});
}
template <bool PV, bool SM, int g> DI void k2_gapB(K2C& c) {
  if constexpr (SM && g == 24) {
    asm volatile("s_waitcnt vmcnt(8) lgkmcnt(0)\n\ts_barrier" ::: "memory");
    c.kr = (lds_cptr)(size_t)((unsigned)(size_t)c.kr ^ 32768u);
    SBAR();
  }
  if constexpr (PV) {
    if constexpr ((g & 3) == 0) mfma32_acc_a4(c.o[g & 7], c.fr[g & 15], c.p[g >> 3], c.fr[(g + 1) & 15], c.fr[(g + 2) & 15], c.fr[(g + 3) & 15]);
    else mfma32_acc_a(c.o[g & 7], c.fr[g & 15], c.p[g >> 3]);
    SBAR();
    if constexpr (g < 2) { c.fr[(8 + 2 * g) & 15] = lds_rd128(c.vr + voff((8 + 2 * g))); c.fr[(9 + 2 * g) & 15] = lds_rd128(c.vr + voff((9 + 2 * g))); }
    else if constexpr (g < 8) c.fr[(g + 10) & 15] = lds_rd128(c.vr + voff((g + 10)));
    else if constexpr (g < 10) { c.fr[(2 * g + 2) & 15] = lds_rd128(c.vr + voff((2 * g + 2))); c.fr[(2 * g + 3) & 15] = lds_rd128(c.vr + voff((2 * g + 3))); }
    else if constexpr (g < 20) c.fr[(g + 12) & 15] = lds_rd128(c.vr + voff((g + 12)));
  }
  if constexpr (!PV && g >= 7) { asm volatile("s_nop 0"); SBAR(); }
  if constexpr (SM) {
    if constexpr (g >= 24) { c.fr[(g + 8) & 15] = lds_rd128(c.kr + koff((g - 24))); k2_dma<g - 24>(c.dt, c.ldsb + c.kslot_w, c.vlane, c.sK, c.ksoff); }
    if constexpr (g == 2) { if (c.domask) { SBAR(); k2_mask(c.s1, c.dq - 32); } }
    if constexpr (g == 3) { max6(c.mx0, c.s1[0], c.s1[1], c.s1[2], c.s1[3], c.s1[4], c.s1[5]); }
    if constexpr (g == 4) { max6(c.mx0, c.s1[6], c.s1[7], c.s1[8], c.s1[9], c.s1[10], c.s1[11]); }
    if constexpr (g == 5) { max4(c.mx0, c.s1[12], c.s1[13], c.s1[14], c.s1[15]); c.mx1 = c.mx0; PINV(c.mx1); }
    if constexpr (g == 6) {
      if constexpr (!PV) asm volatile("s_nop 1");
      auto pr = __builtin_amdgcn_permlane32_swap(__float_as_uint(c.mx0), __float_as_uint(c.mx1), false, false);
      const float rm = max2f(__uint_as_float(pr[0]), __uint_as_float(pr[1]));
      if constexpr (!PV) {
        const float dl = max2f(rm, -1e30f); c.m = dl;
#pragma unroll
        for (int r = 0; r < 16; ++r) { c.s0[r] = fsub_s(c.s0[r], dl); c.s1[r] = fsub_s(c.s1[r], dl); c.negm[r] = -dl; }
        PINV(c.negm);
      } else if (__builtin_expect(__any(rm > THR), 0)) {
        const float dl = max2f(rm, 0.f); c.m = fadd_s(c.m, dl); c.fct = __builtin_amdgcn_exp2f(-dl);
#pragma unroll
        for (int r = 0; r < 16; ++r) { c.s0[r] = fsub_s(c.s0[r], dl); c.s1[r] = fsub_s(c.s1[r], dl); c.negm[r] = -c.m; }
        c.l0 *= c.fct; c.l1 *= c.fct; c.pend = true;
      }
    }
    if constexpr (g >= 7 && g < 23) c.s0[g - 7] = __builtin_amdgcn_exp2f(c.s0[g - 7]);
    if constexpr (g >= 8 && g < 24) k2_lsum<g>(c, c.s0[g - 8]);
    if constexpr (g >= 24) c.s1[g - 24] = __builtin_amdgcn_exp2f(c.s1[g - 24]);
    if constexpr (g >= 25) k2_lsum<g>(c, c.s1[g - 25]);
    if constexpr (g >= 16 && g < 24) { constexpr int w = g - 16; c.p[w >> 2][w & 3] = cvtpk(c.s0[2 * w], c.s0[2 * w + 1]); PINV(c.p[w >> 2]); }
    if constexpr (g >= 7 && g < 23) PINV(c.s0);
    if constexpr (g >= 24) PINV(c.s1);
  }
  SBAR();
}
template <bool PV, bool SM, int... Gs> DI void k2_phaseB_(K2C& c, iseq<Gs...>) { (k2_gapB<PV, SM, Gs>(c), ...); }
template <bool PV, bool SM> DI void k2_phaseB(K2C& c) {
  if constexpr (!PV) { MFMA_DRAIN(); SBAR(); }
  k2_phaseB_<PV, SM>(c, std::make_integer_sequence<int, 32>{});
  if constexpr (PV && SM) {
    if (__builtin_expect(c.pend, 0)) {
      MFMA_DRAIN(); SBAR();
#pragma unroll
      for (int d = 0; d < 8; ++d) { asm volatile("" : "+a"(c.o[d])); c.o[d] *= c.fct; asm volatile("" : "+a"(c.o[d])); SBAR(); }
      c.pend = false;
    }
    SBAR();
  }
}

DI void mfma32_z4(f32x16& d, const bf16x8& a, const u32x4& b, const bf16x8& x, const bf16x8& y, const bf16x8& z) { asm volatile("v_mfma_f32_32x32x16_bf16 %0, %1, %2, 0" : "=&v"(d) : "v"(a), "v"(b), "v"(x), "v"(y), "v"(z)); }
DI void mfma32_vv4(f32x16& c, const bf16x8& a, const u32x4& b, const bf16x8& x, const bf16x8& y, const bf16x8& z) { asm volatile("v_mfma_f32_32x32x16_bf16 %0, %1, %2, %0" : "+v"(c) : "v"(a), "v"(b), "v"(x), "v"(y), "v"(z)); }
DI void mfma32_vv(f32x16& c, const bf16x8& a, const u32x4& b) { asm volatile("v_mfma_f32_32x32x16_bf16 %0, %1, %2, %0" : "+v"(c) : "v"(a), "v"(b)); }
struct K2E { u32x4 cx[16]; lds_cptr rc, rn; unsigned slotw, wsoff; srd_t sP; u32x4 pk; };
template <int CH, int g> DI void k2_gapC(K2C& c, K2E& e) {
  if constexpr (CH == 3 && g == 24) { asm volatile("s_waitcnt vmcnt(6)\n\ts_barrier" ::: "memory"); SBAR(); }
  if constexpr ((g & 15) == 0) mfma32_z4(g < 16 ? c.s0 : c.s1, c.fr[0], e.cx[0], c.fr[1], c.fr[2], c.fr[3]);
  else if constexpr ((g & 3) == 0) mfma32_vv4(g < 16 ? c.s0 : c.s1, c.fr[g & 15], e.cx[g & 15], c.fr[(g + 1) & 15], c.fr[(g + 2) & 15], c.fr[(g + 3) & 15]);
  else mfma32_vv(g < 16 ? c.s0 : c.s1, c.fr[g & 15], e.cx[g & 15]);
  SBAR();
  if constexpr (CH < 2 && g == 28) { asm volatile("s_barrier" ::: "memory"); SBAR(); }
  if constexpr (g < 24) c.fr[(g + 8) & 15] = lds_rd128(e.rc + (g + 8) * 1024);
  else if constexpr (CH < 6) c.fr[(g + 8) & 15] = lds_rd128(e.rn + (g - 24) * 1024);
  if constexpr (CH < 3 && g >= 8 && g < 16) k2_dma<g - 8>(c.dt, c.ldsb + e.slotw, c.vlane, c.sK, e.wsoff);
  constexpr int u = (g & 15) - 2;
  if constexpr (u >= 0 && u < 6 && (g >= 16 || CH > 0)) {
    const f32x16& t = g < 16 ? c.s1 : c.s0; constexpr int n = 2 * CH + (g < 16 ? -1 : 0), a = u / 3, i = u % 3;
    if constexpr (i < 2) { e.pk[2 * i] = cvtpk(t[8 * a + 4 * i], t[8 * a + 4 * i + 1]); e.pk[2 * i + 1] = cvtpk(t[8 * a + 4 * i + 2], t[8 * a + 4 * i + 3]); PINV(e.pk); }
    else st16w<(2 * n + a) * 1024>(e.pk, e.sP, c.vlane, 0u);
  }
  SBAR();
}
template <int CH, int... Gs> DI void k2_phaseC_(K2C& c, K2E& e, iseq<Gs...>) { (k2_gapC<CH, Gs>(c, e), ...); }
template <int CH> DI void k2_phaseC(K2C& c, K2E& e) { k2_phaseC_<CH>(c, e, std::make_integer_sequence<int, 32>{}); }

DI void phase_attn(const Params& P, char* lds, unsigned long long tag, const unsigned wid) {
  const char *Qf = P.ws, *KVW = P.ws + WS_K; char *Pf = P.ws + WS_PF, *part = P.ws + WS_PART; gu64* flags = (gu64*)(P.ws + WS_FL);
  const unsigned lane = lane_id(), hi = lane >> 5, r32 = lane & 31u;
  const unsigned b = blockIdx.x, xcd = b & 7u, idx = b >> 3, h = 2u * xcd + (idx >> 4), pi = (idx >> 1) & 7u, roleB = idx & 1u;
  const unsigned pair = h * 8u + pi;
  const unsigned klane = ((r32 >> 3) & 1u) * 1024u + hi * 256u + ((r32 & 7u) + 8u * (r32 >> 4)) * 16u, vlane_r = (r32 >> 4) * 1024u + hi * 256u + (r32 & 15u) * 16u;
  const int n1 = roleB ? 32 - 2 * (int)pi - NA : NA, tb1 = roleB ? NA : 0, ntot = roleB ? 34 - NA : NA;
  const unsigned wbase = 2u * MB16 + h * (DH * DM * 2) + wid * 8192u;
  auto tile_off = [&](int p) -> unsigned { return h * (SEQ * DH * 2) + (unsigned)(p < n1 ? tb1 + p : p - n1) * 32768u + wid * 8192u; };
  auto k_off = [&](int p) -> unsigned { return p >= ntot ? wbase + (unsigned)(p - ntot) * 32768u : tile_off(p); };
  auto v_off = [&](int p) -> unsigned { return p >= ntot ? wbase + 2u * 32768u : MB16 + tile_off(p); };
  K2C c;
  c.ldsb = (unsigned)(size_t)lds + wid * 8192u; c.vlane = lane * 16u;
  c.sK = mk_srd(KVW, 2u * MB16 + NH * DH * DM * 2);
  unsigned qb = 15u - pi;
  { const char* qp = Qf + ((size_t)h * 64u + qb * 4u + wid) * 16384u + klane;
#pragma unroll
    for (int i = 0; i < 16; ++i) c.q[i] = *(const bf16x8*)(qp + koff(i)); }
  SBAR();
  k2_dma8(c.ldsb, c.vlane, c.sK, k_off(0)); k2_dma8(c.ldsb + 32768u, c.vlane, c.sK, k_off(1)); k2_dma8(c.ldsb + 65536u, c.vlane, c.sK, v_off(0));
  c.kr = (lds_cptr)lds + klane; c.kslot_w = 0u; c.vslot_w = 32768u; c.vslot_r = 0u;
  asm volatile("s_waitcnt vmcnt(16)\n\ts_barrier" ::: "memory");
  int p = 0;
  const int nseg = roleB ? 2 : 1;
  for (int seg = 0; seg < nseg; ++seg) {
    const int pend_ = seg == 0 ? n1 : ntot;
    int t = seg == 0 ? tb1 : 0;
    const int row0 = (int)(qb * 128u + wid * 32u);
    auto setup = [&]() {
      c.vsoff = v_off(p + 1); c.ksoff = k_off(p + 2);
      c.domask = 64 * t + 63 > row0; c.dq = row0 + (int)r32 - 64 * t - 4 * (int)hi;
    };
    auto advance = [&]() {
      c.kslot_w ^= 32768u; c.vslot_w = c.vslot_w == 65536u ? 0u : c.vslot_w + 32768u; ++p; ++t;
      c.vr = (lds_cptr)lds + 65536u + c.vslot_r + vlane_r; c.vslot_r = c.vslot_r == 65536u ? 0u : c.vslot_r + 32768u;
    };
#pragma unroll
    for (int d = 0; d < 8; ++d) { c.o[d] = f32x16{}; asm volatile("" : "+a"(c.o[d])); }
    c.negm = f32x16{}; PINV(c.negm); c.l0 = 0.f; c.l1 = 0.f; c.pend = false; c.fct = 1.f;
#pragma unroll
    for (int i = 0; i < 16; ++i) asm volatile("" : "+a"(c.q[i]));
    asm volatile("s_nop 1"); SBAR();
    setup();
    k2_phaseA<false, true>(c);
    if (seg == 1) asm volatile("s_waitcnt vmcnt(0)" ::: "memory");
    k2_phaseB<false, true>(c);
    if (seg == 1 && wid == 0 && lane == 0) __hip_atomic_store(flags + FL_S2 + pair * 16u, tag, RLX_AGENT);
    advance();
    while (p < pend_) {
      setup();
      k2_phaseA<true, true>(c); k2_phaseB<true, true>(c);
      advance();
    }
    if (seg + 1 < nseg) {
      const char* qp = Qf + ((size_t)h * 64u + pi * 4u + wid) * 16384u + klane;
#pragma unroll
      for (int i = 0; i < 16; ++i) c.q[i] = *(const bf16x8*)(qp + koff(i));
    } else k2_dma8(c.ldsb + 65536u + c.vslot_w, c.vlane, c.sK, wbase + 3u * 32768u);
    SBAR();
    k2_phaseA<true, false>(c); k2_phaseB<true, false>(c);
    MFMA_DRAIN(); SBAR();
#pragma unroll
    for (int d = 0; d < 8; ++d) asm volatile("" : "+a"(c.o[d]));
    float l = c.l0 + c.l1;
    { auto pr = __builtin_amdgcn_permlane32_swap(__float_as_uint(l), __float_as_uint(l), false, false); l = __uint_as_float(pr[0]) + __uint_as_float(pr[1]); }
    const srd_t sP = mk_srd(part + ((size_t)pair * 4u + wid) * 33792u, 33792u);
    if (roleB && seg == 0) {
      sfor<32>([&](auto I) { constexpr int d = I >> 2, k = I & 3;
        f32x4 v = {c.o[d][4 * k], c.o[d][4 * k + 1], c.o[d][4 * k + 2], c.o[d][4 * k + 3]};
        st16wa<I * 1024>(v, sP, c.vlane, 0u); });
      f32x4 v = {c.m, l, 0.f, 0.f};
      st16w<32 * 1024>(__builtin_bit_cast(u32x4, v), sP, c.vlane, 0u);
    } else {
      K2E e;
      if (roleB) {
        const float rl = 1.0f / l;
#pragma unroll
        for (int d = 0; d < 8; ++d) {
#pragma unroll
          for (int s = 0; s < 2; ++s) { e.cx[2 * d + s] = pack8w(c.o[d], s, rl); PINV(e.cx[2 * d + s]); }
          SBAR();
        }
      } else {
        if (wid == 0) wait_flags(flags + FL_S2 + pair * 16u, tag);
        asm volatile("s_waitcnt vmcnt(0)\n\ts_barrier" ::: "memory");
        const f32x4 ml = __builtin_bit_cast(f32x4, __builtin_amdgcn_raw_buffer_load_b128(sP, c.vlane, 32 * 1024, 16));
        f32x4 ob[32];
#pragma unroll
        for (int k = 0; k < 32; ++k) ob[k] = __builtin_bit_cast(f32x4, __builtin_amdgcn_raw_buffer_load_b128(sP, c.vlane, k * 1024, 16));
        SBAR();
        const float mm = fmaxf(c.m, ml[0]), wa = __builtin_amdgcn_exp2f(c.m - mm), wb = __builtin_amdgcn_exp2f(ml[0] - mm);
        const float rl = 1.0f / (wa * l + wb * ml[1]), fa = wa * rl, fb = wb * rl;
#pragma unroll
        for (int d = 0; d < 8; ++d) {
#pragma unroll
          for (int s = 0; s < 2; ++s) {
#pragma unroll
            for (int i = 0; i < 4; ++i) { const int r = 8 * s + 2 * i;
              e.cx[2 * d + s][i] = cvtpk(c.o[d][r] * fa + ob[4 * d + (r >> 2)][r & 3] * fb, c.o[d][r + 1] * fa + ob[4 * d + ((r + 1) >> 2)][(r + 1) & 3] * fb); }
            PINV(e.cx[2 * d + s]);
          }
          SBAR();
        }
      }
      e.sP = mk_srd(Pf + ((size_t)h * 64u + qb * 4u + wid) * 28672u, 28672u);
      const unsigned kA = c.kslot_w, kB = c.kslot_w ^ 32768u;
      const unsigned v2 = 65536u + c.vslot_r, v3 = 65536u + c.vslot_w, v4 = 65536u + (98304u - c.vslot_r - c.vslot_w);
      const lds_cptr lb = (lds_cptr)lds + lane * 16u;
      asm volatile("s_waitcnt vmcnt(0)\n\ts_barrier" ::: "memory");
      e.rc = lb + kA;
#pragma unroll
      for (int i = 0; i < 8; ++i) c.fr[i] = lds_rd128(e.rc + i * 1024);
      e.rn = lb + kB; e.slotw = v4; e.wsoff = wbase + 4u * 32768u; SBAR(); k2_phaseC<0>(c, e);
      e.rc = e.rn; e.rn = lb + v2; e.slotw = kA; e.wsoff = wbase + 5u * 32768u; SBAR(); k2_phaseC<1>(c, e);
      e.rc = e.rn; e.rn = lb + v3; e.slotw = kB; e.wsoff = wbase + 6u * 32768u; SBAR(); k2_phaseC<2>(c, e);
      e.rc = e.rn; e.rn = lb + v4; SBAR(); k2_phaseC<3>(c, e);
      e.rc = e.rn; e.rn = lb + kA; SBAR(); k2_phaseC<4>(c, e);
      e.rc = e.rn; e.rn = lb + kB; SBAR(); k2_phaseC<5>(c, e);
      e.rc = e.rn; SBAR(); k2_phaseC<6>(c, e);
      MFMA_DRAIN(); PINV(c.s1); SBAR();
      st16w<26 * 1024>(pack8w(c.s1, 0, 1.0f), e.sP, c.vlane, 0u); st16w<27 * 1024>(pack8w(c.s1, 1, 1.0f), e.sP, c.vlane, 0u);
    }
    qb = pi;
  }
}
constexpr int OUTW = 2 * DM;
DI void reduce_task(unsigned task, srd_t sP, unsigned lane, unsigned short* out) {
  const unsigned rb = task / 28u, f = task % 28u;
  u32x4 v[16];
#pragma unroll
  for (int h = 0; h < 16; ++h) v[h] = __builtin_amdgcn_raw_buffer_load_b128(sP, lane * 16u, task * 1024u + h * (64u * 28672u), 16);
  SBAR();
  float s[8];
#pragma unroll
  for (int i = 0; i < 8; ++i) {
    float a[16];
#pragma unroll
    for (int h = 0; h < 16; ++h) a[h] = (i & 1) ? __uint_as_float(v[h][i >> 1] & 0xffff0000u) : __uint_as_float(v[h][i >> 1] << 16);
    s[i] = (((a[0] + a[1]) + (a[2] + a[3])) + ((a[4] + a[5]) + (a[6] + a[7]))) + (((a[8] + a[9]) + (a[10] + a[11])) + ((a[12] + a[13]) + (a[14] + a[15])));
  }
  *(u32x4*)(out + (size_t)(32u * rb + (lane & 31u)) * OUTW + 16u * f + 8u * (lane >> 5)) = (u32x4){cvtpk(s[0], s[1]), cvtpk(s[2], s[3]), cvtpk(s[4], s[5]), cvtpk(s[6], s[7])};
}

constexpr int HCOPY = 4160, HCH = 4 * HCOPY, XROW = 80, XCH = 96 * XROW;
constexpr int XOFF = 2 * HCH;
struct K3S { lds_cptr ha, xa; f32x16 y0, y1; s16x4 al[4][2], ah[4][2]; bf16x8 bx[4][2]; };
template <int e> DI void k3_ssm_rd(K3S& c) {
  constexpr int s = e & 3;
  c.al[s][0] = *(const volatile LAS s16x4*)(c.ha + e * 64); c.ah[s][0] = *(const volatile LAS s16x4*)(c.ha + e * 64 + 8);
  c.bx[s][0] = lds_rd128(c.xa + (63 - e) * XROW); c.bx[s][1] = lds_rd128(c.xa + (31 - e) * XROW);
  c.al[s][1] = *(const volatile LAS s16x4*)(c.ha + (e + 32) * 64); c.ah[s][1] = *(const volatile LAS s16x4*)(c.ha + (e + 32) * 64 + 8);
}
template <int e> DI void k3_ssm_step(K3S& c) {
  constexpr int s = e & 3;
  if constexpr (e + 2 < 32) k3_ssm_rd<e + 2>(c);
  SBAR();
  const bf16x8 a0 = cat(c.al[s][0], c.ah[s][0]), a1 = cat(c.al[s][1], c.ah[s][1]);
  c.y1 = MFMA32(a0, c.bx[s][0], c.y1); c.y0 = MFMA32(a0, c.bx[s][1], c.y0); c.y1 = MFMA32(a1, c.bx[s][1], c.y1);
  SBAR();
}
template <int... Es> DI void k3_ssm_loop(K3S& c, iseq<Es...>) { (k3_ssm_step<Es>(c), ...); }
DI void phase_ssm(const Params& P, char* lds, unsigned blk, unsigned long long tag, const unsigned wid) {
  const unsigned lane = lane_id(), tid = wid * 64u + lane, hi = lane >> 5, r = lane & 31u;
  const unsigned c0 = 2u * (28u * (blk & 7u) + (blk >> 3));
  u32x4 xw[2], hA[2]; u32x2 hB[2];
#pragma unroll
  for (int ch = 0; ch < 2; ++ch) {
    const srd_t sh = mk_srd(P.ssmk + (size_t)(c0 + ch) * SEQ, SEQ * 2);
    hA[ch] = __builtin_amdgcn_raw_buffer_load_b128(sh, tid * 16u, 0, 0); hB[ch] = __builtin_amdgcn_raw_buffer_load_b64(sh, tid * 16u + 16u, 0, 0);
  }
  if (wid == 0) wait_flags((gu64*)(P.ws + WS_FL) + FL_S1 + (7u * r + (c0 >> 6)) * 16u, tag);
  asm volatile("s_barrier" ::: "memory");
  const srd_t sx = mk_srd(P.ws + WS_XT, SEQ * DM * 2);
#pragma unroll
  for (int ch = 0; ch < 2; ++ch) xw[ch] = __builtin_amdgcn_raw_buffer_load_b128(sx, (((tid >> 3) * DM + c0 + ch) * 64u + 8u * (tid & 7u)) * 2u, 0, 16);
  const unsigned dw = *(const unsigned*)(P.ssmd + c0);
  SBAR();
#pragma unroll
  for (int ch = 0; ch < 2; ++ch) {
    const unsigned w[6] = {hA[ch][0], hA[ch][1], hA[ch][2], hA[ch][3], hB[ch][0], hB[ch][1]};
    unsigned o[5];
#pragma unroll
    for (int k = 0; k < 5; ++k) o[k] = __builtin_amdgcn_alignbyte(w[k + 1], w[k], 2);
    LAS char* dst = (LAS char*)lds + ch * HCH + tid * 16u + 64u;
    *(LAS u32x4*)(dst) = (u32x4){o[0], o[1], o[2], o[3]};
    *(LAS u32x4*)(dst + HCOPY) = (u32x4){w[1], w[2], w[3], w[4]};
    *(LAS u32x4*)(dst + 2 * HCOPY) = (u32x4){o[1], o[2], o[3], o[4]};
    *(LAS u32x4*)(dst + 3 * HCOPY) = (u32x4){w[2], w[3], w[4], w[5]};
    if (tid < 4u) {
      const unsigned z0 = tid ? 0u : w[0], z1 = tid ? 0u : w[1], zo = tid ? 0u : o[0];
      LAS char* d2 = (LAS char*)lds + ch * HCH + (3u - tid) * 16u;
      *(LAS u32x4*)(d2) = (u32x4){0u, 0u, 0u, z0 << 16};
      *(LAS u32x4*)(d2 + HCOPY) = (u32x4){0u, 0u, 0u, z0};
      *(LAS u32x4*)(d2 + 2 * HCOPY) = (u32x4){0u, 0u, z0 << 16, zo};
      *(LAS u32x4*)(d2 + 3 * HCOPY) = (u32x4){0u, 0u, z0, z1};
    }
    LAS char* xb = (LAS char*)lds + XOFF + ch * XCH;
    if (tid < 160u) *(LAS u32x4*)(xb + tid * 16u) = (u32x4){0u, 0u, 0u, 0u};
  }
#pragma unroll
  for (int ch = 0; ch < 2; ++ch) *(LAS u32x4*)((LAS char*)lds + XOFF + ch * XCH + (32u + (tid >> 2)) * XROW + (tid & 3u) * 16u) = xw[ch];
  SBAR();
  unsigned qw[8];
#pragma unroll
  for (int i = 0; i < 8; ++i) qw[i] = *(const unsigned*)(P.bq + (size_t)(tid + 256u * i) * DM + c0);
  asm volatile("s_waitcnt lgkmcnt(0)\n\ts_barrier" ::: "memory");
  const unsigned ch = wid >> 1, dh = wid & 1u;
  K3S c;
  c.ha = (lds_cptr)lds + ch * HCH + (r & 3u) * HCOPY + (24u - 16u * dh - 8u * hi + 4u * (r >> 2)) * 2u;
  c.xa = (lds_cptr)lds + XOFF + ch * XCH + (r + 1u) * XROW + (2u * dh + hi) * 16u;
  c.y0 = f32x16{}; c.y1 = f32x16{};
  k3_ssm_rd<0>(c); k3_ssm_rd<1>(c);
  k3_ssm_loop(c, mkseq<32>{});
  const f32x16 y0 = c.y0, y1 = c.y1;
  LAS char* ys = (LAS char*)lds + 65536u + wid * 8192u;
#pragma unroll
  for (int k = 0; k < 4; ++k) {
    const unsigned o = r * 128u + (((2u * k + hi) ^ (r & 7u)) << 4);
    *(LAS f32x4*)(ys + o) = (f32x4){y0[4 * k], y0[4 * k + 1], y0[4 * k + 2], y0[4 * k + 3]};
    *(LAS f32x4*)(ys + 4096u + o) = (f32x4){y1[4 * k], y1[4 * k + 1], y1[4 * k + 2], y1[4 * k + 3]};
  }
  asm volatile("s_waitcnt lgkmcnt(0)\n\ts_barrier" ::: "memory");
#pragma unroll
  for (int i = 0; i < 8; ++i) {
    const unsigned t = tid + 256u * i, row = t >> 5, bb = t & 31u;
    const unsigned yo = row * 128u + (((bb >> 2) ^ (row & 7u)) << 4) + (bb & 3u) * 4u, xo = (32u + row) * XROW + (bb & 24u) * 2u + (7u - (bb & 7u)) * 2u;
    unsigned res = 0;
#pragma unroll
    for (int cc = 0; cc < 2; ++cc) {
      const LAS char* yp = (const LAS char*)lds + 65536 + cc * 16384 + yo;
      const float y = *(const LAS float*)yp + *(const LAS float*)(yp + 8192);
      const float kv = bf2f(*(const LAS unsigned short*)((LAS char*)lds + XOFF + cc * XCH + xo));
      const float z = rbf(rbf(y) + rbf(kv * bf2f(cc ? dw >> 16 : dw & 0xffffu)));
      res |= (cvtpk(bf2f(cc ? qw[i] >> 16 : qw[i] & 0xffffu) * z, 0.f) & 0xffffu) << (16 * cc);
    }
    *(unsigned*)(P.out + (size_t)t * OUTW + DM + c0) = res;
  }
}

__global__ __launch_bounds__(256, 1) void k1_fused(Params P) {
  extern __shared__ __attribute__((aligned(16))) char lds[];
  const unsigned wid = rfl(threadIdx.x >> 6);
  const unsigned b = blockIdx.x, xcd = b & 7u, idx = b >> 3, h = 2u * xcd + (idx >> 4), j = idx & 15u;
  gu64* flags = (gu64*)(P.ws + WS_FL);
  phase_proj(P, lds, wid);
  const unsigned long long e0 = __hip_atomic_load(flags + FL_EPOCH, RLX_AGENT);
  unsigned long long tag = (e0 ^ 0x9E3779B97F4A7C15ull) * 0xD1342543DE82EF95ull + 1ull;
  tag = ((unsigned long long)rfl((unsigned)(tag >> 32)) << 32) | rfl((unsigned)tag);
  publish(flags + FL_S1 + b * 16u, tag, wid);
  if (wid == 0) {
    wait_flags(flags + FL_S1 + (xcd + 8u * ((idx & 16u) + (lane_id() & 15u))) * 16u, tag);
    __builtin_amdgcn_fence(__ATOMIC_ACQUIRE, "agent");
    asm volatile("s_waitcnt vmcnt(0)" ::: "memory");
  }
  asm volatile("s_barrier" ::: "memory");
  phase_attn(P, lds, tag, wid);
  publish(flags + FL_S3 + b * 16u, tag, wid);
  if (b < 224u) phase_ssm(P, lds, b, tag, wid);
  const unsigned lane = lane_id();
  if (wid == 0) wait_flags(flags + FL_S3 + ((lane & 7u) + 8u * (16u * ((lane >> 3) & 1u) + j)) * 16u, tag);
  asm volatile("s_barrier" ::: "memory");
  if (b == 0 && wid == 0 && lane == 0) __hip_atomic_store(flags + FL_EPOCH, tag, RLX_AGENT);
  const unsigned qb = (j & 1u) ? (j >> 1) : 15u - (j >> 1);
  const srd_t sP = mk_srd(P.ws + WS_PF, NH * 64u * 28672u);
  reduce_task(qb * 112u + h * 7u + wid, sP, lane, P.out);
  if (wid < 3u) reduce_task(qb * 112u + h * 7u + 4u + wid, sP, lane, P.out);
}

extern "C" void kernel_launch(void* const* d_in, const int* in_sizes, int n_in, void* d_out, int out_size, void* d_ws, size_t ws_size, hipStream_t stream) {
  typedef const unsigned short* cu16;
  Params P = {(cu16)d_in[0], (cu16)d_in[1], (cu16)d_in[2], (cu16)d_in[3], (cu16)d_in[4], (cu16)d_in[5], (cu16)d_in[6], (cu16)d_in[7], (cu16)d_in[8], (cu16)d_in[9], (cu16)d_in[10], (cu16)d_in[11],
              (unsigned short*)d_out, (char*)d_ws};
  (void)hipFuncSetAttribute((const void*)k1_fused, hipFuncAttributeMaxDynamicSharedMemorySize, 163840);
  k1_fused<<<256, 256, 163840, stream>>>(P);
}
```

```cpp
#include <hip/hip_runtime.h>
#include <hip/hip_bf16.h>
#include <utility>

typedef short    bf16x8 __attribute__((ext_vector_type(8)));
typedef short    s16x4  __attribute__((ext_vector_type(4)));
typedef float    f32x16 __attribute__((ext_vector_type(16)));
typedef float    f32x4  __attribute__((ext_vector_type(4)));
typedef float    f32x2  __attribute__((ext_vector_type(2)));
typedef __bf16   bf16x2 __attribute__((ext_vector_type(2)));
typedef unsigned u32x4  __attribute__((ext_vector_type(4)));
typedef unsigned u32x2  __attribute__((ext_vector_type(2)));
#define DI __device__ __forceinline__
#define AI __attribute__((always_inline))
#define LAS __attribute__((address_space(3)))
#define SBAR() __builtin_amdgcn_sched_barrier(0)
template <class T> DI void PINV(T& x) { asm volatile("" : "+v"(x)); }
template <class T> DI void PINA(T& x) { asm volatile("" : "+a"(x)); }
#define MFMA32(a, b, c) __builtin_amdgcn_mfma_f32_32x32x16_bf16((a), (b), (c), 0, 0, 0)
#define MFMA_DRAIN() asm volatile("s_nop 7\n\ts_nop 7" ::: "memory")
typedef LAS const char* lds_cptr;
typedef __amdgpu_buffer_rsrc_t srd_t;
template <int... Is> using iseq = std::integer_sequence<int, Is...>;
template <int N> using mkseq = std::make_integer_sequence<int, N>;

DI unsigned rfl(unsigned x) { return __builtin_amdgcn_readfirstlane(x); }
DI unsigned lane_id() { unsigned l; asm volatile("v_mbcnt_lo_u32_b32 %0, -1, 0\n\tv_mbcnt_hi_u32_b32 %0, -1, %0" : "=v"(l)); return l; }
DI unsigned cvtpk(float lo, float hi) { f32x2 v = {lo, hi}; bf16x2 b = __builtin_convertvector(v, bf16x2); return __builtin_bit_cast(unsigned, b); }
DI float bf2f(unsigned h) { return __uint_as_float(h << 16); }
DI float rbf(float x) { return __uint_as_float(cvtpk(x, x) << 16); }
DI bf16x8 lds_rd128(lds_cptr p) { return *(const LAS bf16x8*)p; }
DI s16x4 lds_tr(lds_cptr p) { return __builtin_amdgcn_ds_read_tr16_b64_v4i16((LAS s16x4*)p); }
DI bf16x8 cat(s16x4 l, s16x4 h) { return __builtin_shufflevector(l, h, 0, 1, 2, 3, 4, 5, 6, 7); }
DI srd_t mk_srd(const void* p, unsigned bytes) { return __builtin_amdgcn_make_buffer_rsrc((void*)p, (short)0, (int)bytes, 0x00020000); }
template <unsigned MI, unsigned SI> DI void dma16(unsigned mb, unsigned voff, srd_t srd, unsigned sb) {
  unsigned t;
  asm volatile("s_add_u32 m0, %1, %5\n\ts_add_u32 %0, %4, %6\n\tbuffer_load_dwordx4 %2, %3, %0 offen lds" : "=&s"(t) : "s"(mb), "v"(voff), "s"(srd), "s"(sb), "n"(MI), "n"(SI) : "memory", "scc");
}
template <unsigned SI> DI void st16w(u32x4 v, srd_t srd, unsigned voff, unsigned sb) {
  unsigned t; asm volatile("s_add_u32 %0, %4, %5\n\tbuffer_store_dwordx4 %1, %2, %3, %0 offen sc1\n\ts_nop 1" : "=&s"(t) : "v"(v), "v"(voff), "s"(srd), "s"(sb), "n"(SI) : "memory", "scc"); }
template <unsigned SI> DI void st16wa(f32x4 v, srd_t srd, unsigned voff, unsigned sb) {
  unsigned t; asm volatile("s_add_u32 %0, %4, %5\n\tbuffer_store_dwordx4 %1, %2, %3, %0 offen sc1\n\ts_nop 1" : "=&s"(t) : "a"(v), "v"(voff), "s"(srd), "s"(sb), "n"(SI) : "memory", "scc"); }
DI void ld4(unsigned& d, srd_t srd, unsigned voff) { asm volatile("buffer_load_dword %0, %1, %2, 0 offen" : "=v"(d) : "v"(voff), "s"(srd) : "memory"); }
template <unsigned SI> DI void ld16a(bf16x8& d, srd_t srd, unsigned voff) { unsigned t; asm volatile("s_mov_b32 %1, %4\n\tbuffer_load_dwordx4 %0, %2, %3, %1 offen sc1" : "=a"(d), "=&s"(t) : "v"(voff), "s"(srd), "n"(SI) : "memory"); }
template <int N, class F> DI void sfor_(F&& f, iseq<>) {}
template <int N, class F, int I, int... Is> DI void sfor_(F&& f, iseq<I, Is...>) { f(std::integral_constant<int, I>{}); sfor_<N>(f, iseq<Is...>{}); }
template <int N, class F> DI void sfor(F&& f) { sfor_<N>(f, mkseq<N>{}); }
DI u32x4 pack8(const f32x4& a, const f32x4& b, float sc) { u32x4 w = {cvtpk(a[0] * sc, a[1] * sc), cvtpk(a[2] * sc, a[3] * sc), cvtpk(b[0] * sc, b[1] * sc), cvtpk(b[2] * sc, b[3] * sc)}; return w; }
DI u32x4 pack8w(const f32x16& a, int s, float sc) {
  u32x4 w = {cvtpk(a[8 * s] * sc, a[8 * s + 1] * sc), cvtpk(a[8 * s + 2] * sc, a[8 * s + 3] * sc), cvtpk(a[8 * s + 4] * sc, a[8 * s + 5] * sc), cvtpk(a[8 * s + 6] * sc, a[8 * s + 7] * sc)};
  return w;
}
constexpr int SEQ = 2048, NH = 16, DH = 256, DM = 448, DC = 1024;
constexpr unsigned WRS = NH * DH * 2;
constexpr unsigned MB16 = SEQ * NH * DH * 2;
constexpr float QSCALE = 1.4426950408889634f / 16.0f;

typedef __attribute__((address_space(1))) unsigned long long gu64;
#define RLX_AGENT __ATOMIC_RELAXED, __HIP_MEMORY_SCOPE_AGENT
struct Params {
  const unsigned short *x, *ckv, *wq, *wuk, *wuv, *wo, *bq, *bk, *bv, *shk, *ssmk, *ssmd;
  unsigned short* out; char* ws;
};
constexpr size_t WS_K = MB16, WS_W = 3 * (size_t)MB16, WS_XT = WS_W + (4u << 20), WS_ZT = WS_XT + (2u << 20), WS_FL = WS_ZT + (2u << 20), WS_PART = WS_FL + (1u << 20), WS_PF = WS_PART + (17u << 20);
constexpr int FL_EPOCH = 0, FL_S0 = 16, FL_S1 = FL_S0 + 256 * 16, FL_S2 = FL_S1 + 256 * 16, FL_S3 = FL_S2 + 128 * 16;
DI void publish(gu64* flag, unsigned long long tag, unsigned wid) {
  asm volatile("s_waitcnt vmcnt(0)\n\ts_barrier" ::: "memory");
  if (wid == 0 && lane_id() == 0) __hip_atomic_store(flag, tag, RLX_AGENT);
}
DI void wait_flags(gu64* lane_flag, unsigned long long tag) {
  for (int spin = 0; spin < (1 << 20); ++spin) {
    if (__all(__hip_atomic_load(lane_flag, RLX_AGENT) == tag)) break;
    __builtin_amdgcn_s_sleep(1);
  }
}

DI void mfma16a(f32x4& c, const bf16x8& a, const bf16x8& b) { asm volatile("v_mfma_f32_16x16x32_bf16 %0, %1, %2, %0" : "+a"(c) : "v"(a), "v"(b)); }
DI void mfma32q(f32x16& c, const bf16x8& a, const bf16x8& b) { asm volatile("v_mfma_f32_32x32x16_bf16 %0, %1, %2, %0" : "+v"(c) : "v"(a), "v"(b)); }
DI void mfma32qz(f32x16& c, const bf16x8& a, const bf16x8& b) { asm volatile("v_mfma_f32_32x32x16_bf16 %0, %1, %2, 0" : "=&v"(c) : "v"(a), "v"(b)); }
DI f32x2 up2(unsigned w) { f32x2 r = {__uint_as_float(w << 16), __uint_as_float(w & 0xffff0000u)}; return r; }
DI f32x2 rb2(f32x2 v) { return up2(cvtpk(v[0], v[1])); }
constexpr unsigned qxs(int s) { return 16384u * s; }
constexpr unsigned qws(int s) { return s == 0 ? 65536u : s == 1 ? 131072u : 98304u; }
constexpr unsigned WO_LDS = 49152u;
struct SsmS { u32x4 xw[2], hA[2]; u32x2 hB[2], f0; unsigned dw, c0; f32x16 y0, y1; f32x4 late[12]; srd_t sO; unsigned so, sA, sB; };
struct Frags { bf16x8 x[8]; s16x4 wl[8], wh[8]; };
struct K1C {
  unsigned ldsb, lds8, lds4, wid, vlane, b, hb;
  unsigned vx_kv[2], vx_q[2], vw[4], vwq[2];
  srd_t sX, sW, sXq, sWq, sO;
  unsigned sx_kv, sw_kv, sx_q, sw_q, so;
  lds_cptr xr_kv[2], wr[8], xq[4], wq[4], wq1[4];
  const char* pf; unsigned pfstep;
  f32x4 acc[8][8]; Frags f0, f1; f32x4 ta, tb; u32x4 pk;
  f32x16 qa[8]; s16x4 ql[16], qh[16]; bf16x8 qx[4];
  unsigned kw[11], vv[8], kv[8]; u32x4 sk; u32x2 ep; f32x2 kf[11], sc[4], pa, pt;
  unsigned long long tag; SsmS* ss;
};
DI void k1_touch(const K1C& c) {
  asm volatile("s_add_u32 m0, %0, 147456\n\ts_nop 0\n\tglobal_load_lds_dword %1, off" :: "s"(c.ldsb), "v"(c.pf) : "memory", "scc"); }
template <int buf, int j> DI void k1_piece(const K1C& c, unsigned sw, unsigned sx) {
  constexpr int p = j >> 1;
  if constexpr (!(j & 1)) dma16<buf * 32768u + 65536u + p * 1024u, 2u * p * WRS>(c.lds8, c.vw[(p & 1) + 2 * (p >> 2)], c.sW, sw);
  else dma16<buf * 32768u + p * 1024u, 8u * p * (DC * 2)>(c.lds8, c.vx_kv[(p >> 1) & 1], c.sX, sx);
}
template <int S, int j> DI void k1_qpiece(const K1C& c) {
  if constexpr (j % 3 != 1) { constexpr int p = 2 * (j / 3) + (j % 3 == 2); dma16<qws(S % 3) + p * 1024u, (64u * S + 2u * p) * WRS>(c.lds8, c.vwq[p & 1], c.sWq, c.sw_q); }
  else { constexpr int p = j / 3; dma16<qxs(S % 3) + p * 1024u, 8u * p * (DM * 2) + 128u * S>(c.lds4, c.vx_q[p & 1], c.sXq, c.sx_q); }
}
template <int buf> DI void k1_stage(const K1C& c, int st) { sfor<16>([&](auto J) AI { k1_piece<buf, J>(c, c.sw_kv + (unsigned)st * (64u * WRS), c.sx_kv + (unsigned)st * 128u); }); }
template <int buf, int ks, int i> DI void k1_read(const K1C& c, Frags& f) {
  constexpr int so = buf * 32768;
  if constexpr (i < 2 || i >= 10) { constexpr int k = i < 2 ? i : i - 8, nb = k >> 1, t = k & 1;
    const s16x4 v = lds_tr(c.wr[nb] + so + ks * 16384 + t * 2048); if constexpr (t) f.wh[nb] = v; else f.wl[nb] = v; }
  else { constexpr int mb = i - 2; f.x[mb] = lds_rd128(c.xr_kv[ks] + so + (mb >> 1) * 4096 + (mb & 1) * 1024); }
}
template <int F> DI void k1_qw(K1C& c) {
  constexpr int T = F >> 5, ks = (F >> 3) & 3, nb = F & 7, sl = T % 3, o = (sl == 2 ? 32768 : 0) + ks * 8192 + (nb >> 2) * 256;
  const lds_cptr p = sl == 1 ? c.wq1[nb & 3] : c.wq[nb & 3];
  c.ql[F & 15] = lds_tr(p + o); c.qh[F & 15] = lds_tr(p + o + 2048);
}
template <int T, int ks> DI void k1_qx(K1C& c) { c.qx[ks] = lds_rd128(c.xq[ks] + qxs(T % 3)); }
template <int m> DI void k1_pre(K1C& c, const Params& P) {
  if constexpr (m == 0) {
    asm volatile("" : "+v"(c.kw[0]), "+v"(c.kw[1]), "+v"(c.kw[2]), "+v"(c.kw[3]), "+v"(c.kw[4]), "+v"(c.kw[5]), "+v"(c.kw[6]), "+v"(c.kw[7]), "+v"(c.kw[8]), "+v"(c.kw[9]), "+v"(c.kw[10]),
                 "+v"(c.vv[0]), "+v"(c.vv[1]), "+v"(c.vv[2]), "+v"(c.vv[3]), "+v"(c.vv[4]), "+v"(c.vv[5]), "+v"(c.vv[6]), "+v"(c.vv[7]), "+v"(c.sk), "+v"(c.ep));
    const unsigned long long e0 = ((unsigned long long)rfl(c.ep[1]) << 32) | rfl(c.ep[0]);
    c.tag = (e0 ^ 0x9E3779B97F4A7C15ull) * 0xD1342543DE82EF95ull + 1ull;
  } else if constexpr (m < 12) { c.kf[m - 1] = up2(c.kw[m - 1]); PINV(c.kf[m - 1]); }
  else if constexpr (m < 14) { constexpr int i = m - 12;
    c.sc[2 * i] = (f32x2){bf2f(c.sk[i] & 0xffffu), bf2f(c.sk[2 + i] & 0xffffu)}; c.sc[2 * i + 1] = (f32x2){bf2f(c.sk[i] >> 16), bf2f(c.sk[2 + i] >> 16)}; PINV(c.sc[2 * i]); PINV(c.sc[2 * i + 1]); }
  else if constexpr (m < 78) { constexpr int i = (m - 14) >> 3, s = (m - 14) & 7;
    if constexpr (s == 0) { c.pa = rb2(c.kf[i] * c.sc[3]); PINV(c.pa); }
    else if constexpr (s == 7) { const f32x2 a = c.pa * up2(c.vv[i]); c.kv[i] = cvtpk(a[0], a[1]); PINV(c.kv[i]); }
    else if constexpr (s & 1) { c.pt = rb2(c.kf[i + (s + 1) / 2] * c.sc[3 - (s + 1) / 2]); PINV(c.pt); }
    else { c.pa = rb2(c.pa + c.pt); PINV(c.pa); }
  } else {
    constexpr unsigned sel = m == 78 ? 0x05040100u : 0x07060302u;
    const unsigned tid = c.wid * 64u + (c.vlane >> 4), xo = (((c.b / 7u) * DM + 64u * (c.b % 7u) + 2u * (tid & 31u)) * 64u + 8u * (tid >> 5)) * 2u;
    st16w<(m - 78) * 128>((u32x4){__builtin_amdgcn_perm(c.kv[6], c.kv[7], sel), __builtin_amdgcn_perm(c.kv[4], c.kv[5], sel), __builtin_amdgcn_perm(c.kv[2], c.kv[3], sel), __builtin_amdgcn_perm(c.kv[0], c.kv[1], sel)},
                          mk_srd(P.ws + WS_XT, SEQ * DM * 2), xo, 0u);
  }
}
template <int i> DI void k1_preload(K1C& c, const Params& P) {
  const unsigned tid = c.wid * 64u + (c.vlane >> 4), bc0 = 64u * (c.b % 7u) + 2u * (tid & 31u), vo = ((64u * (c.b / 7u) + 8u * (tid >> 5)) * DM + bc0) * 2u;
  if constexpr (i < 11) ld4(c.kw[i], mk_srd(P.bk, SEQ * DM * 2), vo + (unsigned)((i - 3) * DM * 2));
  else if constexpr (i < 19) ld4(c.vv[i - 11], mk_srd(P.bv, SEQ * DM * 2), vo + (unsigned)((i - 11) * DM * 2));
  else if constexpr (i == 19) asm volatile("buffer_load_dwordx4 %0, %1, %2, 0 offen" : "=v"(c.sk) : "v"(bc0 * 8u), "s"(mk_srd(P.shk, DM * 8)) : "memory");
  else asm volatile("buffer_load_dwordx2 %0, off, %1, 0 sc1" : "=v"(c.ep) : "s"(mk_srd(P.ws + WS_FL, 64)) : "memory");
}
template <bool WA, int STEP, int buf, int G> DI void k1_gap(K1C& c, const Params& P, unsigned sw, unsigned sx) {
  constexpr int ks = G >> 6, g = G & 63, nb = g >> 3, mb = g & 7;
  Frags& fc = ks ? c.f1 : c.f0; Frags& fn = ks ? c.f0 : c.f1;
  if constexpr (G == 64) {
    asm volatile("s_waitcnt vmcnt(%0) lgkmcnt(0)\n\ts_barrier" :: "n"(STEP == 0 ? 3 : STEP == 14 ? 0 : STEP == 15 ? 12 : 1) : "memory");
    SBAR();
  }
  const bf16x8 w = cat(fc.wl[nb], fc.wh[nb]);
  if constexpr (G == 0) asm volatile("s_nop 1" :: "v"(w), "v"(fc.x[0]));
  if constexpr (WA) mfma16a(c.acc[mb][nb], w, fc.x[mb]); else mfma16a(c.acc[nb][mb], fc.x[mb], w);
  SBAR();
  if constexpr (!(g & 1) && g < 48) {
    if constexpr (ks == 0) k1_read<buf, 1, g / 2>(c, fn);
    else if constexpr (STEP != 15) k1_read<buf ^ 1, 0, g / 2>(c, fn);
  }
  if constexpr (STEP == 15 && ks == 1 && g >= 36 && !(g & 3)) { if constexpr (g == 36) k1_qx<0, 0>(c); else k1_qw<(g - 40) / 4>(c); }
  if constexpr (STEP == 0 && ks == 0 && (g & 1) && g < 16) k1_piece<1, 8 + (g >> 1)>(c, c.sw_kv + 64u * WRS, c.sx_kv + 128u);
  if constexpr (STEP == 0 && (G == 17 || G == 19 || G == 21)) { k1_touch(c); c.pf += c.pfstep; }
  if constexpr (ks == 1) {
    if constexpr (STEP <= 13 && (g & 3) == 1) k1_piece<buf, (g >> 2)>(c, sw, sx);
    if constexpr (STEP == 0 && (g & 3) == 3) k1_preload<(g >> 2)>(c, P);
    if constexpr (STEP == 0 && (g & 3) == 2 && g >= 44) k1_preload<16 + ((g - 44) >> 2)>(c, P);
    if constexpr (STEP == 14 && (g & 1) && g < 60 && (g >> 1) % 5 != 4) { constexpr int j = (g >> 1) - (g >> 1) / 5; k1_qpiece<j / 12, j % 12>(c); }
    if constexpr (STEP == 15 && (g & 3) == 1 && g < 48) k1_qpiece<2, (g >> 2)>(c);
  }
  if constexpr (STEP <= 13 && G == 127) { k1_touch(c); c.pf += c.pfstep; }
  if constexpr (STEP >= 13) { constexpr int X = (STEP - 13) * 128 + G; if constexpr (X >= 56 && X % 4 == 3 && (X - 56) / 4 < 80) k1_pre<(X - 56) / 4>(c, P); }
  SBAR();
}
template <bool WA, int STEP, int buf> DI void k1_step(K1C& c, const Params& P, int sb) {
  const unsigned sw = c.sw_kv + (unsigned)sb * (64u * WRS), sx = c.sx_kv + (unsigned)sb * 128u;
  sfor<128>([&](auto G) AI { k1_gap<WA, STEP, buf, G>(c, P, sw, sx); });
}
#define LPG 3
constexpr int q_lpos(int k) { return k == 0 ? 7 : k == 1 ? 3 : 5; }
constexpr bool q_late(int u) { for (int k = 0; k < LPG; ++k) if (u % 8 == q_lpos(k)) return true; return false; }
constexpr int q_lidx(int u) { for (int k = 0; k < LPG; ++k) if (u % 8 == q_lpos(k)) return LPG * (u / 8) + k; return 0; }
constexpr int q_lunit(int k) { return 8 * (k / (LPG ? LPG : 1)) + q_lpos(k % (LPG ? LPG : 1)); }
constexpr int q_stores(int lo, int hi) { int n = 0; for (int u = 0; u < 32; ++u) n += (!q_late(u) && 6 * u + 5 > lo && 6 * u + 5 < hi); return n; }
constexpr int q_wait(int T) {
  return T == 0 ? 14 + q_stores(-1, 26) : T == 1 ? 12 + q_stores(-1, 58) : (T + 2 <= 6 ? 12 : 0) + q_stores(32 * (T - 2) + 48, 32 * T + 26); }
template <bool WA, int X> DI void k1_qgap(K1C& c, const Params& P) {
  constexpr int T = X >> 5, g = X & 31, ks = g >> 3, nb = g & 7;
  if constexpr (g == 26 && T < 6) {
    asm volatile("s_waitcnt vmcnt(%0) lgkmcnt(0)\n\ts_barrier" :: "n"(q_wait(T)) : "memory"); SBAR();
    if constexpr (T == 1) { if (c.wid == 0 && lane_id() == 0) __hip_atomic_store((gu64*)(P.ws + WS_FL) + FL_S0 + c.b * 16u, c.tag, RLX_AGENT); SBAR(); }
  }
  const bf16x8 w = cat(c.ql[X & 15], c.qh[X & 15]);
  if constexpr (X < 8) mfma32qz(c.qa[nb], w, c.qx[ks]); else mfma32q(c.qa[nb], w, c.qx[ks]);
  SBAR();
  if constexpr (g < 22) { constexpr int f = 6 + g + (g + 4) / 5 - (g > 20 ? 1 : 0); k1_qw<32 * T + f>(c); if constexpr (g % 5 == 0 && g < 20) k1_qw<32 * T + f + 1>(c); }
  if constexpr (g >= 26 && T < 6) k1_qw<32 * (T + 1) + g - 26>(c);
  if constexpr (g == 2 || g == 9 || g == 17) k1_qx<T, ks + 1>(c);
  if constexpr (g == 27 && T < 6) k1_qx<T + 1, 0>(c);
  if constexpr (X >= 26 && !(X & 1) && (X - 26) % 32 < 24 && (X - 26) / 32 + 3 <= 6) k1_qpiece<(X - 26) / 32 + 3, ((X - 26) % 32) / 2>(c);
  constexpr int u = X / 6, ph = X % 6;
  if constexpr (u < 32) {
    constexpr int i = u >> 2, j = u & 3;
    if constexpr (ph == 1) { c.ta = c.acc[i][2 * j]; PINV(c.ta); }
    if constexpr (ph == 2) { c.tb = c.acc[i][2 * j + 1]; PINV(c.tb); }
    if constexpr (ph == 3) { c.pk[0] = cvtpk(c.ta[0], c.ta[1]); c.pk[1] = cvtpk(c.ta[2], c.ta[3]); PINV(c.pk); }
    if constexpr (ph == 4) { c.pk[2] = cvtpk(c.tb[0], c.tb[1]); c.pk[3] = cvtpk(c.tb[2], c.tb[3]); PINV(c.pk); }
    if constexpr (ph == 5 && q_late(u)) { c.ss->late[q_lidx(u)] = __builtin_bit_cast(f32x4, c.pk); PINA(c.ss->late[q_lidx(u)]); }
    if constexpr (ph == 5 && !q_late(u)) st16w<(WA ? (i >> 1) * 16384u + j * 2048u : (i >> 1) * 2048u + j * 16384u) + (i & 1) * 1024u>(c.pk, c.sO, c.vlane, c.so);
  }
  if constexpr (X >= 200 && X < 205) {
    const unsigned tid = c.wid * 64u + (c.vlane >> 4), c0 = 2u * (28u * (c.b & 7u) + (c.b >> 3)); constexpr int ch = (X - 200) >> 1;
    if constexpr (X == 204) asm volatile("buffer_load_dwordx2 %0, %1, %2, 0 offen sc1" : "=v"(c.ss->f0) : "v"((FL_S0 + (7u * (tid & 31u) + (c0 >> 6)) * 16u) * 8u), "s"(mk_srd(P.ws + WS_FL, 1u << 20)) : "memory");
    else if constexpr (X & 1) asm volatile("buffer_load_dwordx2 %0, %1, %2, 0 offen offset:16" : "=v"(c.ss->hB[ch]) : "v"(tid * 16u), "s"(mk_srd(P.ssmk + (size_t)(c0 + ch) * SEQ, c.b < 224u ? SEQ * 2 : 0)) : "memory");
    else asm volatile("buffer_load_dwordx4 %0, %1, %2, 0 offen" : "=v"(c.ss->hA[ch]) : "v"(tid * 16u), "s"(mk_srd(P.ssmk + (size_t)(c0 + ch) * SEQ, c.b < 224u ? SEQ * 2 : 0)) : "memory");
  }
  if constexpr (X >= 196 && X < 200) {
    constexpr int j = X - 196; const unsigned ln = c.vlane >> 4;
    dma16<WO_LDS + j * 1024u, j * 256u>(c.lds4, (ln >> 2) * (DM * 2) + (ln & 3u) * 16u, mk_srd(P.wo, NH * DH * DM * 2), c.hb * (16u * DM * 2) + c.wid * 64u);
  }
  SBAR();
}

template <bool WA> DI void k1_main(K1C& c, const Params& P, const char* pf2, u32x4 (&qo)[16]) {
  sfor<64>([&](auto I) AI { c.acc[I >> 3][I & 7] = f32x4{}; PINA(c.acc[I >> 3][I & 7]); });
  SBAR();
  sfor<8>([&](auto J) AI { k1_piece<1, J>(c, c.sw_kv + 64u * WRS, c.sx_kv + 128u); });
  asm volatile("s_waitcnt vmcnt(8)\n\ts_barrier" ::: "memory");
  sfor<24>([&](auto I) AI { k1_read<0, 0, I>(c, c.f0); });
  SBAR();
  k1_step<WA, 0, 0>(c, P, 2);
#pragma nounroll
  for (int t = 1; t < 13; t += 2) {
    k1_step<WA, 1, 1>(c, P, t + 2);
    c.pf = t == 9 ? pf2 : c.pf;
    k1_step<WA, 1, 0>(c, P, t + 3);
  }
  k1_step<WA, 13, 1>(c, P, 15);
  {
    const unsigned lane = lane_id(), r = lane & 31u, hh = lane >> 5, q = (lane & 15u) >> 2, p = lane & 3u, blk = (lane >> 4) & 1u;
    sfor<2>([&](auto I) AI { c.vx_q[I] = (lane >> 3) * (DM * 2) + 16u * ((lane & 7u) ^ (4u * I + (lane >> 4))); c.vwq[I] = hh * WRS + 16u * (r ^ ((2u * I + hh) << 2)); });
    sfor<4>([&](auto I) AI {
      c.xq[I] = (lds_cptr)(size_t)(c.ldsb + (32u * c.wid + r) * 128u + 16u * ((2u * I + hh) ^ ((r >> 1) & 7u))); PINV(c.xq[I]);
      c.wq[I] = (lds_cptr)(size_t)(c.ldsb + 65536u + (8u * hh + q) * 512u + ((unsigned)I ^ q) * 64u + 8u * (4u * (p >> 1) + 2u * blk + (p & 1u))); PINV(c.wq[I]);
      c.wq1[I] = c.wq[I] + 65536; PINV(c.wq1[I]); });
  }
  k1_step<WA, 14, 0>(c, P, 0);
  k1_step<WA, 15, 1>(c, P, 0);
  sfor<224>([&](auto X) AI { k1_qgap<WA, X>(c, P); });
  MFMA_DRAIN(); SBAR();
  sfor<8>([&](auto D) AI { PINV(c.qa[D]); qo[2 * D] = pack8w(c.qa[D], 0, QSCALE); qo[2 * D + 1] = pack8w(c.qa[D], 1, QSCALE); PINV(qo[2 * D]); PINV(qo[2 * D + 1]); SBAR(); });
  asm volatile("s_waitcnt vmcnt(0)" : "+v"(c.ss->hA[0]), "+v"(c.ss->hA[1]), "+v"(c.ss->hB[0]), "+v"(c.ss->hB[1]), "+v"(c.ss->f0) :: "memory");
}

DI unsigned long long phase_proj(const Params& P, char* lds, const unsigned wid, const unsigned qblk, u32x4 (&qo)[16], SsmS& ss) {
  const unsigned lane = lane_id(), wr = wid >> 1, wc = wid & 1;
  const unsigned b = blockIdx.x, xcd = b & 7u, idx = b >> 3;
  const unsigned pm = idx & 7u, which = idx >> 3, h = 2u * xcd + (which >> 1), isV = which & 1u;
  K1C c; c.ss = &ss;
  c.ldsb = (unsigned)(size_t)lds; c.lds8 = c.ldsb + wid * 8192u; c.lds4 = c.ldsb + wid * 4096u; c.wid = wid; c.vlane = lane * 16u; c.b = b; c.hb = 16u * h + (idx & 15u);
  const unsigned short* wkv = isV ? P.wuv : P.wuk;
  const bool pw = lane >= 32u;
  unsigned pfa = (192u + 8u * pm + 2u * wid + ((lane >> 2) & 1u)) * WRS + h * (DH * 2) + (lane & 3u) * 128u, pfb = (256u * pm + 64u * which + 16u * wid + (lane & 15u)) * (DC * 2) + 384u;
  PINV(pfa); PINV(pfb);
  c.pf = (pw ? (const char*)wkv : (const char*)P.ckv) + (pw ? pfa : pfb);
  c.pfstep = pw ? 64u * WRS : 128u;
  c.sX = mk_srd(P.ckv, SEQ * DC * 2); c.sW = mk_srd(wkv, DC * WRS); c.sXq = mk_srd(P.x, SEQ * DM * 2); c.sWq = mk_srd(P.wq, DM * WRS);
  sfor<2>([&](auto I) AI { c.vx_kv[I] = (lane >> 3) * (DC * 2) + 16u * ((lane & 7u) ^ (4u * I + (lane >> 4))); });
  sfor<4>([&](auto I) AI { const unsigned f = (2u * (I & 1) + (lane >> 5)) | (4u * (I >> 1)); c.vw[I] = (lane >> 5) * WRS + 16u * ((lane & 31u) ^ (f << 1)); });
  c.sx_kv = (256u * pm + 64u * wid) * (DC * 2); c.sw_kv = 16u * wid * WRS + h * (DH * 2);
  c.sx_q = (128u * qblk + 32u * wid) * (DM * 2);  c.sw_q = c.sw_kv;
  k1_stage<0>(c, 0);
    const unsigned rho = lane & 15u, g = lane >> 4;
  sfor<2>([&](auto I) AI { c.xr_kv[I] = (lds_cptr)lds + (wr * 128u + 16u * (rho >> 3) + (rho & 7u)) * 128u + 16u * ((4u * I + g) ^ (rho >> 1)); PINV(c.xr_kv[I]); });
  { const unsigned q = (lane & 15u) >> 2, p = lane & 3u, f = q | ((g & 1u) << 2);
    sfor<8>([&](auto I) AI { c.wr[I] = (lds_cptr)lds + 65536u + (8u * g + q) * 512u + (8u * wc + ((unsigned)I ^ f)) * 32u + 8u * p; PINV(c.wr[I]); }); }
  c.sO = mk_srd(P.ws + WS_K + (isV ? MB16 : 0u) + (size_t)h * (SEQ * DH * 2), SEQ * DH * 2);
  c.so = (8u * pm + 4u * wr) * 16384u + wc * 8192u;
  pfa = (192u + 4u * (idx & 15u) + wid) * WRS + h * (DH * 2) + (lane & 3u) * 128u; pfb = (128u * qblk + 64u * (idx >> 4) + 16u * wid + (lane & 15u)) * (DM * 2) + 384u;
  PINV(pfa); PINV(pfb);
  const char* pf2 = (pw ? (const char*)P.wq : (const char*)P.x) + (pw ? pfa : pfb);
  ss.sO = c.sO; ss.so = c.so; ss.sA = isV ? 2048u : 16384u; ss.sB = isV ? 16384u : 2048u;
  if (isV) k1_main<false>(c, P, pf2, qo); else k1_main<true>(c, P, pf2, qo);
  return c.tag;
}

constexpr float THR = 8.0f;
constexpr int AO = 0, AQ = 128, AQ2 = 192;
template <int D> DI void pv_mfma(const bf16x8& a, const u32x4& b) { asm volatile("v_mfma_f32_32x32x16_bf16 a[%c2:%c3], %0, %1, a[%c2:%c3]" :: "v"(a), "v"(b), "n"(16 * D), "n"(16 * D + 15)); }
template <int D> DI void pv_mfma4(const bf16x8& a, const u32x4& b, const bf16x8& x, const bf16x8& y, const bf16x8& z) { asm volatile("v_mfma_f32_32x32x16_bf16 a[%c2:%c3], %0, %1, a[%c2:%c3]" :: "v"(a), "v"(b), "n"(16 * D), "n"(16 * D + 15), "v"(x), "v"(y), "v"(z)); }
template <int D> DI void pv_mfmaz(const bf16x8& a, const u32x4& b, const bf16x8& x, const bf16x8& y, const bf16x8& z) { asm volatile("v_mfma_f32_32x32x16_bf16 a[%c2:%c3], %0, %1, 0" :: "v"(a), "v"(b), "n"(16 * D), "n"(16 * D + 15), "v"(x), "v"(y), "v"(z)); }
template <int S> DI void qk_mfma(f32x16& c, const bf16x8& a) { asm volatile("v_mfma_f32_32x32x16_bf16 %0, %1, a[%c2:%c3], %0" : "+v"(c) : "v"(a), "n"(AQ + 4 * S), "n"(AQ + 4 * S + 3)); }
template <int S> DI void qk_mfma4(f32x16& c, const bf16x8& a, const bf16x8& x, const bf16x8& y, const bf16x8& z) { asm volatile("v_mfma_f32_32x32x16_bf16 %0, %1, a[%c2:%c3], %0" : "+v"(c) : "v"(a), "n"(AQ + 4 * S), "n"(AQ + 4 * S + 3), "v"(x), "v"(y), "v"(z)); }
DI void qk_init4(f32x16& d, const bf16x8& a, const f32x16& c, const bf16x8& x, const bf16x8& y, const bf16x8& z) { asm volatile("v_mfma_f32_32x32x16_bf16 %0, %1, a[128:131], %2" : "=&v"(d) : "v"(a), "v"(c), "v"(x), "v"(y), "v"(z)); }
template <int R> DI void acc_zero4() { asm volatile("v_accvgpr_write_b32 a[%c0], 0\n\tv_accvgpr_write_b32 a[%c1], 0\n\tv_accvgpr_write_b32 a[%c2], 0\n\tv_accvgpr_write_b32 a[%c3], 0" :: "n"(R), "n"(R + 1), "n"(R + 2), "n"(R + 3)); }
template <int R> DI void acc_write4(const u32x4& w) { asm volatile("v_accvgpr_write_b32 a[%c4], %0\n\tv_accvgpr_write_b32 a[%c5], %1\n\tv_accvgpr_write_b32 a[%c6], %2\n\tv_accvgpr_write_b32 a[%c7], %3" :: "v"(w[0]), "v"(w[1]), "v"(w[2]), "v"(w[3]), "n"(R), "n"(R + 1), "n"(R + 2), "n"(R + 3)); }
template <int R, int S> DI void acc_mov4() { asm volatile("v_accvgpr_mov_b32 a[%c0], a[%c4]\n\tv_accvgpr_mov_b32 a[%c1], a[%c5]\n\tv_accvgpr_mov_b32 a[%c2], a[%c6]\n\tv_accvgpr_mov_b32 a[%c3], a[%c7]" :: "n"(R), "n"(R + 1), "n"(R + 2), "n"(R + 3), "n"(S), "n"(S + 1), "n"(S + 2), "n"(S + 3)); }
template <int R> DI f32x4 acc_read4() { float a, b, c, d; asm volatile("v_accvgpr_read_b32 %0, a[%c4]\n\tv_accvgpr_read_b32 %1, a[%c5]\n\tv_accvgpr_read_b32 %2, a[%c6]\n\tv_accvgpr_read_b32 %3, a[%c7]" : "=v"(a), "=v"(b), "=v"(c), "=v"(d) : "n"(R), "n"(R + 1), "n"(R + 2), "n"(R + 3)); return (f32x4){a, b, c, d}; }
template <int R> DI void acc_scale2(float f) { float t, u; asm volatile("v_accvgpr_read_b32 %0, a[%c3]\n\tv_accvgpr_read_b32 %1, a[%c4]\n\tv_mul_f32 %0, %2, %0\n\tv_mul_f32 %1, %2, %1\n\tv_accvgpr_write_b32 a[%c3], %0\n\tv_accvgpr_write_b32 a[%c4], %1" : "=&v"(t), "=&v"(u) : "v"(f), "n"(R), "n"(R + 1)); }
template <int R, unsigned SI> DI void acc_store4(srd_t srd, unsigned voff) { unsigned t; asm volatile("s_mov_b32 %0, %3\n\tbuffer_store_dwordx4 a[%c4:%c5], %1, %2, %0 offen sc1" : "=&s"(t) : "v"(voff), "s"(srd), "n"(SI), "n"(R), "n"(R + 3) : "memory"); }
template <int R, unsigned SI> DI void acc_load4(srd_t srd, unsigned voff) { unsigned t; asm volatile("s_mov_b32 %0, %3\n\tbuffer_load_dwordx4 a[%c4:%c5], %1, %2, %0 offen sc1" : "=&s"(t) : "v"(voff), "s"(srd), "n"(SI), "n"(R), "n"(R + 3) : "memory"); }
#define A8(n) "a" #n "0", "a" #n "1", "a" #n "2", "a" #n "3", "a" #n "4", "a" #n "5", "a" #n "6", "a" #n "7", "a" #n "8", "a" #n "9"
#define ACC_CLAIM() asm volatile("" ::: "a0", "a1", "a2", "a3", "a4", "a5", "a6", "a7", "a8", "a9", A8(1), A8(2), A8(3), A8(4), A8(5), A8(6), A8(7), A8(8), A8(9), A8(10), A8(11), A8(12), A8(13), A8(14), \
  A8(15), A8(16), A8(17), A8(18), A8(19), A8(20), A8(21), A8(22), A8(23), A8(24), "a250", "a251", "a252", "a253", "a254", "a255")
DI float max3f(float a, float b, float c) { float r; asm("v_max3_f32 %0, %1, %2, %3" : "=v"(r) : "v"(a), "v"(b), "v"(c)); return r; }
DI float max2f(float a, float b) { float r; asm("v_max_f32_e32 %0, %1, %2" : "=v"(r) : "v"(a), "v"(b)); return r; }
DI float fadd_s(float a, float b) { float r; asm volatile("v_add_f32_e32 %0, %1, %2" : "=v"(r) : "v"(a), "v"(b)); return r; }
DI float fsub_s(float a, float b) { float r; asm("v_sub_f32_e32 %0, %1, %2" : "=v"(r) : "v"(a), "v"(b)); return r; }
constexpr int koff(int f) { return (f >> 1) * 2048 + (f & 1) * 512; }
constexpr int voff(int f) { return (f >> 4) * 16384 + (f & 7) * 2048 + ((f >> 3) & 1) * 512; }
struct K2C {
  unsigned ldsb, vlane;
  srd_t sK;
  lds_cptr kr, vr;
  unsigned kslot_w, vslot_w, vslot_r;
  unsigned ksoff, vsoff, dt;
  f32x16 s0, s1, negm; u32x4 p[4]; bf16x8 fr[16];
  float m, l0, l1, mx0, mx1, fct, thr, mS, lS; bool pend;
  f32x4 ml, ob[32];
  int sw; srd_t sP;
  int dq, rel;
};
template <int J> DI void k2_dma(unsigned& t, unsigned m0v, unsigned voff, srd_t srd, unsigned soff) {
  if constexpr (J == 0) asm volatile("s_mov_b32 m0, %1\n\ts_mov_b32 %0, %4\n\tbuffer_load_dwordx4 %2, %3, %0 offen sc1 lds" : "=&s"(t) : "s"(m0v), "v"(voff), "s"(srd), "s"(soff) : "memory");
  else if constexpr (J == 4) asm volatile("s_add_u32 m0, m0, 4096\n\ts_add_u32 %0, %0, 4096\n\tbuffer_load_dwordx4 %1, %2, %0 offen sc1 lds" : "+s"(t) : "v"(voff), "s"(srd) : "memory", "scc");
  else asm volatile("buffer_load_dwordx4 %0, %1, %2 offen offset:%3 sc1 lds" :: "v"(voff), "s"(srd), "s"(t), "n"((J & 3) * 1024) : "memory");
}
DI void k2_dma8(unsigned m0v, unsigned voff, srd_t srd, unsigned soff) { unsigned t; sfor<8>([&](auto J) AI { k2_dma<J>(t, m0v, voff, srd, soff); }); }
template <int I> DI void k2_mask4(f32x16& s, int d, float ninf) {
  constexpr int r = 4 * I; float a = s[r], b = s[r + 1], c = s[r + 2], e = s[r + 3];
  asm volatile("v_cmp_gt_i32 vcc, %5, %4\n\tv_cmp_gt_i32 s[4:5], %6, %4\n\tv_cmp_gt_i32 s[6:7], %7, %4\n\tv_cmp_gt_i32 s[8:9], %8, %4\n\t"
               "v_cndmask_b32 %0, %0, %9, vcc\n\tv_cndmask_b32 %1, %1, %9, s[4:5]\n\tv_cndmask_b32 %2, %2, %9, s[6:7]\n\tv_cndmask_b32 %3, %3, %9, s[8:9]"
               : "+v"(a), "+v"(b), "+v"(c), "+v"(e) : "v"(d), "n"(8 * I), "n"(8 * I + 1), "n"(8 * I + 2), "n"(8 * I + 3), "v"(ninf) : "vcc", "s4", "s5", "s6", "s7", "s8", "s9");
  s[r] = a; s[r + 1] = b; s[r + 2] = c; s[r + 3] = e;
}
DI void k2_mask(f32x16& s, int rel, int dq) {
  const float ninf = -__builtin_inff(); const int d = rel > 0 ? -100 : dq;
  k2_mask4<0>(s, d, ninf); k2_mask4<1>(s, d, ninf); k2_mask4<2>(s, d, ninf); k2_mask4<3>(s, d, ninf);
}
DI void max4(float& m, float a, float b, float c, float d) { asm volatile("v_max3_f32 %0, %0, %1, %2\n\tv_max3_f32 %0, %0, %3, %4" : "+v"(m) : "v"(a), "v"(b), "v"(c), "v"(d)); }
DI void max6(float& m, float a, float b, float c, float d, float e, float f) { asm volatile("v_max3_f32 %0, %0, %1, %2\n\tv_max3_f32 %0, %0, %3, %4\n\tv_max3_f32 %0, %0, %5, %6" : "+v"(m) : "v"(a), "v"(b), "v"(c), "v"(d), "v"(e), "v"(f)); }
DI float max4i(float a, float b, float c, float d) { float m; asm volatile("v_max_f32_e32 %0, %1, %2\n\tv_max3_f32 %0, %0, %3, %4" : "=&v"(m) : "v"(a), "v"(b), "v"(c), "v"(d)); return m; }
template <int e> DI void k2_lsum(K2C& c, float v) { if constexpr (e & 1) c.l1 = fadd_s(c.l1, v); else c.l0 = fadd_s(c.l0, v); }
template <bool FIN, bool QK, bool FL, int g> DI void k2_gapA(K2C& c) {
  if constexpr (QK) {
    if constexpr (g == 0) qk_init4(c.s0, c.fr[0], c.negm, c.fr[1], c.fr[2], c.fr[3]);
    else if constexpr (g == 16) qk_init4(c.s1, c.fr[0], c.negm, c.fr[1], c.fr[2], c.fr[3]);
    else if constexpr ((g & 3) == 0) qk_mfma4<g & 15>(g < 16 ? c.s0 : c.s1, c.fr[g & 15], c.fr[(g + 1) & 15], c.fr[(g + 2) & 15], c.fr[(g + 3) & 15]);
    else qk_mfma<g & 15>(g < 16 ? c.s0 : c.s1, c.fr[g & 15]);
    SBAR();
    if constexpr (g < 24) c.fr[(g + 8) & 15] = lds_rd128(c.kr + koff((g + 8)));
  }
  if constexpr (!QK && g <= 9) { asm volatile("s_nop 0"); SBAR(); }
  if constexpr (!FIN) acc_zero4<4 * g>();
  if constexpr (FIN && (QK ? g >= 24 : g < 8)) c.fr[g & 7] = lds_rd128(c.vr + voff((g & 7)));
  if constexpr (FIN && g <= 8) {
    if constexpr (g < 8) c.s1[8 + g] = __builtin_amdgcn_exp2f(c.s1[8 + g]);
    k2_lsum<g>(c, c.s1[7 + g]);
    if constexpr (g < 4) c.p[2][g] = cvtpk(c.s1[2 * g], c.s1[2 * g + 1]);
    else if constexpr (g < 7) c.p[3][g - 4] = cvtpk(c.s1[2 * g], c.s1[2 * g + 1]);
    else if constexpr (g == 8) c.p[3][3] = cvtpk(c.s1[14], c.s1[15]);
    if constexpr (g < 8) PINV(c.s1);
    if constexpr (g < 4) PINV(c.p[2]); else PINV(c.p[3]);
  }
  if constexpr (FIN && (QK ? g < 24 && (g & 3) == 1 : g < 6)) k2_dma<(QK ? g / 4 : g) + 2>(c.dt, 0u, c.vlane, c.sK, 0u);
  if constexpr (FL) {
    acc_store4<4 * g, 1024 * g>(c.sP, c.vlane);
    if constexpr (g == 12) { float a = c.mS, b = c.lS; PINV(a); PINV(b); const u32x4 v = {__float_as_uint(a), __float_as_uint(b), 0u, 0u}; st16w<32 * 1024>(v, c.sP, c.vlane, 0u); }
  }
  if constexpr (QK) {
    if constexpr (g >= 24 && (g & 3) == 1) k2_dma<(g - 24) / 4>(c.dt, c.ldsb + 65536u + c.vslot_w, c.vlane, c.sK, c.vsoff);
    if constexpr (g == 18) { if (c.rel >= 0) k2_mask(c.s0, c.rel, c.dq); }
    if constexpr (g == 20) { c.mx0 = max4i(c.s0[0], c.s0[1], c.s0[2], c.s0[3]); }
    if constexpr (g >= 21 && g < 24) { constexpr int r = 4 * (g - 20); max4(c.mx0, c.s0[r], c.s0[r + 1], c.s0[r + 2], c.s0[r + 3]); }
  }
  SBAR();
}
template <bool FIN, bool QK, bool FL = false> DI void k2_phaseA(K2C& c) {
  if constexpr (QK && !FIN) {
#pragma unroll
    for (int i = 0; i < 8; ++i) c.fr[i] = lds_rd128(c.kr + koff(i));
  }
  sfor<32>([&](auto G) AI { k2_gapA<FIN, QK, FL, G>(c); });
}
template <bool PV, bool SM, bool FL, int g> DI void k2_gapB(K2C& c) {
  if constexpr (SM && g == 24) {
    asm volatile("s_waitcnt vmcnt(%0) lgkmcnt(0)\n\ts_barrier" :: "n"(FL ? 6 : 8) : "memory");
    c.kr = (lds_cptr)(size_t)((unsigned)(size_t)c.kr ^ 32768u);
    SBAR();
  }
  if constexpr (PV) {
    if constexpr (SM && FL && g < 8) pv_mfmaz<g>(c.fr[g], c.p[0], c.fr[g | 1], c.fr[g | 2], c.fr[g | 3]);
    else if constexpr ((g & 3) == 0) pv_mfma4<g & 7>(c.fr[g & 15], c.p[g >> 3], c.fr[(g + 1) & 15], c.fr[(g + 2) & 15], c.fr[(g + 3) & 15]);
    else pv_mfma<g & 7>(c.fr[g & 15], c.p[g >> 3]);
    SBAR();
    if constexpr (g < 2) { c.fr[(8 + 2 * g) & 15] = lds_rd128(c.vr + voff((8 + 2 * g))); c.fr[(9 + 2 * g) & 15] = lds_rd128(c.vr + voff((9 + 2 * g))); }
    else if constexpr (g < 8) c.fr[(g + 10) & 15] = lds_rd128(c.vr + voff((g + 10)));
    else if constexpr (g < 10) { c.fr[(2 * g + 2) & 15] = lds_rd128(c.vr + voff((2 * g + 2))); c.fr[(2 * g + 3) & 15] = lds_rd128(c.vr + voff((2 * g + 3))); }
    else if constexpr (g < 20) c.fr[(g + 12) & 15] = lds_rd128(c.vr + voff((g + 12)));
  }
  if constexpr (!SM && FL) c.ob[g] = __builtin_bit_cast(f32x4, __builtin_amdgcn_raw_buffer_load_b128(c.sP, c.vlane, g * 1024, 16));
  if constexpr (!PV && g >= 7) { asm volatile("s_nop 0"); SBAR(); }
  if constexpr (SM) {
    if constexpr (g >= 24) c.fr[(g + 8) & 15] = lds_rd128(c.kr + koff((g - 24)));
    if constexpr ((g & 3) == 1) { if constexpr (g < 24) k2_dma<g / 4 + 2>(c.dt, 0u, c.vlane, c.sK, 0u); else k2_dma<(g - 24) / 4>(c.dt, c.ldsb + c.kslot_w, c.vlane, c.sK, c.ksoff); }
    if constexpr (g == 2) { if (c.rel + 32 >= 0) k2_mask(c.s1, c.rel + 32, c.dq); }
    if constexpr (g == 3) { max6(c.mx0, c.s1[0], c.s1[1], c.s1[2], c.s1[3], c.s1[4], c.s1[5]); }
    if constexpr (g == 4) { max6(c.mx0, c.s1[6], c.s1[7], c.s1[8], c.s1[9], c.s1[10], c.s1[11]); }
    if constexpr (g == 5) { max4(c.mx0, c.s1[12], c.s1[13], c.s1[14], c.s1[15]); c.mx1 = c.mx0; PINV(c.mx1); }
    if constexpr (g == 6) {
      if constexpr (!PV) asm volatile("s_nop 1");
      auto pr = __builtin_amdgcn_permlane32_swap(__float_as_uint(c.mx0), __float_as_uint(c.mx1), false, false);
      const float rm = max2f(__uint_as_float(pr[0]), __uint_as_float(pr[1]));
      if constexpr (!PV) {
        const float dl = max2f(rm, -1e30f); c.m = dl;
#pragma unroll
        for (int r = 0; r < 16; ++r) { c.s0[r] = fsub_s(c.s0[r], dl); c.s1[r] = fsub_s(c.s1[r], dl); c.negm[r] = -dl; }
        PINV(c.negm);
      } else if (__builtin_expect(__any(rm > c.thr), 0)) {
        float dl;
        if (!FL && c.sw == 1) {
          float l = c.l0 + c.l1; auto pl = __builtin_amdgcn_permlane32_swap(__float_as_uint(l), __float_as_uint(l), false, false);
          c.mS = c.m; c.lS = __uint_as_float(pl[0]) + __uint_as_float(pl[1]); c.l0 = 0.f; c.l1 = 0.f;
          dl = max2f(rm, -1e30f); c.m = dl;
        } else {
          dl = max2f(rm, 0.f); c.m = fadd_s(c.m, dl); c.fct = __builtin_amdgcn_exp2f(-dl);
          c.l0 *= c.fct; c.l1 *= c.fct; c.pend = true;
        }
#pragma unroll
        for (int r = 0; r < 16; ++r) { c.s0[r] = fsub_s(c.s0[r], dl); c.s1[r] = fsub_s(c.s1[r], dl); c.negm[r] = -c.m; }
      }
    }
    if constexpr (g >= 7 && g < 23) c.s0[g - 7] = __builtin_amdgcn_exp2f(c.s0[g - 7]);
    if constexpr (g >= 8 && g < 24) k2_lsum<g>(c, c.s0[g - 8]);
    if constexpr (g >= 24) c.s1[g - 24] = __builtin_amdgcn_exp2f(c.s1[g - 24]);
    if constexpr (g >= 25) k2_lsum<g>(c, c.s1[g - 25]);
    if constexpr (g >= 16 && g < 24) { constexpr int w = g - 16; c.p[w >> 2][w & 3] = cvtpk(c.s0[2 * w], c.s0[2 * w + 1]); PINV(c.p[w >> 2]); }
    if constexpr (g >= 7 && g < 23) PINV(c.s0);
    if constexpr (g >= 24) PINV(c.s1);
  }
  SBAR();
}
template <bool PV, bool SM, bool FL = false> DI void k2_phaseB(K2C& c) {
  if constexpr (!PV) { MFMA_DRAIN(); SBAR(); }
  sfor<32>([&](auto G) AI { k2_gapB<PV, SM, FL, G>(c); });
  if constexpr (PV && SM) {
    if (__builtin_expect(c.pend, 0)) {
      MFMA_DRAIN(); SBAR();
      sfor<64>([&](auto I) AI { acc_scale2<2 * I>(c.fct); });
      c.pend = false;
    }
    SBAR();
  }
}

DI void mfma32_z4(f32x16& d, const bf16x8& a, const u32x4& b, const bf16x8& x, const bf16x8& y, const bf16x8& z) { asm volatile("v_mfma_f32_32x32x16_bf16 %0, %1, %2, 0" : "=&v"(d) : "v"(a), "v"(b), "v"(x), "v"(y), "v"(z)); }
DI void mfma32_vv4(f32x16& c, const bf16x8& a, const u32x4& b, const bf16x8& x, const bf16x8& y, const bf16x8& z) { asm volatile("v_mfma_f32_32x32x16_bf16 %0, %1, %2, %0" : "+v"(c) : "v"(a), "v"(b), "v"(x), "v"(y), "v"(z)); }
DI void mfma32_vv(f32x16& c, const bf16x8& a, const u32x4& b) { asm volatile("v_mfma_f32_32x32x16_bf16 %0, %1, %2, %0" : "+v"(c) : "v"(a), "v"(b)); }
struct K2E { u32x4 cx[16]; lds_cptr rc, rn; unsigned slotw, wsoff; srd_t sP; u32x4 pk; };
template <int CH, int g> DI void k2_gapC(K2C& c, K2E& e) {
  if constexpr (CH == 3 && g == 24) { asm volatile("s_waitcnt vmcnt(6)\n\ts_barrier" ::: "memory"); SBAR(); }
  if constexpr ((g & 15) == 0) mfma32_z4(g < 16 ? c.s0 : c.s1, c.fr[0], e.cx[0], c.fr[1], c.fr[2], c.fr[3]);
  else if constexpr ((g & 3) == 0) mfma32_vv4(g < 16 ? c.s0 : c.s1, c.fr[g & 15], e.cx[g & 15], c.fr[(g + 1) & 15], c.fr[(g + 2) & 15], c.fr[(g + 3) & 15]);
  else mfma32_vv(g < 16 ? c.s0 : c.s1, c.fr[g & 15], e.cx[g & 15]);
  SBAR();
  if constexpr (CH < 2 && g == 28) { asm volatile("s_barrier" ::: "memory"); SBAR(); }
  if constexpr (g < 24) c.fr[(g + 8) & 15] = lds_rd128(e.rc + (g + 8) * 1024);
  else if constexpr (CH < 6) c.fr[(g + 8) & 15] = lds_rd128(e.rn + (g - 24) * 1024);
  if constexpr (CH < 3 && g >= 8 && g < 16) k2_dma<g - 8>(c.dt, c.ldsb + e.slotw, c.vlane, c.sK, e.wsoff);
  constexpr int u = (g & 15) - 2;
  if constexpr (u >= 0 && u < 6 && (g >= 16 || CH > 0)) {
    const f32x16& t = g < 16 ? c.s1 : c.s0; constexpr int n = 2 * CH + (g < 16 ? -1 : 0), a = u / 3, i = u % 3;
    if constexpr (i < 2) { e.pk[2 * i] = cvtpk(t[8 * a + 4 * i], t[8 * a + 4 * i + 1]); e.pk[2 * i + 1] = cvtpk(t[8 * a + 4 * i + 2], t[8 * a + 4 * i + 3]); PINV(e.pk); }
    else st16w<(2 * n + a) * 1024>(e.pk, e.sP, c.vlane, 0u);
  }
  SBAR();
}
template <int CH, int... Gs> DI void k2_phaseC_(K2C& c, K2E& e, iseq<Gs...>) { (k2_gapC<CH, Gs>(c, e), ...); }
template <int CH> DI void k2_phaseC(K2C& c, K2E& e) { k2_phaseC_<CH>(c, e, std::make_integer_sequence<int, 32>{}); }

DI void phase_attn(const Params& P, char* lds, unsigned long long tag, const unsigned wid, u32x4 (&qo)[16]) {
  const char *KVW = P.ws + WS_K; char *Pf = P.ws + WS_PF, *part = P.ws + WS_PART; gu64* flags = (gu64*)(P.ws + WS_FL);
  const unsigned b = blockIdx.x, xcd = b & 7u, idx = b >> 3, h = 2u * xcd + (idx >> 4), pi = (idx >> 1) & 7u, roleB = idx & 1u;
  const unsigned pair = h * 8u + pi;
  const int NA = pi == 0 ? 18 : 17;
  const int n1 = roleB ? 32 - 2 * (int)pi - NA : NA, tb1 = roleB ? NA : 0, ntot = roleB ? 34 - NA : NA;
  const unsigned wbase = 2u * MB16 + h * (DH * DM * 2) + wid * 8192u;
  auto tile_off = [&](int p) AI -> unsigned { return h * (SEQ * DH * 2) + (unsigned)(p < n1 ? tb1 + p : p - n1) * 32768u + wid * 8192u; };
  auto k_off = [&](int p) AI -> unsigned { return p >= ntot ? wbase + (unsigned)(p - ntot) * 32768u : tile_off(p); };
  auto v_off = [&](int p) AI -> unsigned { return p >= ntot ? wbase + 2u * 32768u : MB16 + tile_off(p); };
  K2C c;
  c.ldsb = (unsigned)(size_t)lds + wid * 8192u; c.vlane = lane_id() * 16u;
  c.sK = mk_srd(KVW, 2u * MB16 + NH * DH * DM * 2);
  unsigned qb = 15u - pi;
  k2_dma8(c.ldsb, c.vlane, c.sK, k_off(0));
  ACC_CLAIM();
  if (roleB) {
    const srd_t sq = mk_srd(P.ws + ((size_t)pair * 4u + wid) * 16384u, 16384u);
    sfor<16>([&](auto I) AI { acc_load4<AQ + 4 * I, I * 1024>(sq, c.vlane); });
  }
  k2_dma8(c.ldsb + 32768u, c.vlane, c.sK, k_off(1)); k2_dma8(c.ldsb + 65536u, c.vlane, c.sK, v_off(0));
  if (roleB) sfor<16>([&](auto I) AI { acc_write4<AQ2 + 4 * I>(qo[I]); }); else sfor<16>([&](auto I) AI { acc_write4<AQ + 4 * I>(qo[I]); });
  SBAR();
  const unsigned lane = lane_id(), hi = lane >> 5, r32 = lane & 31u;
  const unsigned klane = ((r32 >> 3) & 1u) * 1024u + hi * 256u + ((r32 & 7u) + 8u * (r32 >> 4)) * 16u, vlane_r = (r32 >> 4) * 1024u + hi * 256u + (r32 & 15u) * 16u;
  c.dq = (int)r32 - 4 * (int)hi;
  c.kr = (lds_cptr)lds + klane; c.kslot_w = 0u; c.vslot_w = 32768u; c.vslot_r = 0u;
  asm volatile("s_waitcnt vmcnt(16)\n\ts_barrier" ::: "memory");
  int p = 0;
  c.sP = mk_srd(part + ((size_t)pair * 4u + wid) * 33792u, 33792u);
  const int p1 = roleB ? n1 - 1 : -8;
  int rel = 64 * tb1 - (int)(qb * 128u + wid * 32u);
  auto setup = [&]() AI { c.vsoff = v_off(p + 1); c.ksoff = k_off(p + 2); c.rel = rel; c.sw = p - p1; c.thr = c.sw == 1 ? -3e38f : THR; };
  auto advance = [&]() AI {
    c.kslot_w ^= 32768u; c.vslot_w = c.vslot_w == 65536u ? 0u : c.vslot_w + 32768u; rel += 64;
    c.vr = (lds_cptr)lds + 65536u + c.vslot_r + vlane_r; c.vslot_r = c.vslot_r == 65536u ? 0u : c.vslot_r + 32768u;
    if (p == p1) {
      sfor<16>([&](auto I) AI { acc_mov4<AQ + 4 * I, AQ2 + 4 * I>(); });
      c.negm = f32x16{}; PINV(c.negm); asm volatile("s_nop 1");
      rel = -(int)(pi * 128u + wid * 32u);
    }
    ++p;
  };
  c.negm = f32x16{}; PINV(c.negm); c.l0 = 0.f; c.l1 = 0.f; c.pend = false; c.fct = 1.f; c.mS = 0.f; c.lS = 0.f;
  asm volatile("s_nop 1"); SBAR();
  setup();
  k2_phaseA<false, true>(c); k2_phaseB<false, true>(c);
  advance();
  u32x2 f2 = {0u, 0u};
  int stop = roleB ? p1 + 2 : ntot;
  for (;;) {
#pragma nounroll
    while (p < stop) {
      setup();
      if (!roleB && p == ntot - 1) asm volatile("buffer_load_dwordx2 %0, off, %1, 0 sc1" : "=v"(f2) : "s"(mk_srd(P.ws + WS_FL + (FL_S2 + pair * 16u) * 8u, 8u)) : "memory");
      k2_phaseA<true, true>(c); k2_phaseB<true, true>(c);
      PINV(f2);
      advance();
    }
    if (p >= ntot) break;
    setup();
    k2_phaseA<true, true, true>(c); k2_phaseB<true, true, true>(c);
    if (wid == 0 && lane == 0) __hip_atomic_store(flags + FL_S2 + pair * 16u, tag, RLX_AGENT);
    advance();
    stop = ntot;
  }
  qb = roleB ? pi : qb;
  const bool early = !roleB && rfl(f2[0]) == (unsigned)tag && rfl(f2[1]) == (unsigned)(tag >> 32);
  SBAR();
  k2_phaseA<true, false>(c);
  k2_dma8(c.ldsb + 65536u + c.vslot_w, c.vlane, c.sK, wbase + 3u * 32768u);
  if (early) { c.ml = __builtin_bit_cast(f32x4, __builtin_amdgcn_raw_buffer_load_b128(c.sP, c.vlane, 32 * 1024, 16)); SBAR(); k2_phaseB<true, false, true>(c); } else k2_phaseB<true, false>(c);
  MFMA_DRAIN(); SBAR();
  float l = c.l0 + c.l1;
  { auto pr = __builtin_amdgcn_permlane32_swap(__float_as_uint(l), __float_as_uint(l), false, false); l = __uint_as_float(pr[0]) + __uint_as_float(pr[1]); }
  K2E e;
  if (roleB) {
    const float rl = 1.0f / l;
    sfor<16>([&](auto I) AI { e.cx[I] = pack8(acc_read4<8 * I>(), acc_read4<8 * I + 4>(), rl); PINV(e.cx[I]); });
  } else {
    if (!early) {
      wait_flags(flags + FL_S2 + pair * 16u, tag);
      c.ml = __builtin_bit_cast(f32x4, __builtin_amdgcn_raw_buffer_load_b128(c.sP, c.vlane, 32 * 1024, 16));
      sfor<32>([&](auto K) AI { c.ob[K] = __builtin_bit_cast(f32x4, __builtin_amdgcn_raw_buffer_load_b128(c.sP, c.vlane, K * 1024, 16)); });
    }
    const f32x4 ml = c.ml;
    SBAR();
    const float mm = fmaxf(c.m, ml[0]), wa = __builtin_amdgcn_exp2f(c.m - mm), wb = __builtin_amdgcn_exp2f(ml[0] - mm);
    const float rl = 1.0f / (wa * l + wb * ml[1]), fa = wa * rl, fb = wb * rl;
    sfor<16>([&](auto I) AI { e.cx[I] = pack8(acc_read4<8 * I>() * fa + c.ob[2 * I] * fb, acc_read4<8 * I + 4>() * fa + c.ob[2 * I + 1] * fb, 1.0f); PINV(e.cx[I]); });
  }
  e.sP = mk_srd(Pf + ((size_t)h * 64u + qb * 4u + wid) * 28672u, 28672u);
  const unsigned kA = c.kslot_w, kB = c.kslot_w ^ 32768u;
  const unsigned v2 = 65536u + c.vslot_r, v3 = 65536u + c.vslot_w, v4 = 65536u + (98304u - c.vslot_r - c.vslot_w);
  const lds_cptr lb = (lds_cptr)lds + c.vlane;
  asm volatile("s_waitcnt vmcnt(0)\n\ts_barrier" ::: "memory");
  e.rc = lb + kA;
#pragma unroll
  for (int i = 0; i < 8; ++i) c.fr[i] = lds_rd128(e.rc + i * 1024);
  e.rn = lb + kB; e.slotw = v4; e.wsoff = wbase + 4u * 32768u; SBAR(); k2_phaseC<0>(c, e);
  e.rc = e.rn; e.rn = lb + v2; e.slotw = kA; e.wsoff = wbase + 5u * 32768u; SBAR(); k2_phaseC<1>(c, e);
  e.rc = e.rn; e.rn = lb + v3; e.slotw = kB; e.wsoff = wbase + 6u * 32768u; SBAR(); k2_phaseC<2>(c, e);
  e.rc = e.rn; e.rn = lb + v4; SBAR(); k2_phaseC<3>(c, e);
  e.rc = e.rn; e.rn = lb + kA; SBAR(); k2_phaseC<4>(c, e);
  e.rc = e.rn; e.rn = lb + kB; SBAR(); k2_phaseC<5>(c, e);
  e.rc = e.rn; SBAR(); k2_phaseC<6>(c, e);
  MFMA_DRAIN(); PINV(c.s1); SBAR();
  st16w<26 * 1024>(pack8w(c.s1, 0, 1.0f), e.sP, c.vlane, 0u); st16w<27 * 1024>(pack8w(c.s1, 1, 1.0f), e.sP, c.vlane, 0u);
}
constexpr int OUTW = 2 * DM;
DI void reduce_load(u32x4 (&v)[16], unsigned task, srd_t sP, unsigned lane) {
#pragma unroll
  for (int h = 0; h < 16; ++h) v[h] = __builtin_amdgcn_raw_buffer_load_b128(sP, lane * 16u, task * 1024u + h * (64u * 28672u), 16);
}
DI void reduce_sum(const u32x4 (&v)[16], unsigned task, unsigned lane, unsigned short* out) {
  const unsigned rb = task / 28u, f = task % 28u;
  float s[8];
#pragma unroll
  for (int i = 0; i < 8; ++i) {
    float a[16];
#pragma unroll
    for (int h = 0; h < 16; ++h) a[h] = (i & 1) ? __uint_as_float(v[h][i >> 1] & 0xffff0000u) : __uint_as_float(v[h][i >> 1] << 16);
    s[i] = (((a[0] + a[1]) + (a[2] + a[3])) + ((a[4] + a[5]) + (a[6] + a[7]))) + (((a[8] + a[9]) + (a[10] + a[11])) + ((a[12] + a[13]) + (a[14] + a[15])));
  }
  *(u32x4*)(out + (size_t)(32u * rb + (lane & 31u)) * OUTW + 16u * f + 8u * (lane >> 5)) = (u32x4){cvtpk(s[0], s[1]), cvtpk(s[2], s[3]), cvtpk(s[4], s[5]), cvtpk(s[6], s[7])};
}

constexpr int HCOPY = 4160, HCH = 4 * HCOPY, XROW = 80, XCH = 96 * XROW;
constexpr int SSM_LDS = 98304, XOFF = SSM_LDS + 2 * HCH;
struct K3S { lds_cptr ha, xa; f32x16 y0, y1; s16x4 al[4][2], ah[4][2]; bf16x8 bx[4][2]; };
template <int e> DI void k3_ssm_rd(K3S& c) {
  constexpr int s = e & 3;
  c.al[s][0] = *(const volatile LAS s16x4*)(c.ha + e * 64); c.ah[s][0] = *(const volatile LAS s16x4*)(c.ha + e * 64 + 8);
  c.bx[s][0] = lds_rd128(c.xa + (63 - e) * XROW); c.bx[s][1] = lds_rd128(c.xa + (31 - e) * XROW);
  c.al[s][1] = *(const volatile LAS s16x4*)(c.ha + (e + 32) * 64); c.ah[s][1] = *(const volatile LAS s16x4*)(c.ha + (e + 32) * 64 + 8);
}
template <int e, class F> DI void k3_ssm_step(K3S& c, F&& f) {
  constexpr int s = e & 3;
  if constexpr (e + 2 < 32) k3_ssm_rd<e + 2>(c);
  SBAR();
  const bf16x8 a0 = cat(c.al[s][0], c.ah[s][0]), a1 = cat(c.al[s][1], c.ah[s][1]);
  c.y1 = MFMA32(a0, c.bx[s][0], c.y1); c.y0 = MFMA32(a0, c.bx[s][1], c.y0); c.y1 = MFMA32(a1, c.bx[s][1], c.y1);
  SBAR();
  f(std::integral_constant<int, e>{});
  SBAR();
}
DI void ssm_load(SsmS& s, const Params& P, unsigned blk, unsigned long long tag, const unsigned wid) {
  const unsigned lane = lane_id(), tid = wid * 64u + lane;
  s.c0 = 2u * (28u * (blk & 7u) + (blk >> 3));
  if (!__all((((unsigned long long)s.f0[1] << 32) | s.f0[0]) == tag))
    wait_flags((gu64*)(P.ws + WS_FL) + FL_S0 + (7u * (lane & 31u) + (s.c0 >> 6)) * 16u, tag);
  const srd_t sx = mk_srd(P.ws + WS_XT, SEQ * DM * 2);
#pragma unroll
  for (int ch = 0; ch < 2; ++ch) s.xw[ch] = __builtin_amdgcn_raw_buffer_load_b128(sx, (((tid >> 3) * DM + s.c0 + ch) * 64u + 8u * (tid & 7u)) * 2u, 0, 16);
  s.dw = *(const unsigned*)(P.ssmd + s.c0);
}
DI void ssm_build(SsmS& s, char* lds, const unsigned wid) {
  const unsigned tid = wid * 64u + lane_id();
#pragma unroll
  for (int ch = 0; ch < 2; ++ch) {
    const unsigned w[6] = {s.hA[ch][0], s.hA[ch][1], s.hA[ch][2], s.hA[ch][3], s.hB[ch][0], s.hB[ch][1]};
    unsigned o[5];
#pragma unroll
    for (int k = 0; k < 5; ++k) o[k] = __builtin_amdgcn_alignbyte(w[k + 1], w[k], 2);
    LAS char* dst = (LAS char*)lds + SSM_LDS + ch * HCH + tid * 16u + 64u;
    *(LAS u32x4*)(dst) = (u32x4){o[0], o[1], o[2], o[3]};
    *(LAS u32x4*)(dst + HCOPY) = (u32x4){w[1], w[2], w[3], w[4]};
    *(LAS u32x4*)(dst + 2 * HCOPY) = (u32x4){o[1], o[2], o[3], o[4]};
    *(LAS u32x4*)(dst + 3 * HCOPY) = (u32x4){w[2], w[3], w[4], w[5]};
    if (tid < 4u) {
      const unsigned z0 = tid ? 0u : w[0], z1 = tid ? 0u : w[1], zo = tid ? 0u : o[0];
      LAS char* d2 = (LAS char*)lds + SSM_LDS + ch * HCH + (3u - tid) * 16u;
      *(LAS u32x4*)(d2) = (u32x4){0u, 0u, 0u, z0 << 16};
      *(LAS u32x4*)(d2 + HCOPY) = (u32x4){0u, 0u, 0u, z0};
      *(LAS u32x4*)(d2 + 2 * HCOPY) = (u32x4){0u, 0u, z0 << 16, zo};
      *(LAS u32x4*)(d2 + 3 * HCOPY) = (u32x4){0u, 0u, z0, z1};
    }
    LAS char* xb = (LAS char*)lds + XOFF + ch * XCH;
    if (tid < 160u) *(LAS u32x4*)(xb + tid * 16u) = (u32x4){0u, 0u, 0u, 0u};
  }
#pragma unroll
  for (int ch = 0; ch < 2; ++ch) *(LAS u32x4*)((LAS char*)lds + XOFF + ch * XCH + (32u + (tid >> 2)) * XROW + (tid & 3u) * 16u) = s.xw[ch];
}
template <class F> DI void ssm_conv(SsmS& s, char* lds, const unsigned wid, F&& f) {
  const unsigned lane = lane_id(), hi = lane >> 5, r = lane & 31u;
  const unsigned ch = wid >> 1, dh = wid & 1u;
  K3S c;
  c.ha = (lds_cptr)lds + SSM_LDS + ch * HCH + (r & 3u) * HCOPY + (24u - 16u * dh - 8u * hi + 4u * (r >> 2)) * 2u; PINV(c.ha);
  c.xa = (lds_cptr)lds + XOFF + ch * XCH + (r + 1u) * XROW + (2u * dh + hi) * 16u; PINV(c.xa);
  c.y0 = f32x16{}; c.y1 = f32x16{};
  k3_ssm_rd<0>(c); k3_ssm_rd<1>(c);
  sfor<32>([&](auto E) AI { k3_ssm_step<E>(c, f); });
  s.y0 = c.y0; s.y1 = c.y1;
}
DI void ssm_finish(SsmS& s, const Params& P, char* lds, const unsigned wid) {
  const unsigned lane = lane_id(), tid = wid * 64u + lane, hi = lane >> 5, r = lane & 31u;
  LAS char* ys = (LAS char*)lds + SSM_LDS + wid * 8192u;
#pragma unroll
  for (int k = 0; k < 4; ++k) {
    const unsigned o = r * 128u + (((2u * k + hi) ^ (r & 7u)) << 4);
    *(LAS f32x4*)(ys + o) = (f32x4){s.y0[4 * k], s.y0[4 * k + 1], s.y0[4 * k + 2], s.y0[4 * k + 3]};
    *(LAS f32x4*)(ys + 4096u + o) = (f32x4){s.y1[4 * k], s.y1[4 * k + 1], s.y1[4 * k + 2], s.y1[4 * k + 3]};
  }
  asm volatile("s_waitcnt lgkmcnt(0)\n\ts_barrier" ::: "memory");
  const unsigned row = tid >> 2, yo0 = row * 128u + (((2u * (tid & 3u)) ^ (row & 7u)) << 4), yo1 = row * 128u + (((2u * (tid & 3u) + 1u) ^ (row & 7u)) << 4);
  const srd_t sz = mk_srd(P.ws + WS_ZT, SEQ * DM * 2);
  sfor<2>([&](auto C) AI {
    const LAS char* yp = (const LAS char*)lds + SSM_LDS + C * 16384;
    const f32x4 a = *(const LAS f32x4*)(yp + yo0) + *(const LAS f32x4*)(yp + 8192 + yo0), bb = *(const LAS f32x4*)(yp + yo1) + *(const LAS f32x4*)(yp + 8192 + yo1);
    const float dd = bf2f(C ? s.dw >> 16 : s.dw & 0xffffu);
    u32x4 z;
#pragma unroll
    for (int i = 0; i < 4; ++i) {
      const f32x2 kv = up2(s.xw[C][3 - i]), y = rb2(i < 2 ? (f32x2){a[2 * i], a[2 * i + 1]} : (f32x2){bb[2 * i - 4], bb[2 * i - 3]}), kd = rb2((f32x2){kv[1] * dd, kv[0] * dd});
      z[i] = cvtpk(y[0] + kd[0], y[1] + kd[1]);
    }
    st16w<C * 128>(z, sz, (((tid >> 3) * DM + s.c0) * 64u + 8u * (tid & 7u)) * 2u, 0u);
  });
}
DI void ssm_gate(const Params& P, unsigned blk, unsigned long long tag, const unsigned wid) {
  const unsigned lane = lane_id(), tid = wid * 64u + lane, bc0 = 64u * (blk % 7u) + 2u * (tid & 31u), t0 = 64u * (blk / 7u) + 8u * (tid >> 5);
  unsigned qw[8];
#pragma unroll
  for (int i = 0; i < 8; ++i) qw[i] = *(const unsigned*)(P.bq + (size_t)(t0 + i) * DM + bc0);
  { const unsigned pr = 32u * (blk % 7u) + (lane & 31u);
    wait_flags((gu64*)(P.ws + WS_FL) + FL_S1 + (pr / 28u + 8u * (pr % 28u)) * 16u, tag); }
  const srd_t sz = mk_srd(P.ws + WS_ZT, SEQ * DM * 2);
  const unsigned zo = (((blk / 7u) * DM + bc0) * 64u + 8u * (tid >> 5)) * 2u;
  const u32x4 z0 = __builtin_amdgcn_raw_buffer_load_b128(sz, zo, 0, 16), z1 = __builtin_amdgcn_raw_buffer_load_b128(sz, zo + 128u, 0, 16);
#pragma unroll
  for (int i = 0; i < 8; ++i) {
    const f32x2 q = up2(qw[i]), a = up2(z0[i >> 1]), bb = up2(z1[i >> 1]);
    *(unsigned*)(P.out + (size_t)(t0 + i) * OUTW + DM + bc0) = cvtpk(q[0] * a[i & 1], q[1] * bb[i & 1]);
  }
}

__global__ __launch_bounds__(256, 1) void k1_fused(Params P) {
  extern __shared__ __attribute__((aligned(16))) char lds[];
  const unsigned wid = rfl(threadIdx.x >> 6);
  const unsigned b = blockIdx.x, xcd = b & 7u, idx = b >> 3, h = 2u * xcd + (idx >> 4), j = idx & 15u;
  if (wid == 3) {
    unsigned long long pc; unsigned len;
    asm volatile("s_getpc_b64 %0\n.Lcode_begin:\n\ts_mov_b32 %1, .Lcode_end-.Lcode_begin" : "=s"(pc), "=s"(len));
    const unsigned off = (idx * 64u + lane_id()) * 64u;
    const char* a = (const char*)pc + (off < len ? off : 0u);
    asm volatile("s_add_u32 m0, %0, 147456\n\ts_nop 0\n\tglobal_load_lds_dword %1, off" :: "s"((unsigned)(size_t)lds), "v"(a) : "memory", "scc");
  }
  gu64* flags = (gu64*)(P.ws + WS_FL);
  const unsigned qb = (j & 1u) ? (j >> 1) : 15u - (j >> 1);
  const bool ssm = b < 224u;
  u32x4 qo[16]; SsmS s;
  const unsigned long long tag = phase_proj(P, lds, wid, qb, qo, s);
  if (ssm) ssm_load(s, P, b, tag, wid);
  SBAR();
  {
    const unsigned lane = lane_id(), vl = lane * 16u;
    const unsigned q = (lane & 15u) >> 2, p = lane & 3u;
    const lds_cptr rp = (lds_cptr)lds + WO_LDS + wid * 4096u + (4u * (lane >> 5) + q) * 64u + 32u * ((lane >> 4) & 1u) + 16u * (p & 1u) + 8u * (p >> 1);
    bf16x8 wf[4];
    sfor<4>([&](auto J) AI { wf[J] = cat(lds_tr(rp + J * 1024), lds_tr(rp + J * 1024 + 512)); });
    const srd_t sw_ = mk_srd(P.ws + WS_W + (size_t)h * (DH * DM * 2), DH * DM * 2);
    const srd_t sq = mk_srd(P.ws + ((size_t)(h * 8u + (j >> 1)) * 4u + wid) * 16384u, (j & 1u) ? 0u : 16384u);
    auto deferred = [&](auto E) AI {
      constexpr int k = E >> 1;
      if constexpr (E & 1) st16w<k * 1024>(qo[k], sq, vl, 0u);
      else if constexpr (k < 12) { if constexpr (k < 4 * LPG) { constexpr int u = q_lunit(k), i = u >> 2, jj = u & 3; st16wa<(i & 1) * 1024>(s.late[k], s.sO, vl, s.so + (i >> 1) * s.sA + jj * s.sB); } }
      else st16w<(k - 12) * 65536u>(__builtin_bit_cast(u32x4, wf[k - 12]), sw_, vl, (wid * 16u + j) * 1024u);
    };
    if (ssm) {
      ssm_build(s, lds, wid);
      asm volatile("s_waitcnt lgkmcnt(0)\n\ts_barrier" ::: "memory");
      ssm_conv(s, lds, wid, deferred);
      asm volatile("s_barrier" ::: "memory");
      ssm_finish(s, P, lds, wid);
    } else sfor<32>(deferred);
  }
  publish(flags + FL_S1 + b * 16u, tag, wid);
  if (wid == 0) wait_flags(flags + FL_S1 + (xcd + 8u * ((idx & 16u) + (lane_id() & 15u))) * 16u, tag);
  asm volatile("s_barrier" ::: "memory");
  phase_attn(P, lds, tag, wid, qo);
  publish(flags + FL_S3 + b * 16u, tag, wid);
  if (!(j & 1u) && j >= 2u) { const unsigned g0 = 2u * (7u * h + (j >> 1) - 1u); ssm_gate(P, g0, tag, wid); ssm_gate(P, g0 + 1u, tag, wid); }
  const unsigned lane = lane_id();
  if (wid == 0) wait_flags(flags + FL_S3 + ((lane & 7u) + 8u * (16u * ((lane >> 3) & 1u) + j)) * 16u, tag);
  asm volatile("s_barrier" ::: "memory");
  if (b == 0 && wid == 0 && lane == 0) __hip_atomic_store(flags + FL_EPOCH, tag, RLX_AGENT);
  const srd_t sP = mk_srd(P.ws + WS_PF, NH * 64u * 28672u);
  const unsigned task = qb * 112u + h * 7u + wid;
  u32x4 va[16], vb[16];
  reduce_load(va, task, sP, lane);
  if (wid < 3u) reduce_load(vb, task + 4u, sP, lane);
  SBAR();
  reduce_sum(va, task, lane, P.out);
  if (wid < 3u) reduce_sum(vb, task + 4u, lane, P.out);
  asm volatile(".Lcode_end:");
}

extern "C" void kernel_launch(void* const* d_in, const int* in_sizes, int n_in, void* d_out, int out_size, void* d_ws, size_t ws_size, hipStream_t stream) {
  typedef const unsigned short* cu16;
  Params P = {(cu16)d_in[0], (cu16)d_in[1], (cu16)d_in[2], (cu16)d_in[3], (cu16)d_in[4], (cu16)d_in[5], (cu16)d_in[6], (cu16)d_in[7], (cu16)d_in[8], (cu16)d_in[9], (cu16)d_in[10], (cu16)d_in[11],
              (unsigned short*)d_out, (char*)d_ws};
  (void)hipFuncSetAttribute((const void*)k1_fused, hipFuncAttributeMaxDynamicSharedMemorySize, 163840);
  k1_fused<<<256, 256, 163840, stream>>>(P);
}
```

```cpp
#include <hip/hip_runtime.h>
#include <hip/hip_bf16.h>
#include <utility>

typedef short    bf16x8 __attribute__((ext_vector_type(8)));
typedef short    s16x4  __attribute__((ext_vector_type(4)));
typedef float    f32x16 __attribute__((ext_vector_type(16)));
typedef float    f32x4  __attribute__((ext_vector_type(4)));
typedef float    f32x2  __attribute__((ext_vector_type(2)));
typedef __bf16   bf16x2 __attribute__((ext_vector_type(2)));
typedef unsigned u32x4  __attribute__((ext_vector_type(4)));
typedef unsigned u32x2  __attribute__((ext_vector_type(2)));
#define DI __device__ __forceinline__
#define AI __attribute__((always_inline))
#define LAS __attribute__((address_space(3)))
#define SBAR() __builtin_amdgcn_sched_barrier(0)
template <class T> DI void PINV(T& x) { asm volatile("" : "+v"(x)); }
template <class T> DI void PINA(T& x) { asm volatile("" : "+a"(x)); }
#define MFMA32(a, b, c) __builtin_amdgcn_mfma_f32_32x32x16_bf16((a), (b), (c), 0, 0, 0)
#define MFMA_DRAIN() asm volatile("s_nop 7\n\ts_nop 7" ::: "memory")
typedef LAS const char* lds_cptr;
typedef __amdgpu_buffer_rsrc_t srd_t;
template <int... Is> using iseq = std::integer_sequence<int, Is...>;
template <int N> using mkseq = std::make_integer_sequence<int, N>;

DI unsigned rfl(unsigned x) { return __builtin_amdgcn_readfirstlane(x); }
DI unsigned lane_id() { unsigned l; asm volatile("v_mbcnt_lo_u32_b32 %0, -1, 0\n\tv_mbcnt_hi_u32_b32 %0, -1, %0" : "=v"(l)); return l; }
DI unsigned cvtpk(float lo, float hi) { f32x2 v = {lo, hi}; bf16x2 b = __builtin_convertvector(v, bf16x2); return __builtin_bit_cast(unsigned, b); }
DI float bf2f(unsigned h) { return __uint_as_float(h << 16); }
DI float rbf(float x) { return __uint_as_float(cvtpk(x, x) << 16); }
DI bf16x8 lds_rd128(lds_cptr p) { return *(const LAS bf16x8*)p; }
DI s16x4 lds_tr(lds_cptr p) { return __builtin_amdgcn_ds_read_tr16_b64_v4i16((LAS s16x4*)p); }
DI bf16x8 cat(s16x4 l, s16x4 h) { return __builtin_shufflevector(l, h, 0, 1, 2, 3, 4, 5, 6, 7); }
DI srd_t mk_srd(const void* p, unsigned bytes) { return __builtin_amdgcn_make_buffer_rsrc((void*)p, (short)0, (int)bytes, 0x00020000); }
template <unsigned MI, unsigned SI> DI void dma16(unsigned mb, unsigned voff, srd_t srd, unsigned sb) {
  unsigned t;
  asm volatile("s_add_u32 m0, %1, %5\n\ts_add_u32 %0, %4, %6\n\tbuffer_load_dwordx4 %2, %3, %0 offen lds" : "=&s"(t) : "s"(mb), "v"(voff), "s"(srd), "s"(sb), "n"(MI), "n"(SI) : "memory", "scc");
}
template <unsigned SI> DI void st16w(u32x4 v, srd_t srd, unsigned voff, unsigned sb) {
  unsigned t; asm volatile("s_add_u32 %0, %4, %5\n\tbuffer_store_dwordx4 %1, %2, %3, %0 offen sc1\n\ts_nop 1" : "=&s"(t) : "v"(v), "v"(voff), "s"(srd), "s"(sb), "n"(SI) : "memory", "scc"); }
template <unsigned SI> DI void st16wa(f32x4 v, srd_t srd, unsigned voff, unsigned sb) {
  unsigned t; asm volatile("s_add_u32 %0, %4, %5\n\tbuffer_store_dwordx4 %1, %2, %3, %0 offen sc1\n\ts_nop 1" : "=&s"(t) : "a"(v), "v"(voff), "s"(srd), "s"(sb), "n"(SI) : "memory", "scc"); }
DI void ld4(unsigned& d, srd_t srd, unsigned voff) { asm volatile("buffer_load_dword %0, %1, %2, 0 offen" : "=v"(d) : "v"(voff), "s"(srd) : "memory"); }
template <unsigned SI> DI void ld16a(bf16x8& d, srd_t srd, unsigned voff) { unsigned t; asm volatile("s_mov_b32 %1, %4\n\tbuffer_load_dwordx4 %0, %2, %3, %1 offen sc1" : "=a"(d), "=&s"(t) : "v"(voff), "s"(srd), "n"(SI) : "memory"); }
template <int N, class F> DI void sfor_(F&& f, iseq<>) {}
template <int N, class F, int I, int... Is> DI void sfor_(F&& f, iseq<I, Is...>) { f(std::integral_constant<int, I>{}); sfor_<N>(f, iseq<Is...>{}); }
template <int N, class F> DI void sfor(F&& f) { sfor_<N>(f, mkseq<N>{}); }
DI u32x4 pack8(const f32x4& a, const f32x4& b, float sc) { u32x4 w = {cvtpk(a[0] * sc, a[1] * sc), cvtpk(a[2] * sc, a[3] * sc), cvtpk(b[0] * sc, b[1] * sc), cvtpk(b[2] * sc, b[3] * sc)}; return w; }
DI u32x4 pack8w(const f32x16& a, int s, float sc) {
  u32x4 w = {cvtpk(a[8 * s] * sc, a[8 * s + 1] * sc), cvtpk(a[8 * s + 2] * sc, a[8 * s + 3] * sc), cvtpk(a[8 * s + 4] * sc, a[8 * s + 5] * sc), cvtpk(a[8 * s + 6] * sc, a[8 * s + 7] * sc)};
  return w;
}
constexpr int SEQ = 2048, NH = 16, DH = 256, DM = 448, DC = 1024;
constexpr unsigned WRS = NH * DH * 2;
constexpr unsigned MB16 = SEQ * NH * DH * 2;
constexpr float QSCALE = 1.4426950408889634f / 16.0f;

DI unsigned code_off() { return ((blockIdx.x >> 3) * 64u + lane_id()) * 64u; }
typedef __attribute__((address_space(1))) unsigned long long gu64;
#define RLX_AGENT __ATOMIC_RELAXED, __HIP_MEMORY_SCOPE_AGENT
struct Params {
  const unsigned short *x, *ckv, *wq, *wuk, *wuv, *wo, *bq, *bk, *bv, *shk, *ssmk, *ssmd;
  unsigned short* out; char* ws;
};
struct Ctx { srd_t code; };
constexpr size_t WS_K = MB16, WS_W = 3 * (size_t)MB16, WS_XT = WS_W + (4u << 20), WS_ZT = WS_XT + (2u << 20), WS_FL = WS_ZT + (2u << 20), WS_PART = WS_FL + (1u << 20), WS_PF = WS_PART + (17u << 20);
constexpr int FL_EPOCH = 0, FL_S0 = 16, FL_S1 = FL_S0 + 256 * 16, FL_S2 = FL_S1 + 256 * 16, FL_S3 = FL_S2 + 128 * 16;
DI void publish(gu64* flag, unsigned long long tag, unsigned wid) {
  asm volatile("s_waitcnt vmcnt(0)\n\ts_barrier" ::: "memory");
  if (wid == 0 && lane_id() == 0) __hip_atomic_store(flag, tag, RLX_AGENT);
}
DI void wait_flags(gu64* lane_flag, unsigned long long tag) {
  for (int spin = 0; spin < (1 << 20); ++spin) {
    if (__all(__hip_atomic_load(lane_flag, RLX_AGENT) == tag)) break;
    __builtin_amdgcn_s_sleep(1);
  }
}

DI void mfma16a(f32x4& c, const bf16x8& a, const bf16x8& b) { asm volatile("v_mfma_f32_16x16x32_bf16 %0, %1, %2, %0" : "+a"(c) : "v"(a), "v"(b)); }
DI void mfma32q(f32x16& c, const bf16x8& a, const bf16x8& b) { asm volatile("v_mfma_f32_32x32x16_bf16 %0, %1, %2, %0" : "+v"(c) : "v"(a), "v"(b)); }
DI void mfma32qz(f32x16& c, const bf16x8& a, const bf16x8& b) { asm volatile("v_mfma_f32_32x32x16_bf16 %0, %1, %2, 0" : "=&v"(c) : "v"(a), "v"(b)); }
DI f32x2 up2(unsigned w) { f32x2 r = {__uint_as_float(w << 16), __uint_as_float(w & 0xffff0000u)}; return r; }
DI f32x2 rb2(f32x2 v) { return up2(cvtpk(v[0], v[1])); }
constexpr unsigned qxs(int s) { return 16384u * s; }
constexpr unsigned qws(int s) { return s == 0 ? 65536u : s == 1 ? 131072u : 98304u; }
constexpr unsigned WO_LDS = 49152u;
struct SsmS { u32x4 xw[2], hA[2]; u32x2 hB[2], f0; unsigned dw, c0; f32x16 y0, y1; f32x4 late[12]; srd_t sO; unsigned so, sA, sB; };
struct Frags { bf16x8 x[8]; s16x4 wl[8], wh[8]; };
struct K1C {
  unsigned ldsb, lds8, lds4, wid, vlane, b, hb;
  unsigned vx_kv[2], vx_q[2], vw[4], vwq[2];
  srd_t sX, sW, sXq, sWq, sO;
  unsigned sx_kv, sw_kv, sx_q, sw_q, so;
  lds_cptr xr_kv[2], wr[8], xq[4], wq[4], wq1[4];
  const char* pf; unsigned pfstep;
  f32x4 acc[8][8]; Frags f0, f1; f32x4 ta, tb; u32x4 pk;
  f32x16 qa[8]; s16x4 ql[16], qh[16]; bf16x8 qx[4];
  unsigned kw[11], vv[8], kv[8]; u32x4 sk; u32x2 ep; f32x2 kf[11], sc[4], pa, pt;
  unsigned long long tag; SsmS* ss; srd_t scode;
};
DI void touch_lds(unsigned ldsb, srd_t srd, unsigned voff) {
  asm volatile("s_add_u32 m0, %0, 147456\n\ts_nop 0\n\tbuffer_load_dword %1, %2, 0 offen lds" :: "s"(ldsb), "v"(voff), "s"(srd) : "memory", "scc"); }
DI void k1_touch(const K1C& c) {
  asm volatile("s_add_u32 m0, %0, 147456\n\ts_nop 0\n\tglobal_load_lds_dword %1, off" :: "s"(c.ldsb), "v"(c.pf) : "memory", "scc"); }
template <int buf, int j> DI void k1_piece(const K1C& c, unsigned sw, unsigned sx) {
  constexpr int p = j >> 1;
  if constexpr (!(j & 1)) dma16<buf * 32768u + 65536u + p * 1024u, 2u * p * WRS>(c.lds8, c.vw[(p & 1) + 2 * (p >> 2)], c.sW, sw);
  else dma16<buf * 32768u + p * 1024u, 8u * p * (DC * 2)>(c.lds8, c.vx_kv[(p >> 1) & 1], c.sX, sx);
}
template <int S, int j> DI void k1_qpiece(const K1C& c) {
  if constexpr (j % 3 != 1) { constexpr int p = 2 * (j / 3) + (j % 3 == 2); dma16<qws(S % 3) + p * 1024u, (64u * S + 2u * p) * WRS>(c.lds8, c.vwq[p & 1], c.sWq, c.sw_q); }
  else { constexpr int p = j / 3; dma16<qxs(S % 3) + p * 1024u, 8u * p * (DM * 2) + 128u * S>(c.lds4, c.vx_q[p & 1], c.sXq, c.sx_q); }
}
template <int buf> DI void k1_stage(const K1C& c, int st) { sfor<16>([&](auto J) AI { k1_piece<buf, J>(c, c.sw_kv + (unsigned)st * (64u * WRS), c.sx_kv + (unsigned)st * 128u); }); }
template <int buf, int ks, int i> DI void k1_read(const K1C& c, Frags& f) {
  constexpr int so = buf * 32768;
  if constexpr (i < 2 || i >= 10) { constexpr int k = i < 2 ? i : i - 8, nb = k >> 1, t = k & 1;
    const s16x4 v = lds_tr(c.wr[nb] + so + ks * 16384 + t * 2048); if constexpr (t) f.wh[nb] = v; else f.wl[nb] = v; }
  else { constexpr int mb = i - 2; f.x[mb] = lds_rd128(c.xr_kv[ks] + so + (mb >> 1) * 4096 + (mb & 1) * 1024); }
}
template <int F> DI void k1_qw(K1C& c) {
  constexpr int T = F >> 5, ks = (F >> 3) & 3, nb = F & 7, sl = T % 3, o = (sl == 2 ? 32768 : 0) + ks * 8192 + (nb >> 2) * 256;
  const lds_cptr p = sl == 1 ? c.wq1[nb & 3] : c.wq[nb & 3];
  c.ql[F & 15] = lds_tr(p + o); c.qh[F & 15] = lds_tr(p + o + 2048);
}
template <int T, int ks> DI void k1_qx(K1C& c) { c.qx[ks] = lds_rd128(c.xq[ks] + qxs(T % 3)); }
template <int m> DI void k1_pre(K1C& c, const Params& P) {
  if constexpr (m == 0) {
    asm volatile("" : "+v"(c.kw[0]), "+v"(c.kw[1]), "+v"(c.kw[2]), "+v"(c.kw[3]), "+v"(c.kw[4]), "+v"(c.kw[5]), "+v"(c.kw[6]), "+v"(c.kw[7]), "+v"(c.kw[8]), "+v"(c.kw[9]), "+v"(c.kw[10]),
                 "+v"(c.vv[0]), "+v"(c.vv[1]), "+v"(c.vv[2]), "+v"(c.vv[3]), "+v"(c.vv[4]), "+v"(c.vv[5]), "+v"(c.vv[6]), "+v"(c.vv[7]), "+v"(c.sk), "+v"(c.ep));
    const unsigned long long e0 = ((unsigned long long)rfl(c.ep[1]) << 32) | rfl(c.ep[0]);
    c.tag = (e0 ^ 0x9E3779B97F4A7C15ull) * 0xD1342543DE82EF95ull + 1ull;
  } else if constexpr (m < 12) { c.kf[m - 1] = up2(c.kw[m - 1]); PINV(c.kf[m - 1]); }
  else if constexpr (m < 14) { constexpr int i = m - 12;
    c.sc[2 * i] = (f32x2){bf2f(c.sk[i] & 0xffffu), bf2f(c.sk[2 + i] & 0xffffu)}; c.sc[2 * i + 1] = (f32x2){bf2f(c.sk[i] >> 16), bf2f(c.sk[2 + i] >> 16)}; PINV(c.sc[2 * i]); PINV(c.sc[2 * i + 1]); }
  else if constexpr (m < 78) { constexpr int i = (m - 14) >> 3, s = (m - 14) & 7;
    if constexpr (s == 0) { c.pa = rb2(c.kf[i] * c.sc[3]); PINV(c.pa); }
    else if constexpr (s == 7) { const f32x2 a = c.pa * up2(c.vv[i]); c.kv[i] = cvtpk(a[0], a[1]); PINV(c.kv[i]); }
    else if constexpr (s & 1) { c.pt = rb2(c.kf[i + (s + 1) / 2] * c.sc[3 - (s + 1) / 2]); PINV(c.pt); }
    else { c.pa = rb2(c.pa + c.pt); PINV(c.pa); }
  } else {
    constexpr unsigned sel = m == 78 ? 0x05040100u : 0x07060302u;
    const unsigned tid = c.wid * 64u + (c.vlane >> 4), xo = (((c.b / 7u) * DM + 64u * (c.b % 7u) + 2u * (tid & 31u)) * 64u + 8u * (tid >> 5)) * 2u;
    st16w<(m - 78) * 128>((u32x4){__builtin_amdgcn_perm(c.kv[6], c.kv[7], sel), __builtin_amdgcn_perm(c.kv[4], c.kv[5], sel), __builtin_amdgcn_perm(c.kv[2], c.kv[3], sel), __builtin_amdgcn_perm(c.kv[0], c.kv[1], sel)},
                          mk_srd(P.ws + WS_XT, SEQ * DM * 2), xo, 0u);
  }
}
template <int i> DI void k1_preload(K1C& c, const Params& P) {
  const unsigned tid = c.wid * 64u + (c.vlane >> 4), bc0 = 64u * (c.b % 7u) + 2u * (tid & 31u), vo = ((64u * (c.b / 7u) + 8u * (tid >> 5)) * DM + bc0) * 2u;
  if constexpr (i < 11) ld4(c.kw[i], mk_srd(P.bk, SEQ * DM * 2), vo + (unsigned)((i - 3) * DM * 2));
  else if constexpr (i < 19) ld4(c.vv[i - 11], mk_srd(P.bv, SEQ * DM * 2), vo + (unsigned)((i - 11) * DM * 2));
  else if constexpr (i == 19) asm volatile("buffer_load_dwordx4 %0, %1, %2, 0 offen" : "=v"(c.sk) : "v"(bc0 * 8u), "s"(mk_srd(P.shk, DM * 8)) : "memory");
  else asm volatile("buffer_load_dwordx2 %0, off, %1, 0 sc1" : "=v"(c.ep) : "s"(mk_srd(P.ws + WS_FL, 64)) : "memory");
}
template <bool WA, int STEP, int buf, int G> DI void k1_gap(K1C& c, const Params& P, unsigned sw, unsigned sx) {
  constexpr int ks = G >> 6, g = G & 63, nb = g >> 3, mb = g & 7;
  Frags& fc = ks ? c.f1 : c.f0; Frags& fn = ks ? c.f0 : c.f1;
  if constexpr (G == 64) {
    asm volatile("s_waitcnt vmcnt(%0) lgkmcnt(0)\n\ts_barrier" :: "n"(STEP == 0 ? 3 : STEP == 14 ? 0 : STEP == 15 ? 12 : 1) : "memory");
    SBAR();
  }
  const bf16x8 w = cat(fc.wl[nb], fc.wh[nb]);
  if constexpr (G == 0) asm volatile("s_nop 1" :: "v"(w), "v"(fc.x[0]));
  if constexpr (WA) mfma16a(c.acc[mb][nb], w, fc.x[mb]); else mfma16a(c.acc[nb][mb], fc.x[mb], w);
  SBAR();
  if constexpr (!(g & 1) && g < 48) {
    if constexpr (ks == 0) k1_read<buf, 1, g / 2>(c, fn);
    else if constexpr (STEP != 15) k1_read<buf ^ 1, 0, g / 2>(c, fn);
  }
  if constexpr (STEP == 15 && ks == 1 && g >= 36 && !(g & 3)) { if constexpr (g == 36) k1_qx<0, 0>(c); else k1_qw<(g - 40) / 4>(c); }
  if constexpr (STEP == 0 && ks == 0 && (g & 1) && g < 16) k1_piece<1, 8 + (g >> 1)>(c, c.sw_kv + 64u * WRS, c.sx_kv + 128u);
  if constexpr (STEP == 0 && (G == 17 || G == 19 || G == 21)) { k1_touch(c); c.pf += c.pfstep; }
  if constexpr (STEP == 13 && G == 64) { if (c.wid == 3) touch_lds(c.ldsb, c.scode, code_off()); }
  if constexpr (ks == 1) {
    if constexpr (STEP <= 13 && (g & 3) == 1) k1_piece<buf, (g >> 2)>(c, sw, sx);
    if constexpr (STEP == 0 && (g & 3) == 3) k1_preload<(g >> 2)>(c, P);
    if constexpr (STEP == 0 && (g & 3) == 2 && g >= 44) k1_preload<16 + ((g - 44) >> 2)>(c, P);
    if constexpr (STEP == 14 && (g & 1) && g < 60 && (g >> 1) % 5 != 4) { constexpr int j = (g >> 1) - (g >> 1) / 5; k1_qpiece<j / 12, j % 12>(c); }
    if constexpr (STEP == 15 && (g & 3) == 1 && g < 48) k1_qpiece<2, (g >> 2)>(c);
  }
  if constexpr (STEP <= 13 && G == 127) { k1_touch(c); c.pf += c.pfstep; }
  if constexpr (STEP >= 13) { constexpr int X = (STEP - 13) * 128 + G; if constexpr (X >= 56 && X % 4 == 3 && (X - 56) / 4 < 80) k1_pre<(X - 56) / 4>(c, P); }
  SBAR();
}
template <bool WA, int STEP, int buf> DI void k1_step(K1C& c, const Params& P, int sb) {
  const unsigned sw = c.sw_kv + (unsigned)sb * (64u * WRS), sx = c.sx_kv + (unsigned)sb * 128u;
  sfor<128>([&](auto G) AI { k1_gap<WA, STEP, buf, G>(c, P, sw, sx); });
}
#define LPG 3
constexpr int q_lpos(int k) { return k == 0 ? 7 : k == 1 ? 3 : 5; }
constexpr bool q_late(int u) { for (int k = 0; k < LPG; ++k) if (u % 8 == q_lpos(k)) return true; return false; }
constexpr int q_lidx(int u) { for (int k = 0; k < LPG; ++k) if (u % 8 == q_lpos(k)) return LPG * (u / 8) + k; return 0; }
constexpr int q_lunit(int k) { return 8 * (k / (LPG ? LPG : 1)) + q_lpos(k % (LPG ? LPG : 1)); }
constexpr int q_stores(int lo, int hi) { int n = 0; for (int u = 0; u < 32; ++u) n += (!q_late(u) && 6 * u + 5 > lo && 6 * u + 5 < hi); return n; }
constexpr int q_wait(int T) {
  return T == 0 ? 14 + q_stores(-1, 26) : T == 1 ? 12 + q_stores(-1, 58) : (T + 2 <= 6 ? 12 : 0) + q_stores(32 * (T - 2) + 48, 32 * T + 26); }
template <bool WA, int X> DI void k1_qgap(K1C& c, const Params& P) {
  constexpr int T = X >> 5, g = X & 31, ks = g >> 3, nb = g & 7;
  if constexpr (g == 26 && T < 6) {
    asm volatile("s_waitcnt vmcnt(%0) lgkmcnt(0)\n\ts_barrier" :: "n"(q_wait(T)) : "memory"); SBAR();
    if constexpr (T == 1) { if (c.wid == 0 && lane_id() == 0) __hip_atomic_store((gu64*)(P.ws + WS_FL) + FL_S0 + c.b * 16u, c.tag, RLX_AGENT); SBAR(); }
  }
  const bf16x8 w = cat(c.ql[X & 15], c.qh[X & 15]);
  if constexpr (X < 8) mfma32qz(c.qa[nb], w, c.qx[ks]); else mfma32q(c.qa[nb], w, c.qx[ks]);
  SBAR();
  if constexpr (g < 22) { constexpr int f = 6 + g + (g + 4) / 5 - (g > 20 ? 1 : 0); k1_qw<32 * T + f>(c); if constexpr (g % 5 == 0 && g < 20) k1_qw<32 * T + f + 1>(c); }
  if constexpr (g >= 26 && T < 6) k1_qw<32 * (T + 1) + g - 26>(c);
  if constexpr (g == 2 || g == 9 || g == 17) k1_qx<T, ks + 1>(c);
  if constexpr (g == 27 && T < 6) k1_qx<T + 1, 0>(c);
  if constexpr (X >= 26 && !(X & 1) && (X - 26) % 32 < 24 && (X - 26) / 32 + 3 <= 6) k1_qpiece<(X - 26) / 32 + 3, ((X - 26) % 32) / 2>(c);
  constexpr int u = X / 6, ph = X % 6;
  if constexpr (u < 32) {
    constexpr int i = u >> 2, j = u & 3;
    if constexpr (ph == 1) { c.ta = c.acc[i][2 * j]; PINV(c.ta); }
    if constexpr (ph == 2) { c.tb = c.acc[i][2 * j + 1]; PINV(c.tb); }
    if constexpr (ph == 3) { c.pk[0] = cvtpk(c.ta[0], c.ta[1]); c.pk[1] = cvtpk(c.ta[2], c.ta[3]); PINV(c.pk); }
    if constexpr (ph == 4) { c.pk[2] = cvtpk(c.tb[0], c.tb[1]); c.pk[3] = cvtpk(c.tb[2], c.tb[3]); PINV(c.pk); }
    if constexpr (ph == 5 && q_late(u)) { c.ss->late[q_lidx(u)] = __builtin_bit_cast(f32x4, c.pk); PINA(c.ss->late[q_lidx(u)]); }
    if constexpr (ph == 5 && !q_late(u)) st16w<(WA ? (i >> 1) * 16384u + j * 2048u : (i >> 1) * 2048u + j * 16384u) + (i & 1) * 1024u>(c.pk, c.sO, c.vlane, c.so);
  }
  if constexpr (X >= 200 && X < 205) {
    const unsigned tid = c.wid * 64u + (c.vlane >> 4), c0 = 2u * (28u * (c.b & 7u) + (c.b >> 3)); constexpr int ch = (X - 200) >> 1;
    if constexpr (X == 204) asm volatile("buffer_load_dwordx2 %0, %1, %2, 0 offen sc1" : "=v"(c.ss->f0) : "v"((FL_S0 + (7u * (tid & 31u) + (c0 >> 6)) * 16u) * 8u), "s"(mk_srd(P.ws + WS_FL, 1u << 20)) : "memory");
    else if constexpr (X & 1) asm volatile("buffer_load_dwordx2 %0, %1, %2, 0 offen offset:16" : "=v"(c.ss->hB[ch]) : "v"(tid * 16u), "s"(mk_srd(P.ssmk + (size_t)(c0 + ch) * SEQ, c.b < 224u ? SEQ * 2 : 0)) : "memory");
    else asm volatile("buffer_load_dwordx4 %0, %1, %2, 0 offen" : "=v"(c.ss->hA[ch]) : "v"(tid * 16u), "s"(mk_srd(P.ssmk + (size_t)(c0 + ch) * SEQ, c.b < 224u ? SEQ * 2 : 0)) : "memory");
  }
  if constexpr (X >= 196 && X < 200) {
    constexpr int j = X - 196; const unsigned ln = c.vlane >> 4;
    dma16<WO_LDS + j * 1024u, j * 256u>(c.lds4, (ln >> 2) * (DM * 2) + (ln & 3u) * 16u, mk_srd(P.wo, NH * DH * DM * 2), c.hb * (16u * DM * 2) + c.wid * 64u);
  }
  SBAR();
}

template <bool WA> DI void k1_main(K1C& c, const Params& P, const char* pf2, u32x4 (&qo)[16]) {
  sfor<64>([&](auto I) AI { c.acc[I >> 3][I & 7] = f32x4{}; PINA(c.acc[I >> 3][I & 7]); });
  SBAR();
  sfor<8>([&](auto J) AI { k1_piece<1, J>(c, c.sw_kv + 64u * WRS, c.sx_kv + 128u); });
  asm volatile("s_waitcnt vmcnt(8)\n\ts_barrier" ::: "memory");
  sfor<24>([&](auto I) AI { k1_read<0, 0, I>(c, c.f0); });
  SBAR();
  k1_step<WA, 0, 0>(c, P, 2);
#pragma nounroll
  for (int t = 1; t < 13; t += 2) {
    if (t >= 3 && t <= 7) {
      const unsigned l = c.vlane >> 4, w = (c.b * 4u + c.wid) * 32u + (l & 31u);
      const unsigned short* p0 = t == 3 ? P.wq : t == 5 ? P.x : P.ssmk; const unsigned short* p1 = t == 3 ? P.wo : t == 5 ? P.bq : P.ssmk;
      const unsigned n = t == 3 ? DM * WRS : SEQ * DM * 2, o = w * 128u < n ? w * 128u : (w * 128u) & 0xfffffu;
      const char* a = (const char*)(l < 32u ? p0 : p1) + o;
      asm volatile("s_add_u32 m0, %0, 147456\n\ts_nop 0\n\tglobal_load_lds_dword %1, off" :: "s"(c.ldsb), "v"(a) : "memory", "scc");
    }
    k1_step<WA, 1, 1>(c, P, t + 2);
    c.pf = t == 9 ? pf2 : c.pf;
    k1_step<WA, 1, 0>(c, P, t + 3);
  }
  k1_step<WA, 13, 1>(c, P, 15);
  {
    const unsigned lane = lane_id(), r = lane & 31u, hh = lane >> 5, q = (lane & 15u) >> 2, p = lane & 3u, blk = (lane >> 4) & 1u;
    sfor<2>([&](auto I) AI { c.vx_q[I] = (lane >> 3) * (DM * 2) + 16u * ((lane & 7u) ^ (4u * I + (lane >> 4))); c.vwq[I] = hh * WRS + 16u * (r ^ ((2u * I + hh) << 2)); });
    sfor<4>([&](auto I) AI {
      c.xq[I] = (lds_cptr)(size_t)(c.ldsb + (32u * c.wid + r) * 128u + 16u * ((2u * I + hh) ^ ((r >> 1) & 7u))); PINV(c.xq[I]);
      c.wq[I] = (lds_cptr)(size_t)(c.ldsb + 65536u + (8u * hh + q) * 512u + ((unsigned)I ^ q) * 64u + 8u * (4u * (p >> 1) + 2u * blk + (p & 1u))); PINV(c.wq[I]);
      c.wq1[I] = c.wq[I] + 65536; PINV(c.wq1[I]); });
  }
  k1_step<WA, 14, 0>(c, P, 0);
  k1_step<WA, 15, 1>(c, P, 0);
  sfor<224>([&](auto X) AI { k1_qgap<WA, X>(c, P); });
  MFMA_DRAIN(); SBAR();
  sfor<8>([&](auto D) AI { PINV(c.qa[D]); qo[2 * D] = pack8w(c.qa[D], 0, QSCALE); qo[2 * D + 1] = pack8w(c.qa[D], 1, QSCALE); PINV(qo[2 * D]); PINV(qo[2 * D + 1]); SBAR(); });
  asm volatile("s_waitcnt vmcnt(0)" : "+v"(c.ss->hA[0]), "+v"(c.ss->hA[1]), "+v"(c.ss->hB[0]), "+v"(c.ss->hB[1]), "+v"(c.ss->f0) :: "memory");
}

DI unsigned long long phase_proj(const Params& P, char* lds, const unsigned wid, const unsigned qblk, u32x4 (&qo)[16], SsmS& ss, srd_t scode) {
  const unsigned lane = lane_id(), wr = wid >> 1, wc = wid & 1;
  const unsigned b = blockIdx.x, xcd = b & 7u, idx = b >> 3;
  const unsigned pm = idx & 7u, which = idx >> 3, h = 2u * xcd + (which >> 1), isV = which & 1u;
  K1C c; c.ss = &ss; c.scode = scode;
  c.ldsb = (unsigned)(size_t)lds; c.lds8 = c.ldsb + wid * 8192u; c.lds4 = c.ldsb + wid * 4096u; c.wid = wid; c.vlane = lane * 16u; c.b = b; c.hb = 16u * h + (idx & 15u);
  const unsigned short* wkv = isV ? P.wuv : P.wuk;
  const bool pw = lane >= 32u;
  unsigned pfa = (192u + 8u * pm + 2u * wid + ((lane >> 2) & 1u)) * WRS + h * (DH * 2) + (lane & 3u) * 128u, pfb = (256u * pm + 64u * which + 16u * wid + (lane & 15u)) * (DC * 2) + 384u;
  PINV(pfa); PINV(pfb);
  c.pf = (pw ? (const char*)wkv : (const char*)P.ckv) + (pw ? pfa : pfb);
  c.pfstep = pw ? 64u * WRS : 128u;
  c.sX = mk_srd(P.ckv, SEQ * DC * 2); c.sW = mk_srd(wkv, DC * WRS); c.sXq = mk_srd(P.x, SEQ * DM * 2); c.sWq = mk_srd(P.wq, DM * WRS);
  sfor<2>([&](auto I) AI { c.vx_kv[I] = (lane >> 3) * (DC * 2) + 16u * ((lane & 7u) ^ (4u * I + (lane >> 4))); });
  sfor<4>([&](auto I) AI { const unsigned f = (2u * (I & 1) + (lane >> 5)) | (4u * (I >> 1)); c.vw[I] = (lane >> 5) * WRS + 16u * ((lane & 31u) ^ (f << 1)); });
  c.sx_kv = (256u * pm + 64u * wid) * (DC * 2); c.sw_kv = 16u * wid * WRS + h * (DH * 2);
  c.sx_q = (128u * qblk + 32u * wid) * (DM * 2);  c.sw_q = c.sw_kv;
  k1_stage<0>(c, 0);
    const unsigned rho = lane & 15u, g = lane >> 4;
  sfor<2>([&](auto I) AI { c.xr_kv[I] = (lds_cptr)lds + (wr * 128u + 16u * (rho >> 3) + (rho & 7u)) * 128u + 16u * ((4u * I + g) ^ (rho >> 1)); PINV(c.xr_kv[I]); });
  { const unsigned q = (lane & 15u) >> 2, p = lane & 3u, f = q | ((g & 1u) << 2);
    sfor<8>([&](auto I) AI { c.wr[I] = (lds_cptr)lds + 65536u + (8u * g + q) * 512u + (8u * wc + ((unsigned)I ^ f)) * 32u + 8u * p; PINV(c.wr[I]); }); }
  c.sO = mk_srd(P.ws + WS_K + (isV ? MB16 : 0u) + (size_t)h * (SEQ * DH * 2), SEQ * DH * 2);
  c.so = (8u * pm + 4u * wr) * 16384u + wc * 8192u;
  pfa = (192u + 4u * (idx & 15u) + wid) * WRS + h * (DH * 2) + (lane & 3u) * 128u; pfb = (128u * qblk + 64u * (idx >> 4) + 16u * wid + (lane & 15u)) * (DM * 2) + 384u;
  PINV(pfa); PINV(pfb);
  const char* pf2 = (pw ? (const char*)P.wq : (const char*)P.x) + (pw ? pfa : pfb);
  ss.sO = c.sO; ss.so = c.so; ss.sA = isV ? 2048u : 16384u; ss.sB = isV ? 16384u : 2048u;
  if (isV) k1_main<false>(c, P, pf2, qo); else k1_main<true>(c, P, pf2, qo);
  return c.tag;
}

constexpr float THR = 8.0f;
constexpr int AO = 0, AQ = 128, AQ2 = 192;
template <int D> DI void pv_mfma(const bf16x8& a, const u32x4& b) { asm volatile("v_mfma_f32_32x32x16_bf16 a[%c2:%c3], %0, %1, a[%c2:%c3]" :: "v"(a), "v"(b), "n"(16 * D), "n"(16 * D + 15)); }
template <int D> DI void pv_mfma4(const bf16x8& a, const u32x4& b, const bf16x8& x, const bf16x8& y, const bf16x8& z) { asm volatile("v_mfma_f32_32x32x16_bf16 a[%c2:%c3], %0, %1, a[%c2:%c3]" :: "v"(a), "v"(b), "n"(16 * D), "n"(16 * D + 15), "v"(x), "v"(y), "v"(z)); }
template <int D> DI void pv_mfmaz(const bf16x8& a, const u32x4& b, const bf16x8& x, const bf16x8& y, const bf16x8& z) { asm volatile("v_mfma_f32_32x32x16_bf16 a[%c2:%c3], %0, %1, 0" :: "v"(a), "v"(b), "n"(16 * D), "n"(16 * D + 15), "v"(x), "v"(y), "v"(z)); }
template <int S> DI void qk_mfma(f32x16& c, const bf16x8& a) { asm volatile("v_mfma_f32_32x32x16_bf16 %0, %1, a[%c2:%c3], %0" : "+v"(c) : "v"(a), "n"(AQ + 4 * S), "n"(AQ + 4 * S + 3)); }
template <int S> DI void qk_mfma4(f32x16& c, const bf16x8& a, const bf16x8& x, const bf16x8& y, const bf16x8& z) { asm volatile("v_mfma_f32_32x32x16_bf16 %0, %1, a[%c2:%c3], %0" : "+v"(c) : "v"(a), "n"(AQ + 4 * S), "n"(AQ + 4 * S + 3), "v"(x), "v"(y), "v"(z)); }
DI void qk_init4(f32x16& d, const bf16x8& a, const f32x16& c, const bf16x8& x, const bf16x8& y, const bf16x8& z) { asm volatile("v_mfma_f32_32x32x16_bf16 %0, %1, a[128:131], %2" : "=&v"(d) : "v"(a), "v"(c), "v"(x), "v"(y), "v"(z)); }
template <int R> DI void acc_zero4() { asm volatile("v_accvgpr_write_b32 a[%c0], 0\n\tv_accvgpr_write_b32 a[%c1], 0\n\tv_accvgpr_write_b32 a[%c2], 0\n\tv_accvgpr_write_b32 a[%c3], 0" :: "n"(R), "n"(R + 1), "n"(R + 2), "n"(R + 3)); }
template <int R> DI void acc_write4(const u32x4& w) { asm volatile("v_accvgpr_write_b32 a[%c4], %0\n\tv_accvgpr_write_b32 a[%c5], %1\n\tv_accvgpr_write_b32 a[%c6], %2\n\tv_accvgpr_write_b32 a[%c7], %3" :: "v"(w[0]), "v"(w[1]), "v"(w[2]), "v"(w[3]), "n"(R), "n"(R + 1), "n"(R + 2), "n"(R + 3)); }
template <int R, int S> DI void acc_mov4() { asm volatile("v_accvgpr_mov_b32 a[%c0], a[%c4]\n\tv_accvgpr_mov_b32 a[%c1], a[%c5]\n\tv_accvgpr_mov_b32 a[%c2], a[%c6]\n\tv_accvgpr_mov_b32 a[%c3], a[%c7]" :: "n"(R), "n"(R + 1), "n"(R + 2), "n"(R + 3), "n"(S), "n"(S + 1), "n"(S + 2), "n"(S + 3)); }
template <int R> DI f32x4 acc_read4() { float a, b, c, d; asm volatile("v_accvgpr_read_b32 %0, a[%c4]\n\tv_accvgpr_read_b32 %1, a[%c5]\n\tv_accvgpr_read_b32 %2, a[%c6]\n\tv_accvgpr_read_b32 %3, a[%c7]" : "=v"(a), "=v"(b), "=v"(c), "=v"(d) : "n"(R), "n"(R + 1), "n"(R + 2), "n"(R + 3)); return (f32x4){a, b, c, d}; }
template <int R> DI void acc_scale2(float f) { float t, u; asm volatile("v_accvgpr_read_b32 %0, a[%c3]\n\tv_accvgpr_read_b32 %1, a[%c4]\n\tv_mul_f32 %0, %2, %0\n\tv_mul_f32 %1, %2, %1\n\tv_accvgpr_write_b32 a[%c3], %0\n\tv_accvgpr_write_b32 a[%c4], %1" : "=&v"(t), "=&v"(u) : "v"(f), "n"(R), "n"(R + 1)); }
template <int R, unsigned SI> DI void acc_store4(srd_t srd, unsigned voff) { unsigned t; asm volatile("s_mov_b32 %0, %3\n\tbuffer_store_dwordx4 a[%c4:%c5], %1, %2, %0 offen sc1" : "=&s"(t) : "v"(voff), "s"(srd), "n"(SI), "n"(R), "n"(R + 3) : "memory"); }
template <int R, unsigned SI> DI void acc_load4(srd_t srd, unsigned voff) { unsigned t; asm volatile("s_mov_b32 %0, %3\n\tbuffer_load_dwordx4 a[%c4:%c5], %1, %2, %0 offen sc1" : "=&s"(t) : "v"(voff), "s"(srd), "n"(SI), "n"(R), "n"(R + 3) : "memory"); }
#define A8(n) "a" #n "0", "a" #n "1", "a" #n "2", "a" #n "3", "a" #n "4", "a" #n "5", "a" #n "6", "a" #n "7", "a" #n "8", "a" #n "9"
#define ACC_CLAIM() asm volatile("" ::: "a0", "a1", "a2", "a3", "a4", "a5", "a6", "a7", "a8", "a9", A8(1), A8(2), A8(3), A8(4), A8(5), A8(6), A8(7), A8(8), A8(9), A8(10), A8(11), A8(12), A8(13), A8(14), \
  A8(15), A8(16), A8(17), A8(18), A8(19), A8(20), A8(21), A8(22), A8(23), A8(24), "a250", "a251", "a252", "a253", "a254", "a255")
DI float max3f(float a, float b, float c) { float r; asm("v_max3_f32 %0, %1, %2, %3" : "=v"(r) : "v"(a), "v"(b), "v"(c)); return r; }
DI float max2f(float a, float b) { float r; asm("v_max_f32_e32 %0, %1, %2" : "=v"(r) : "v"(a), "v"(b)); return r; }
DI float fadd_s(float a, float b) { float r; asm volatile("v_add_f32_e32 %0, %1, %2" : "=v"(r) : "v"(a), "v"(b)); return r; }
DI float fsub_s(float a, float b) { float r; asm("v_sub_f32_e32 %0, %1, %2" : "=v"(r) : "v"(a), "v"(b)); return r; }
constexpr int koff(int f) { return (f >> 1) * 2048 + (f & 1) * 512; }
constexpr int voff(int f) { return (f >> 4) * 16384 + (f & 7) * 2048 + ((f >> 3) & 1) * 512; }
struct K2C {
  unsigned ldsb, vlane;
  srd_t sK;
  lds_cptr kr, vr;
  unsigned kslot_w, vslot_w, vslot_r;
  unsigned ksoff, vsoff, dt;
  f32x16 s0, s1, negm; u32x4 p[4]; bf16x8 fr[16];
  float m, l0, l1, mx0, mx1, fct, thr, mS, lS; bool pend;
  f32x4 ml, ob[32];
  int sw; srd_t sP;
  int dq, rel;
};
template <int J> DI void k2_dma(unsigned& t, unsigned m0v, unsigned voff, srd_t srd, unsigned soff) {
  if constexpr (J == 0) asm volatile("s_mov_b32 m0, %1\n\ts_mov_b32 %0, %4\n\tbuffer_load_dwordx4 %2, %3, %0 offen sc1 lds" : "=&s"(t) : "s"(m0v), "v"(voff), "s"(srd), "s"(soff) : "memory");
  else if constexpr (J == 4) asm volatile("s_add_u32 m0, m0, 4096\n\ts_add_u32 %0, %0, 4096\n\tbuffer_load_dwordx4 %1, %2, %0 offen sc1 lds" : "+s"(t) : "v"(voff), "s"(srd) : "memory", "scc");
  else asm volatile("buffer_load_dwordx4 %0, %1, %2 offen offset:%3 sc1 lds" :: "v"(voff), "s"(srd), "s"(t), "n"((J & 3) * 1024) : "memory");
}
DI void k2_dma8(unsigned m0v, unsigned voff, srd_t srd, unsigned soff) { unsigned t; sfor<8>([&](auto J) AI { k2_dma<J>(t, m0v, voff, srd, soff); }); }
template <int I> DI void k2_mask4(f32x16& s, int d, float ninf) {
  constexpr int r = 4 * I; float a = s[r], b = s[r + 1], c = s[r + 2], e = s[r + 3];
  asm volatile("v_cmp_gt_i32 vcc, %5, %4\n\tv_cmp_gt_i32 s[4:5], %6, %4\n\tv_cmp_gt_i32 s[6:7], %7, %4\n\tv_cmp_gt_i32 s[8:9], %8, %4\n\t"
               "v_cndmask_b32 %0, %0, %9, vcc\n\tv_cndmask_b32 %1, %1, %9, s[4:5]\n\tv_cndmask_b32 %2, %2, %9, s[6:7]\n\tv_cndmask_b32 %3, %3, %9, s[8:9]"
               : "+v"(a), "+v"(b), "+v"(c), "+v"(e) : "v"(d), "n"(8 * I), "n"(8 * I + 1), "n"(8 * I + 2), "n"(8 * I + 3), "v"(ninf) : "vcc", "s4", "s5", "s6", "s7", "s8", "s9");
  s[r] = a; s[r + 1] = b; s[r + 2] = c; s[r + 3] = e;
}
DI void k2_mask(f32x16& s, int rel, int dq) {
  const float ninf = -__builtin_inff(); const int d = rel > 0 ? -100 : dq;
  k2_mask4<0>(s, d, ninf); k2_mask4<1>(s, d, ninf); k2_mask4<2>(s, d, ninf); k2_mask4<3>(s, d, ninf);
}
DI void max4(float& m, float a, float b, float c, float d) { asm volatile("v_max3_f32 %0, %0, %1, %2\n\tv_max3_f32 %0, %0, %3, %4" : "+v"(m) : "v"(a), "v"(b), "v"(c), "v"(d)); }
DI void max6(float& m, float a, float b, float c, float d, float e, float f) { asm volatile("v_max3_f32 %0, %0, %1, %2\n\tv_max3_f32 %0, %0, %3, %4\n\tv_max3_f32 %0, %0, %5, %6" : "+v"(m) : "v"(a), "v"(b), "v"(c), "v"(d), "v"(e), "v"(f)); }
DI float max4i(float a, float b, float c, float d) { float m; asm volatile("v_max_f32_e32 %0, %1, %2\n\tv_max3_f32 %0, %0, %3, %4" : "=&v"(m) : "v"(a), "v"(b), "v"(c), "v"(d)); return m; }
template <int e> DI void k2_lsum(K2C& c, float v) { if constexpr (e & 1) c.l1 = fadd_s(c.l1, v); else c.l0 = fadd_s(c.l0, v); }
template <bool FIN, bool QK, bool FL, int g> DI void k2_gapA(K2C& c) {
  if constexpr (QK) {
    if constexpr (g == 0) qk_init4(c.s0, c.fr[0], c.negm, c.fr[1], c.fr[2], c.fr[3]);
    else if constexpr (g == 16) qk_init4(c.s1, c.fr[0], c.negm, c.fr[1], c.fr[2], c.fr[3]);
    else if constexpr ((g & 3) == 0) qk_mfma4<g & 15>(g < 16 ? c.s0 : c.s1, c.fr[g & 15], c.fr[(g + 1) & 15], c.fr[(g + 2) & 15], c.fr[(g + 3) & 15]);
    else qk_mfma<g & 15>(g < 16 ? c.s0 : c.s1, c.fr[g & 15]);
    SBAR();
    if constexpr (g < 24) c.fr[(g + 8) & 15] = lds_rd128(c.kr + koff((g + 8)));
  }
  if constexpr (!QK && g <= 9) { asm volatile("s_nop 0"); SBAR(); }
  if constexpr (!FIN) acc_zero4<4 * g>();
  if constexpr (FIN && (QK ? g >= 24 : g < 8)) c.fr[g & 7] = lds_rd128(c.vr + voff((g & 7)));
  if constexpr (FIN && g <= 8) {
    if constexpr (g < 8) c.s1[8 + g] = __builtin_amdgcn_exp2f(c.s1[8 + g]);
    k2_lsum<g>(c, c.s1[7 + g]);
    if constexpr (g < 4) c.p[2][g] = cvtpk(c.s1[2 * g], c.s1[2 * g + 1]);
    else if constexpr (g < 7) c.p[3][g - 4] = cvtpk(c.s1[2 * g], c.s1[2 * g + 1]);
    else if constexpr (g == 8) c.p[3][3] = cvtpk(c.s1[14], c.s1[15]);
    if constexpr (g < 8) PINV(c.s1);
    if constexpr (g < 4) PINV(c.p[2]); else PINV(c.p[3]);
  }
  if constexpr (FIN && (QK ? g < 24 && (g & 3) == 1 : g < 6)) k2_dma<(QK ? g / 4 : g) + 2>(c.dt, 0u, c.vlane, c.sK, 0u);
  if constexpr (FL) {
    acc_store4<4 * g, 1024 * g>(c.sP, c.vlane);
    if constexpr (g == 12) { float a = c.mS, b = c.lS; PINV(a); PINV(b); const u32x4 v = {__float_as_uint(a), __float_as_uint(b), 0u, 0u}; st16w<32 * 1024>(v, c.sP, c.vlane, 0u); }
  }
  if constexpr (QK) {
    if constexpr (g >= 24 && (g & 3) == 1) k2_dma<(g - 24) / 4>(c.dt, c.ldsb + 65536u + c.vslot_w, c.vlane, c.sK, c.vsoff);
    if constexpr (g == 18) { if (c.rel >= 0) k2_mask(c.s0, c.rel, c.dq); }
    if constexpr (g == 20) { c.mx0 = max4i(c.s0[0], c.s0[1], c.s0[2], c.s0[3]); }
    if constexpr (g >= 21 && g < 24) { constexpr int r = 4 * (g - 20); max4(c.mx0, c.s0[r], c.s0[r + 1], c.s0[r + 2], c.s0[r + 3]); }
  }
  SBAR();
}
template <bool FIN, bool QK, bool FL = false> DI void k2_phaseA(K2C& c) {
  if constexpr (QK && !FIN) {
#pragma unroll
    for (int i = 0; i < 8; ++i) c.fr[i] = lds_rd128(c.kr + koff(i));
  }
  sfor<32>([&](auto G) AI { k2_gapA<FIN, QK, FL, G>(c); });
}
template <bool PV, bool SM, bool FL, int g> DI void k2_gapB(K2C& c) {
  if constexpr (SM && g == 24) {
    asm volatile("s_waitcnt vmcnt(%0) lgkmcnt(0)\n\ts_barrier" :: "n"(FL ? 6 : 8) : "memory");
    c.kr = (lds_cptr)(size_t)((unsigned)(size_t)c.kr ^ 32768u);
    SBAR();
  }
  if constexpr (PV) {
    if constexpr (SM && FL && g < 8) pv_mfmaz<g>(c.fr[g], c.p[0], c.fr[g | 1], c.fr[g | 2], c.fr[g | 3]);
    else if constexpr ((g & 3) == 0) pv_mfma4<g & 7>(c.fr[g & 15], c.p[g >> 3], c.fr[(g + 1) & 15], c.fr[(g + 2) & 15], c.fr[(g + 3) & 15]);
    else pv_mfma<g & 7>(c.fr[g & 15], c.p[g >> 3]);
    SBAR();
    if constexpr (g < 2) { c.fr[(8 + 2 * g) & 15] = lds_rd128(c.vr + voff((8 + 2 * g))); c.fr[(9 + 2 * g) & 15] = lds_rd128(c.vr + voff((9 + 2 * g))); }
    else if constexpr (g < 8) c.fr[(g + 10) & 15] = lds_rd128(c.vr + voff((g + 10)));
    else if constexpr (g < 10) { c.fr[(2 * g + 2) & 15] = lds_rd128(c.vr + voff((2 * g + 2))); c.fr[(2 * g + 3) & 15] = lds_rd128(c.vr + voff((2 * g + 3))); }
    else if constexpr (g < 20) c.fr[(g + 12) & 15] = lds_rd128(c.vr + voff((g + 12)));
  }
  if constexpr (!SM && FL) c.ob[g] = __builtin_bit_cast(f32x4, __builtin_amdgcn_raw_buffer_load_b128(c.sP, c.vlane, g * 1024, 16));
  if constexpr (!PV && g >= 7) { asm volatile("s_nop 0"); SBAR(); }
  if constexpr (SM) {
    if constexpr (g >= 24) c.fr[(g + 8) & 15] = lds_rd128(c.kr + koff((g - 24)));
    if constexpr ((g & 3) == 1) { if constexpr (g < 24) k2_dma<g / 4 + 2>(c.dt, 0u, c.vlane, c.sK, 0u); else k2_dma<(g - 24) / 4>(c.dt, c.ldsb + c.kslot_w, c.vlane, c.sK, c.ksoff); }
    if constexpr (g == 2) { if (c.rel + 32 >= 0) k2_mask(c.s1, c.rel + 32, c.dq); }
    if constexpr (g == 3) { max6(c.mx0, c.s1[0], c.s1[1], c.s1[2], c.s1[3], c.s1[4], c.s1[5]); }
    if constexpr (g == 4) { max6(c.mx0, c.s1[6], c.s1[7], c.s1[8], c.s1[9], c.s1[10], c.s1[11]); }
    if constexpr (g == 5) { max4(c.mx0, c.s1[12], c.s1[13], c.s1[14], c.s1[15]); c.mx1 = c.mx0; PINV(c.mx1); }
    if constexpr (g == 6) {
      if constexpr (!PV) asm volatile("s_nop 1");
      auto pr = __builtin_amdgcn_permlane32_swap(__float_as_uint(c.mx0), __float_as_uint(c.mx1), false, false);
      const float rm = max2f(__uint_as_float(pr[0]), __uint_as_float(pr[1]));
      if constexpr (!PV) {
        const float dl = max2f(rm, -1e30f); c.m = dl;
#pragma unroll
        for (int r = 0; r < 16; ++r) { c.s0[r] = fsub_s(c.s0[r], dl); c.s1[r] = fsub_s(c.s1[r], dl); c.negm[r] = -dl; }
        PINV(c.negm);
      } else if (__builtin_expect(__any(rm > c.thr), 0)) {
        float dl;
        if (!FL && c.sw == 1) {
          float l = c.l0 + c.l1; auto pl = __builtin_amdgcn_permlane32_swap(__float_as_uint(l), __float_as_uint(l), false, false);
          c.mS = c.m; c.lS = __uint_as_float(pl[0]) + __uint_as_float(pl[1]); c.l0 = 0.f; c.l1 = 0.f;
          dl = max2f(rm, -1e30f); c.m = dl;
        } else {
          dl = max2f(rm, 0.f); c.m = fadd_s(c.m, dl); c.fct = __builtin_amdgcn_exp2f(-dl);
          c.l0 *= c.fct; c.l1 *= c.fct; c.pend = true;
        }
#pragma unroll
        for (int r = 0; r < 16; ++r) { c.s0[r] = fsub_s(c.s0[r], dl); c.s1[r] = fsub_s(c.s1[r], dl); c.negm[r] = -c.m; }
      }
    }
    if constexpr (g >= 7 && g < 23) c.s0[g - 7] = __builtin_amdgcn_exp2f(c.s0[g - 7]);
    if constexpr (g >= 8 && g < 24) k2_lsum<g>(c, c.s0[g - 8]);
    if constexpr (g >= 24) c.s1[g - 24] = __builtin_amdgcn_exp2f(c.s1[g - 24]);
    if constexpr (g >= 25) k2_lsum<g>(c, c.s1[g - 25]);
    if constexpr (g >= 16 && g < 24) { constexpr int w = g - 16; c.p[w >> 2][w & 3] = cvtpk(c.s0[2 * w], c.s0[2 * w + 1]); PINV(c.p[w >> 2]); }
    if constexpr (g >= 7 && g < 23) PINV(c.s0);
    if constexpr (g >= 24) PINV(c.s1);
  }
  SBAR();
}
template <bool PV, bool SM, bool FL = false> DI void k2_phaseB(K2C& c) {
  if constexpr (!PV) { MFMA_DRAIN(); SBAR(); }
  sfor<32>([&](auto G) AI { k2_gapB<PV, SM, FL, G>(c); });
  if constexpr (PV && SM) {
    if (__builtin_expect(c.pend, 0)) {
      MFMA_DRAIN(); SBAR();
      sfor<64>([&](auto I) AI { acc_scale2<2 * I>(c.fct); });
      c.pend = false;
    }
    SBAR();
  }
}

DI void mfma32_z4(f32x16& d, const bf16x8& a, const u32x4& b, const bf16x8& x, const bf16x8& y, const bf16x8& z) { asm volatile("v_mfma_f32_32x32x16_bf16 %0, %1, %2, 0" : "=&v"(d) : "v"(a), "v"(b), "v"(x), "v"(y), "v"(z)); }
DI void mfma32_vv4(f32x16& c, const bf16x8& a, const u32x4& b, const bf16x8& x, const bf16x8& y, const bf16x8& z) { asm volatile("v_mfma_f32_32x32x16_bf16 %0, %1, %2, %0" : "+v"(c) : "v"(a), "v"(b), "v"(x), "v"(y), "v"(z)); }
DI void mfma32_vv(f32x16& c, const bf16x8& a, const u32x4& b) { asm volatile("v_mfma_f32_32x32x16_bf16 %0, %1, %2, %0" : "+v"(c) : "v"(a), "v"(b)); }
struct K2E { u32x4 cx[16]; lds_cptr rc, rn; unsigned slotw, wsoff; srd_t sP; u32x4 pk; };
template <int CH, int g> DI void k2_gapC(K2C& c, K2E& e) {
  if constexpr (CH == 3 && g == 24) { asm volatile("s_waitcnt vmcnt(6)\n\ts_barrier" ::: "memory"); SBAR(); }
  if constexpr ((g & 15) == 0) mfma32_z4(g < 16 ? c.s0 : c.s1, c.fr[0], e.cx[0], c.fr[1], c.fr[2], c.fr[3]);
  else if constexpr ((g & 3) == 0) mfma32_vv4(g < 16 ? c.s0 : c.s1, c.fr[g & 15], e.cx[g & 15], c.fr[(g + 1) & 15], c.fr[(g + 2) & 15], c.fr[(g + 3) & 15]);
  else mfma32_vv(g < 16 ? c.s0 : c.s1, c.fr[g & 15], e.cx[g & 15]);
  SBAR();
  if constexpr (CH < 2 && g == 28) { asm volatile("s_barrier" ::: "memory"); SBAR(); }
  if constexpr (g < 24) c.fr[(g + 8) & 15] = lds_rd128(e.rc + (g + 8) * 1024);
  else if constexpr (CH < 6) c.fr[(g + 8) & 15] = lds_rd128(e.rn + (g - 24) * 1024);
  if constexpr (CH < 3 && g >= 8 && g < 16) k2_dma<g - 8>(c.dt, c.ldsb + e.slotw, c.vlane, c.sK, e.wsoff);
  constexpr int u = (g & 15) - 2;
  if constexpr (u >= 0 && u < 6 && (g >= 16 || CH > 0)) {
    const f32x16& t = g < 16 ? c.s1 : c.s0; constexpr int n = 2 * CH + (g < 16 ? -1 : 0), a = u / 3, i = u % 3;
    if constexpr (i < 2) { e.pk[2 * i] = cvtpk(t[8 * a + 4 * i], t[8 * a + 4 * i + 1]); e.pk[2 * i + 1] = cvtpk(t[8 * a + 4 * i + 2], t[8 * a + 4 * i + 3]); PINV(e.pk); }
    else st16w<(2 * n + a) * 1024>(e.pk, e.sP, c.vlane, 0u);
  }
  SBAR();
}
template <int CH, int... Gs> DI void k2_phaseC_(K2C& c, K2E& e, iseq<Gs...>) { (k2_gapC<CH, Gs>(c, e), ...); }
template <int CH> DI void k2_phaseC(K2C& c, K2E& e) { k2_phaseC_<CH>(c, e, std::make_integer_sequence<int, 32>{}); }

DI void phase_attn(const Params& P, char* lds, unsigned long long tag, const unsigned wid, u32x4 (&qo)[16], srd_t scode) {
  const char *KVW = P.ws + WS_K; char *Pf = P.ws + WS_PF, *part = P.ws + WS_PART; gu64* flags = (gu64*)(P.ws + WS_FL);
  const unsigned b = blockIdx.x, xcd = b & 7u, idx = b >> 3, h = 2u * xcd + (idx >> 4), pi = (idx >> 1) & 7u, roleB = idx & 1u;
  const unsigned pair = h * 8u + pi;
  const int NA = pi == 0 ? 18 : 17;
  const int n1 = roleB ? 32 - 2 * (int)pi - NA : NA, tb1 = roleB ? NA : 0, ntot = roleB ? 34 - NA : NA;
  const unsigned wbase = 2u * MB16 + h * (DH * DM * 2) + wid * 8192u;
  auto tile_off = [&](int p) AI -> unsigned { return h * (SEQ * DH * 2) + (unsigned)(p < n1 ? tb1 + p : p - n1) * 32768u + wid * 8192u; };
  auto k_off = [&](int p) AI -> unsigned { return p >= ntot ? wbase + (unsigned)(p - ntot) * 32768u : tile_off(p); };
  auto v_off = [&](int p) AI -> unsigned { return p >= ntot ? wbase + 2u * 32768u : MB16 + tile_off(p); };
  K2C c;
  c.ldsb = (unsigned)(size_t)lds + wid * 8192u; c.vlane = lane_id() * 16u;
  c.sK = mk_srd(KVW, 2u * MB16 + NH * DH * DM * 2);
  unsigned qb = 15u - pi;
  k2_dma8(c.ldsb, c.vlane, c.sK, k_off(0));
  ACC_CLAIM();
  if (roleB) {
    const srd_t sq = mk_srd(P.ws + ((size_t)pair * 4u + wid) * 16384u, 16384u);
    sfor<16>([&](auto I) AI { acc_load4<AQ + 4 * I, I * 1024>(sq, c.vlane); });
  }
  k2_dma8(c.ldsb + 32768u, c.vlane, c.sK, k_off(1)); k2_dma8(c.ldsb + 65536u, c.vlane, c.sK, v_off(0));
  if (roleB) sfor<16>([&](auto I) AI { acc_write4<AQ2 + 4 * I>(qo[I]); }); else sfor<16>([&](auto I) AI { acc_write4<AQ + 4 * I>(qo[I]); });
  SBAR();
  const unsigned lane = lane_id(), hi = lane >> 5, r32 = lane & 31u;
  const unsigned klane = ((r32 >> 3) & 1u) * 1024u + hi * 256u + ((r32 & 7u) + 8u * (r32 >> 4)) * 16u, vlane_r = (r32 >> 4) * 1024u + hi * 256u + (r32 & 15u) * 16u;
  c.dq = (int)r32 - 4 * (int)hi;
  c.kr = (lds_cptr)lds + klane; c.kslot_w = 0u; c.vslot_w = 32768u; c.vslot_r = 0u;
  asm volatile("s_waitcnt vmcnt(16)\n\ts_barrier" ::: "memory");
  int p = 0;
  c.sP = mk_srd(part + ((size_t)pair * 4u + wid) * 33792u, 33792u);
  const int p1 = roleB ? n1 - 1 : -8;
  int rel = 64 * tb1 - (int)(qb * 128u + wid * 32u);
  auto setup = [&]() AI { c.vsoff = v_off(p + 1); c.ksoff = k_off(p + 2); c.rel = rel; c.sw = p - p1; c.thr = c.sw == 1 ? -3e38f : THR; };
  auto advance = [&]() AI {
    c.kslot_w ^= 32768u; c.vslot_w = c.vslot_w == 65536u ? 0u : c.vslot_w + 32768u; rel += 64;
    c.vr = (lds_cptr)lds + 65536u + c.vslot_r + vlane_r; c.vslot_r = c.vslot_r == 65536u ? 0u : c.vslot_r + 32768u;
    if (p == p1) {
      sfor<16>([&](auto I) AI { acc_mov4<AQ + 4 * I, AQ2 + 4 * I>(); });
      c.negm = f32x16{}; PINV(c.negm); asm volatile("s_nop 1");
      rel = -(int)(pi * 128u + wid * 32u);
    }
    ++p;
  };
  c.negm = f32x16{}; PINV(c.negm); c.l0 = 0.f; c.l1 = 0.f; c.pend = false; c.fct = 1.f; c.mS = 0.f; c.lS = 0.f;
  asm volatile("s_nop 1"); SBAR();
  setup();
  k2_phaseA<false, true>(c); k2_phaseB<false, true>(c);
  advance();
  u32x2 f2 = {0u, 0u}; unsigned sink = 0u;
  int stop = roleB ? p1 + 2 : ntot;
  for (;;) {
#pragma nounroll
    while (p < stop) {
      setup();
      if (!roleB && p == ntot - 1) asm volatile("buffer_load_dwordx2 %0, off, %1, 0 sc1" : "=v"(f2) : "s"(mk_srd(P.ws + WS_FL + (FL_S2 + pair * 16u) * 8u, 8u)) : "memory");
      if (p == ntot - 3 && wid == 3) ld4(sink, scode, code_off());
      k2_phaseA<true, true>(c); k2_phaseB<true, true>(c);
      PINV(f2); PINV(sink);
      advance();
    }
    if (p >= ntot) break;
    setup();
    k2_phaseA<true, true, true>(c); k2_phaseB<true, true, true>(c);
    if (wid == 0 && lane == 0) __hip_atomic_store(flags + FL_S2 + pair * 16u, tag, RLX_AGENT);
    advance();
    stop = ntot;
  }
  qb = roleB ? pi : qb;
  const bool early = !roleB && rfl(f2[0]) == (unsigned)tag && rfl(f2[1]) == (unsigned)(tag >> 32);
  SBAR();
  k2_phaseA<true, false>(c);
  k2_dma8(c.ldsb + 65536u + c.vslot_w, c.vlane, c.sK, wbase + 3u * 32768u);
  if (early) { c.ml = __builtin_bit_cast(f32x4, __builtin_amdgcn_raw_buffer_load_b128(c.sP, c.vlane, 32 * 1024, 16)); SBAR(); k2_phaseB<true, false, true>(c); } else k2_phaseB<true, false>(c);
  MFMA_DRAIN(); SBAR();
  float l = c.l0 + c.l1;
  { auto pr = __builtin_amdgcn_permlane32_swap(__float_as_uint(l), __float_as_uint(l), false, false); l = __uint_as_float(pr[0]) + __uint_as_float(pr[1]); }
  K2E e;
  if (roleB) {
    const float rl = 1.0f / l;
    sfor<16>([&](auto I) AI { e.cx[I] = pack8(acc_read4<8 * I>(), acc_read4<8 * I + 4>(), rl); PINV(e.cx[I]); });
  } else {
    if (!early) {
      wait_flags(flags + FL_S2 + pair * 16u, tag);
      c.ml = __builtin_bit_cast(f32x4, __builtin_amdgcn_raw_buffer_load_b128(c.sP, c.vlane, 32 * 1024, 16));
      sfor<32>([&](auto K) AI { c.ob[K] = __builtin_bit_cast(f32x4, __builtin_amdgcn_raw_buffer_load_b128(c.sP, c.vlane, K * 1024, 16)); });
    }
    const f32x4 ml = c.ml;
    SBAR();
    const float mm = fmaxf(c.m, ml[0]), wa = __builtin_amdgcn_exp2f(c.m - mm), wb = __builtin_amdgcn_exp2f(ml[0] - mm);
    const float rl = 1.0f / (wa * l + wb * ml[1]), fa = wa * rl, fb = wb * rl;
    sfor<16>([&](auto I) AI { e.cx[I] = pack8(acc_read4<8 * I>() * fa + c.ob[2 * I] * fb, acc_read4<8 * I + 4>() * fa + c.ob[2 * I + 1] * fb, 1.0f); PINV(e.cx[I]); });
  }
  e.sP = mk_srd(Pf + ((size_t)h * 64u + qb * 4u + wid) * 28672u, 28672u);
  const unsigned kA = c.kslot_w, kB = c.kslot_w ^ 32768u;
  const unsigned v2 = 65536u + c.vslot_r, v3 = 65536u + c.vslot_w, v4 = 65536u + (98304u - c.vslot_r - c.vslot_w);
  const lds_cptr lb = (lds_cptr)lds + c.vlane;
  asm volatile("s_waitcnt vmcnt(0)\n\ts_barrier" ::: "memory");
  e.rc = lb + kA;
#pragma unroll
  for (int i = 0; i < 8; ++i) c.fr[i] = lds_rd128(e.rc + i * 1024);
  e.rn = lb + kB; e.slotw = v4; e.wsoff = wbase + 4u * 32768u; SBAR(); k2_phaseC<0>(c, e);
  e.rc = e.rn; e.rn = lb + v2; e.slotw = kA; e.wsoff = wbase + 5u * 32768u; SBAR(); k2_phaseC<1>(c, e);
  e.rc = e.rn; e.rn = lb + v3; e.slotw = kB; e.wsoff = wbase + 6u * 32768u; SBAR(); k2_phaseC<2>(c, e);
  e.rc = e.rn; e.rn = lb + v4; SBAR(); k2_phaseC<3>(c, e);
  e.rc = e.rn; e.rn = lb + kA; SBAR(); k2_phaseC<4>(c, e);
  e.rc = e.rn; e.rn = lb + kB; SBAR(); k2_phaseC<5>(c, e);
  e.rc = e.rn; SBAR(); k2_phaseC<6>(c, e);
  MFMA_DRAIN(); PINV(c.s1); SBAR();
  st16w<26 * 1024>(pack8w(c.s1, 0, 1.0f), e.sP, c.vlane, 0u); st16w<27 * 1024>(pack8w(c.s1, 1, 1.0f), e.sP, c.vlane, 0u);
}
constexpr int OUTW = 2 * DM;
DI void reduce_load(u32x4 (&v)[16], unsigned task, srd_t sP, unsigned lane) {
#pragma unroll
  for (int h = 0; h < 16; ++h) v[h] = __builtin_amdgcn_raw_buffer_load_b128(sP, lane * 16u, task * 1024u + h * (64u * 28672u), 16);
}
DI void reduce_sum(const u32x4 (&v)[16], unsigned task, unsigned lane, unsigned short* out) {
  const unsigned rb = task / 28u, f = task % 28u;
  float s[8];
#pragma unroll
  for (int i = 0; i < 8; ++i) {
    float a[16];
#pragma unroll
    for (int h = 0; h < 16; ++h) a[h] = (i & 1) ? __uint_as_float(v[h][i >> 1] & 0xffff0000u) : __uint_as_float(v[h][i >> 1] << 16);
    s[i] = (((a[0] + a[1]) + (a[2] + a[3])) + ((a[4] + a[5]) + (a[6] + a[7]))) + (((a[8] + a[9]) + (a[10] + a[11])) + ((a[12] + a[13]) + (a[14] + a[15])));
  }
  *(u32x4*)(out + (size_t)(32u * rb + (lane & 31u)) * OUTW + 16u * f + 8u * (lane >> 5)) = (u32x4){cvtpk(s[0], s[1]), cvtpk(s[2], s[3]), cvtpk(s[4], s[5]), cvtpk(s[6], s[7])};
}

constexpr int HCOPY = 4160, HCH = 4 * HCOPY, XROW = 80, XCH = 96 * XROW;
constexpr int SSM_LDS = 98304, XOFF = SSM_LDS + 2 * HCH;
struct K3S { lds_cptr ha, xa; f32x16 y0, y1; s16x4 al[4][2], ah[4][2]; bf16x8 bx[4][2]; };
template <int e> DI void k3_ssm_rd(K3S& c) {
  constexpr int s = e & 3;
  c.al[s][0] = *(const volatile LAS s16x4*)(c.ha + e * 64); c.ah[s][0] = *(const volatile LAS s16x4*)(c.ha + e * 64 + 8);
  c.bx[s][0] = lds_rd128(c.xa + (63 - e) * XROW); c.bx[s][1] = lds_rd128(c.xa + (31 - e) * XROW);
  c.al[s][1] = *(const volatile LAS s16x4*)(c.ha + (e + 32) * 64); c.ah[s][1] = *(const volatile LAS s16x4*)(c.ha + (e + 32) * 64 + 8);
}
template <int e, class F> DI void k3_ssm_step(K3S& c, F&& f) {
  constexpr int s = e & 3;
  if constexpr (e + 2 < 32) k3_ssm_rd<e + 2>(c);
  SBAR();
  const bf16x8 a0 = cat(c.al[s][0], c.ah[s][0]), a1 = cat(c.al[s][1], c.ah[s][1]);
  c.y1 = MFMA32(a0, c.bx[s][0], c.y1); c.y0 = MFMA32(a0, c.bx[s][1], c.y0); c.y1 = MFMA32(a1, c.bx[s][1], c.y1);
  SBAR();
  f(std::integral_constant<int, e>{});
  SBAR();
}
DI void ssm_load(SsmS& s, const Params& P, unsigned blk, unsigned long long tag, const unsigned wid) {
  const unsigned lane = lane_id(), tid = wid * 64u + lane;
  s.c0 = 2u * (28u * (blk & 7u) + (blk >> 3));
  if (!__all((((unsigned long long)s.f0[1] << 32) | s.f0[0]) == tag))
    wait_flags((gu64*)(P.ws + WS_FL) + FL_S0 + (7u * (lane & 31u) + (s.c0 >> 6)) * 16u, tag);
  const srd_t sx = mk_srd(P.ws + WS_XT, SEQ * DM * 2);
#pragma unroll
  for (int ch = 0; ch < 2; ++ch) s.xw[ch] = __builtin_amdgcn_raw_buffer_load_b128(sx, (((tid >> 3) * DM + s.c0 + ch) * 64u + 8u * (tid & 7u)) * 2u, 0, 16);
  s.dw = *(const unsigned*)(P.ssmd + s.c0);
}
DI void ssm_build(SsmS& s, char* lds, const unsigned wid) {
  const unsigned tid = wid * 64u + lane_id();
#pragma unroll
  for (int ch = 0; ch < 2; ++ch) {
    const unsigned w[6] = {s.hA[ch][0], s.hA[ch][1], s.hA[ch][2], s.hA[ch][3], s.hB[ch][0], s.hB[ch][1]};
    unsigned o[5];
#pragma unroll
    for (int k = 0; k < 5; ++k) o[k] = __builtin_amdgcn_alignbyte(w[k + 1], w[k], 2);
    LAS char* dst = (LAS char*)lds + SSM_LDS + ch * HCH + tid * 16u + 64u;
    *(LAS u32x4*)(dst) = (u32x4){o[0], o[1], o[2], o[3]};
    *(LAS u32x4*)(dst + HCOPY) = (u32x4){w[1], w[2], w[3], w[4]};
    *(LAS u32x4*)(dst + 2 * HCOPY) = (u32x4){o[1], o[2], o[3], o[4]};
    *(LAS u32x4*)(dst + 3 * HCOPY) = (u32x4){w[2], w[3], w[4], w[5]};
    if (tid < 4u) {
      const unsigned z0 = tid ? 0u : w[0], z1 = tid ? 0u : w[1], zo = tid ? 0u : o[0];
      LAS char* d2 = (LAS char*)lds + SSM_LDS + ch * HCH + (3u - tid) * 16u;
      *(LAS u32x4*)(d2) = (u32x4){0u, 0u, 0u, z0 << 16};
      *(LAS u32x4*)(d2 + HCOPY) = (u32x4){0u, 0u, 0u, z0};
      *(LAS u32x4*)(d2 + 2 * HCOPY) = (u32x4){0u, 0u, z0 << 16, zo};
      *(LAS u32x4*)(d2 + 3 * HCOPY) = (u32x4){0u, 0u, z0, z1};
    }
    LAS char* xb = (LAS char*)lds + XOFF + ch * XCH;
    if (tid < 160u) *(LAS u32x4*)(xb + tid * 16u) = (u32x4){0u, 0u, 0u, 0u};
  }
#pragma unroll
  for (int ch = 0; ch < 2; ++ch) *(LAS u32x4*)((LAS char*)lds + XOFF + ch * XCH + (32u + (tid >> 2)) * XROW + (tid & 3u) * 16u) = s.xw[ch];
}
template <class F> DI void ssm_conv(SsmS& s, char* lds, const unsigned wid, F&& f) {
  const unsigned lane = lane_id(), hi = lane >> 5, r = lane & 31u;
  const unsigned ch = wid >> 1, dh = wid & 1u;
  K3S c;
  c.ha = (lds_cptr)lds + SSM_LDS + ch * HCH + (r & 3u) * HCOPY + (24u - 16u * dh - 8u * hi + 4u * (r >> 2)) * 2u; PINV(c.ha);
  c.xa = (lds_cptr)lds + XOFF + ch * XCH + (r + 1u) * XROW + (2u * dh + hi) * 16u; PINV(c.xa);
  c.y0 = f32x16{}; c.y1 = f32x16{};
  k3_ssm_rd<0>(c); k3_ssm_rd<1>(c);
  sfor<32>([&](auto E) AI { k3_ssm_step<E>(c, f); });
  s.y0 = c.y0; s.y1 = c.y1;
}
DI void ssm_finish(SsmS& s, const Params& P, char* lds, const unsigned wid) {
  const unsigned lane = lane_id(), tid = wid * 64u + lane, hi = lane >> 5, r = lane & 31u;
  LAS char* ys = (LAS char*)lds + SSM_LDS + wid * 8192u;
#pragma unroll
  for (int k = 0; k < 4; ++k) {
    const unsigned o = r * 128u + (((2u * k + hi) ^ (r & 7u)) << 4);
    *(LAS f32x4*)(ys + o) = (f32x4){s.y0[4 * k], s.y0[4 * k + 1], s.y0[4 * k + 2], s.y0[4 * k + 3]};
    *(LAS f32x4*)(ys + 4096u + o) = (f32x4){s.y1[4 * k], s.y1[4 * k + 1], s.y1[4 * k + 2], s.y1[4 * k + 3]};
  }
  asm volatile("s_waitcnt lgkmcnt(0)\n\ts_barrier" ::: "memory");
  const unsigned row = tid >> 2, yo0 = row * 128u + (((2u * (tid & 3u)) ^ (row & 7u)) << 4), yo1 = row * 128u + (((2u * (tid & 3u) + 1u) ^ (row & 7u)) << 4);
  const srd_t sz = mk_srd(P.ws + WS_ZT, SEQ * DM * 2);
  sfor<2>([&](auto C) AI {
    const LAS char* yp = (const LAS char*)lds + SSM_LDS + C * 16384;
    const f32x4 a = *(const LAS f32x4*)(yp + yo0) + *(const LAS f32x4*)(yp + 8192 + yo0), bb = *(const LAS f32x4*)(yp + yo1) + *(const LAS f32x4*)(yp + 8192 + yo1);
    const float dd = bf2f(C ? s.dw >> 16 : s.dw & 0xffffu);
    u32x4 z;
#pragma unroll
    for (int i = 0; i < 4; ++i) {
      const f32x2 kv = up2(s.xw[C][3 - i]), y = rb2(i < 2 ? (f32x2){a[2 * i], a[2 * i + 1]} : (f32x2){bb[2 * i - 4], bb[2 * i - 3]}), kd = rb2((f32x2){kv[1] * dd, kv[0] * dd});
      z[i] = cvtpk(y[0] + kd[0], y[1] + kd[1]);
    }
    st16w<C * 128>(z, sz, (((tid >> 3) * DM + s.c0) * 64u + 8u * (tid & 7u)) * 2u, 0u);
  });
}
DI void ssm_gate(const Params& P, unsigned blk, unsigned long long tag, const unsigned wid) {
  const unsigned lane = lane_id(), tid = wid * 64u + lane, bc0 = 64u * (blk % 7u) + 2u * (tid & 31u), t0 = 64u * (blk / 7u) + 8u * (tid >> 5);
  unsigned qw[8];
#pragma unroll
  for (int i = 0; i < 8; ++i) qw[i] = *(const unsigned*)(P.bq + (size_t)(t0 + i) * DM + bc0);
  { const unsigned pr = 32u * (blk % 7u) + (lane & 31u);
    wait_flags((gu64*)(P.ws + WS_FL) + FL_S1 + (pr / 28u + 8u * (pr % 28u)) * 16u, tag); }
  const srd_t sz = mk_srd(P.ws + WS_ZT, SEQ * DM * 2);
  const unsigned zo = (((blk / 7u) * DM + bc0) * 64u + 8u * (tid >> 5)) * 2u;
  const u32x4 z0 = __builtin_amdgcn_raw_buffer_load_b128(sz, zo, 0, 16), z1 = __builtin_amdgcn_raw_buffer_load_b128(sz, zo + 128u, 0, 16);
#pragma unroll
  for (int i = 0; i < 8; ++i) {
    const f32x2 q = up2(qw[i]), a = up2(z0[i >> 1]), bb = up2(z1[i >> 1]);
    *(unsigned*)(P.out + (size_t)(t0 + i) * OUTW + DM + bc0) = cvtpk(q[0] * a[i & 1], q[1] * bb[i & 1]);
  }
}

__global__ __launch_bounds__(256, 1) void k1_fused(Params P) {
  extern __shared__ __attribute__((aligned(16))) char lds[];
  const unsigned wid = rfl(threadIdx.x >> 6);
  const unsigned b = blockIdx.x, xcd = b & 7u, idx = b >> 3, h = 2u * xcd + (idx >> 4), j = idx & 15u;
  srd_t scode;
  { unsigned long long pc; unsigned len;
    asm volatile("s_getpc_b64 %0\n.Lcode_begin:\n\ts_mov_b32 %1, .Lcode_end-.Lcode_begin" : "=s"(pc), "=s"(len));
    scode = mk_srd((const void*)pc, len); }
  if (wid == 3) touch_lds((unsigned)(size_t)lds, scode, code_off());
  gu64* flags = (gu64*)(P.ws + WS_FL);
  const unsigned qb = (j & 1u) ? (j >> 1) : 15u - (j >> 1);
  const bool ssm = b < 224u;
  u32x4 qo[16]; SsmS s;
  const unsigned long long tag = phase_proj(P, lds, wid, qb, qo, s, scode);
  if (ssm) ssm_load(s, P, b, tag, wid);
  SBAR();
  {
    const unsigned lane = lane_id(), vl = lane * 16u;
    const unsigned q = (lane & 15u) >> 2, p = lane & 3u;
    const lds_cptr rp = (lds_cptr)lds + WO_LDS + wid * 4096u + (4u * (lane >> 5) + q) * 64u + 32u * ((lane >> 4) & 1u) + 16u * (p & 1u) + 8u * (p >> 1);
    bf16x8 wf[4];
    sfor<4>([&](auto J) AI { wf[J] = cat(lds_tr(rp + J * 1024), lds_tr(rp + J * 1024 + 512)); });
    const srd_t sw_ = mk_srd(P.ws + WS_W + (size_t)h * (DH * DM * 2), DH * DM * 2);
    const srd_t sq = mk_srd(P.ws + ((size_t)(h * 8u + (j >> 1)) * 4u + wid) * 16384u, (j & 1u) ? 0u : 16384u);
    auto deferred = [&](auto E) AI {
      constexpr int k = E >> 1;
      if constexpr (E & 1) st16w<k * 1024>(qo[k], sq, vl, 0u);
      else if constexpr (k < 12) { if constexpr (k < 4 * LPG) { constexpr int u = q_lunit(k), i = u >> 2, jj = u & 3; st16wa<(i & 1) * 1024>(s.late[k], s.sO, vl, s.so + (i >> 1) * s.sA + jj * s.sB); } }
      else st16w<(k - 12) * 65536u>(__builtin_bit_cast(u32x4, wf[k - 12]), sw_, vl, (wid * 16u + j) * 1024u);
    };
    if (ssm) {
      ssm_build(s, lds, wid);
      asm volatile("s_waitcnt lgkmcnt(0)\n\ts_barrier" ::: "memory");
      ssm_conv(s, lds, wid, deferred);
      asm volatile("s_barrier" ::: "memory");
      ssm_finish(s, P, lds, wid);
    } else sfor<32>(deferred);
  }
  publish(flags + FL_S1 + b * 16u, tag, wid);
  if (wid == 0) wait_flags(flags + FL_S1 + (xcd + 8u * ((idx & 16u) + (lane_id() & 15u))) * 16u, tag);
  asm volatile("s_barrier" ::: "memory");
  phase_attn(P, lds, tag, wid, qo, scode);
  publish(flags + FL_S3 + b * 16u, tag, wid);
  if (!(j & 1u) && j >= 2u) { const unsigned g0 = 2u * (7u * h + (j >> 1) - 1u); ssm_gate(P, g0, tag, wid); ssm_gate(P, g0 + 1u, tag, wid); }
  const unsigned lane = lane_id();
  if (wid == 0) wait_flags(flags + FL_S3 + ((lane & 7u) + 8u * (16u * ((lane >> 3) & 1u) + j)) * 16u, tag);
  asm volatile("s_barrier" ::: "memory");
  if (b == 0 && wid == 0 && lane == 0) __hip_atomic_store(flags + FL_EPOCH, tag, RLX_AGENT);
  const srd_t sP = mk_srd(P.ws + WS_PF, NH * 64u * 28672u);
  const unsigned task = qb * 112u + h * 7u + wid;
  u32x4 va[16], vb[16];
  reduce_load(va, task, sP, lane);
  if (wid < 3u) reduce_load(vb, task + 4u, sP, lane);
  SBAR();
  reduce_sum(va, task, lane, P.out);
  if (wid < 3u) reduce_sum(vb, task + 4u, lane, P.out);
  asm volatile(".Lcode_end:");
}

extern "C" void kernel_launch(void* const* d_in, const int* in_sizes, int n_in, void* d_out, int out_size, void* d_ws, size_t ws_size, hipStream_t stream) {
  typedef const unsigned short* cu16;
  Params P = {(cu16)d_in[0], (cu16)d_in[1], (cu16)d_in[2], (cu16)d_in[3], (cu16)d_in[4], (cu16)d_in[5], (cu16)d_in[6], (cu16)d_in[7], (cu16)d_in[8], (cu16)d_in[9], (cu16)d_in[10], (cu16)d_in[11],
              (unsigned short*)d_out, (char*)d_ws};
  (void)hipFuncSetAttribute((const void*)k1_fused, hipFuncAttributeMaxDynamicSharedMemorySize, 163840);
  k1_fused<<<256, 256, 163840, stream>>>(P);
}
```

```cpp
#include <hip/hip_runtime.h>
#include <hip/hip_bf16.h>
#include <utility>

typedef short    bf16x8 __attribute__((ext_vector_type(8)));
typedef short    s16x4  __attribute__((ext_vector_type(4)));
typedef float    f32x16 __attribute__((ext_vector_type(16)));
typedef float    f32x4  __attribute__((ext_vector_type(4)));
typedef float    f32x2  __attribute__((ext_vector_type(2)));
typedef __bf16   bf16x2 __attribute__((ext_vector_type(2)));
typedef unsigned u32x4  __attribute__((ext_vector_type(4)));
typedef unsigned u32x2  __attribute__((ext_vector_type(2)));
#define DI __device__ __forceinline__
#define AI __attribute__((always_inline))
#define LAS __attribute__((address_space(3)))
#define SBAR() __builtin_amdgcn_sched_barrier(0)
template <class T> DI void PINV(T& x) { asm volatile("" : "+v"(x)); }
template <class T> DI void PINA(T& x) { asm volatile("" : "+a"(x)); }
#define MFMA32(a, b, c) __builtin_amdgcn_mfma_f32_32x32x16_bf16((a), (b), (c), 0, 0, 0)
#define MFMA_DRAIN() asm volatile("s_nop 7\n\ts_nop 7" ::: "memory")
typedef LAS const char* lds_cptr;
typedef __amdgpu_buffer_rsrc_t srd_t;
template <int... Is> using iseq = std::integer_sequence<int, Is...>;
template <int N> using mkseq = std::make_integer_sequence<int, N>;

DI unsigned rfl(unsigned x) { return __builtin_amdgcn_readfirstlane(x); }
DI unsigned lane_id() { unsigned l; asm volatile("v_mbcnt_lo_u32_b32 %0, -1, 0\n\tv_mbcnt_hi_u32_b32 %0, -1, %0" : "=v"(l)); return l; }
DI unsigned cvtpk(float lo, float hi) { f32x2 v = {lo, hi}; bf16x2 b = __builtin_convertvector(v, bf16x2); return __builtin_bit_cast(unsigned, b); }
DI float bf2f(unsigned h) { return __uint_as_float(h << 16); }
DI float rbf(float x) { return __uint_as_float(cvtpk(x, x) << 16); }
DI bf16x8 lds_rd128(lds_cptr p) { return *(const LAS bf16x8*)p; }
DI s16x4 lds_tr(lds_cptr p) { return __builtin_amdgcn_ds_read_tr16_b64_v4i16((LAS s16x4*)p); }
DI bf16x8 cat(s16x4 l, s16x4 h) { return __builtin_shufflevector(l, h, 0, 1, 2, 3, 4, 5, 6, 7); }
DI srd_t mk_srd(const void* p, unsigned bytes) { return __builtin_amdgcn_make_buffer_rsrc((void*)p, (short)0, (int)bytes, 0x00020000); }
template <unsigned MI, unsigned SI> DI void dma16(unsigned mb, unsigned voff, srd_t srd, unsigned sb) {
  unsigned t;
  asm volatile("s_add_u32 m0, %1, %5\n\ts_add_u32 %0, %4, %6\n\tbuffer_load_dwordx4 %2, %3, %0 offen lds" : "=&s"(t) : "s"(mb), "v"(voff), "s"(srd), "s"(sb), "n"(MI), "n"(SI) : "memory", "scc");
}
template <unsigned SI> DI void st16w(u32x4 v, srd_t srd, unsigned voff, unsigned sb) {
  unsigned t; asm volatile("s_add_u32 %0, %4, %5\n\tbuffer_store_dwordx4 %1, %2, %3, %0 offen sc1\n\ts_nop 1" : "=&s"(t) : "v"(v), "v"(voff), "s"(srd), "s"(sb), "n"(SI) : "memory", "scc"); }
template <unsigned SI> DI void st16wa(f32x4 v, srd_t srd, unsigned voff, unsigned sb) {
  unsigned t; asm volatile("s_add_u32 %0, %4, %5\n\tbuffer_store_dwordx4 %1, %2, %3, %0 offen sc1\n\ts_nop 1" : "=&s"(t) : "a"(v), "v"(voff), "s"(srd), "s"(sb), "n"(SI) : "memory", "scc"); }
DI void ld4(unsigned& d, srd_t srd, unsigned voff) { asm volatile("buffer_load_dword %0, %1, %2, 0 offen" : "=v"(d) : "v"(voff), "s"(srd) : "memory"); }
template <unsigned SI> DI void ld16a(bf16x8& d, srd_t srd, unsigned voff) { unsigned t; asm volatile("s_mov_b32 %1, %4\n\tbuffer_load_dwordx4 %0, %2, %3, %1 offen sc1" : "=a"(d), "=&s"(t) : "v"(voff), "s"(srd), "n"(SI) : "memory"); }
template <int N, class F> DI void sfor_(F&& f, iseq<>) {}
template <int N, class F, int I, int... Is> DI void sfor_(F&& f, iseq<I, Is...>) { f(std::integral_constant<int, I>{}); sfor_<N>(f, iseq<Is...>{}); }
template <int N, class F> DI void sfor(F&& f) { sfor_<N>(f, mkseq<N>{}); }
DI u32x4 pack8(const f32x4& a, const f32x4& b, float sc) { u32x4 w = {cvtpk(a[0] * sc, a[1] * sc), cvtpk(a[2] * sc, a[3] * sc), cvtpk(b[0] * sc, b[1] * sc), cvtpk(b[2] * sc, b[3] * sc)}; return w; }
DI u32x4 pack8w(const f32x16& a, int s, float sc) {
  u32x4 w = {cvtpk(a[8 * s] * sc, a[8 * s + 1] * sc), cvtpk(a[8 * s + 2] * sc, a[8 * s + 3] * sc), cvtpk(a[8 * s + 4] * sc, a[8 * s + 5] * sc), cvtpk(a[8 * s + 6] * sc, a[8 * s + 7] * sc)};
  return w;
}
constexpr int SEQ = 2048, NH = 16, DH = 256, DM = 448, DC = 1024;
constexpr unsigned WRS = NH * DH * 2;
constexpr unsigned MB16 = SEQ * NH * DH * 2;
constexpr float QSCALE = 1.4426950408889634f / 16.0f;

DI unsigned code_off() { return ((blockIdx.x >> 3) * 64u + lane_id()) * 64u; }
typedef __attribute__((address_space(1))) unsigned long long gu64;
#define RLX_AGENT __ATOMIC_RELAXED, __HIP_MEMORY_SCOPE_AGENT
struct Params {
  const unsigned short *x, *ckv, *wq, *wuk, *wuv, *wo, *bq, *bk, *bv, *shk, *ssmk, *ssmd;
  unsigned short* out; char* ws;
};
struct Ctx { srd_t code; };
constexpr size_t WS_K = MB16, WS_W = 3 * (size_t)MB16, WS_XT = WS_W + (4u << 20), WS_ZT = WS_XT + (2u << 20), WS_FL = WS_ZT + (2u << 20), WS_PART = WS_FL + (1u << 20), WS_PF = WS_PART + (17u << 20);
constexpr int FL_EPOCH = 0, FL_S0 = 16, FL_S1 = FL_S0 + 256 * 16, FL_S2 = FL_S1 + 256 * 16, FL_S3 = FL_S2 + 128 * 16;
DI void publish(gu64* flag, unsigned long long tag, unsigned wid) {
  asm volatile("s_waitcnt vmcnt(0)\n\ts_barrier" ::: "memory");
  if (wid == 0 && lane_id() == 0) __hip_atomic_store(flag, tag, RLX_AGENT);
}
DI void wait_flags(gu64* lane_flag, unsigned long long tag) {
  for (int spin = 0; spin < (1 << 20); ++spin) {
    if (__all(__hip_atomic_load(lane_flag, RLX_AGENT) == tag)) break;
    __builtin_amdgcn_s_sleep(1);
  }
}

DI void mfma16a(f32x4& c, const bf16x8& a, const bf16x8& b) { asm volatile("v_mfma_f32_16x16x32_bf16 %0, %1, %2, %0" : "+a"(c) : "v"(a), "v"(b)); }
DI void mfma32q(f32x16& c, const bf16x8& a, const bf16x8& b) { asm volatile("v_mfma_f32_32x32x16_bf16 %0, %1, %2, %0" : "+v"(c) : "v"(a), "v"(b)); }
DI void mfma32qz(f32x16& c, const bf16x8& a, const bf16x8& b) { asm volatile("v_mfma_f32_32x32x16_bf16 %0, %1, %2, 0" : "=&v"(c) : "v"(a), "v"(b)); }
DI f32x2 up2(unsigned w) { f32x2 r = {__uint_as_float(w << 16), __uint_as_float(w & 0xffff0000u)}; return r; }
DI f32x2 rb2(f32x2 v) { return up2(cvtpk(v[0], v[1])); }
constexpr unsigned qxs(int s) { return 16384u * s; }
constexpr unsigned qws(int s) { return s == 0 ? 65536u : s == 1 ? 131072u : 98304u; }
constexpr unsigned WO_LDS = 49152u;
struct SsmS { u32x4 xw[2], hA[2]; u32x2 hB[2], f0; unsigned dw, c0; f32x16 y0, y1; f32x4 late[12]; srd_t sO; unsigned so, sA, sB; };
struct Frags { bf16x8 x[8]; s16x4 wl[8], wh[8]; };
struct K1C {
  unsigned ldsb, lds8, lds4, wid, vlane, b, hb;
  unsigned vx_kv[2], vx_q[2], vw[4], vwq[2];
  srd_t sX, sW, sXq, sWq, sO;
  unsigned sx_kv, sw_kv, sx_q, sw_q, so;
  lds_cptr xr_kv[2], wr[8], xq[4], wq[4], wq1[4];
  const char* pf; unsigned pfstep;
  f32x4 acc[8][8]; Frags f0, f1; f32x4 ta, tb; u32x4 pk;
  f32x16 qa[8]; s16x4 ql[16], qh[16]; bf16x8 qx[4];
  unsigned kw[11], vv[8], kv[8]; u32x4 sk; u32x2 ep; f32x2 kf[11], sc[4], pa, pt;
  unsigned long long tag; SsmS* ss; srd_t scode;
};
DI void touch_lds(unsigned ldsb, srd_t srd, unsigned voff) {
  asm volatile("s_add_u32 m0, %0, 147456\n\ts_nop 0\n\tbuffer_load_dword %1, %2, 0 offen lds" :: "s"(ldsb), "v"(voff), "s"(srd) : "memory", "scc"); }
DI void k1_touch(const K1C& c) {
  asm volatile("s_add_u32 m0, %0, 147456\n\ts_nop 0\n\tglobal_load_lds_dword %1, off" :: "s"(c.ldsb), "v"(c.pf) : "memory", "scc"); }
template <int buf, int j> DI void k1_piece(const K1C& c, unsigned sw, unsigned sx) {
  constexpr int p = j >> 1;
  if constexpr (!(j & 1)) dma16<buf * 32768u + 65536u + p * 1024u, 2u * p * WRS>(c.lds8, c.vw[(p & 1) + 2 * (p >> 2)], c.sW, sw);
  else dma16<buf * 32768u + p * 1024u, 8u * p * (DC * 2)>(c.lds8, c.vx_kv[(p >> 1) & 1], c.sX, sx);
}
template <int S, int j> DI void k1_qpiece(const K1C& c) {
  if constexpr (j % 3 != 1) { constexpr int p = 2 * (j / 3) + (j % 3 == 2); dma16<qws(S % 3) + p * 1024u, (64u * S + 2u * p) * WRS>(c.lds8, c.vwq[p & 1], c.sWq, c.sw_q); }
  else { constexpr int p = j / 3; dma16<qxs(S % 3) + p * 1024u, 8u * p * (DM * 2) + 128u * S>(c.lds4, c.vx_q[p & 1], c.sXq, c.sx_q); }
}
template <int buf> DI void k1_stage(const K1C& c, int st) { sfor<16>([&](auto J) AI { k1_piece<buf, J>(c, c.sw_kv + (unsigned)st * (64u * WRS), c.sx_kv + (unsigned)st * 128u); }); }
template <int buf, int ks, int i> DI void k1_read(const K1C& c, Frags& f) {
  constexpr int so = buf * 32768;
  if constexpr (i < 2 || i >= 10) { constexpr int k = i < 2 ? i : i - 8, nb = k >> 1, t = k & 1;
    const s16x4 v = lds_tr(c.wr[nb] + so + ks * 16384 + t * 2048); if constexpr (t) f.wh[nb] = v; else f.wl[nb] = v; }
  else { constexpr int mb = i - 2; f.x[mb] = lds_rd128(c.xr_kv[ks] + so + (mb >> 1) * 4096 + (mb & 1) * 1024); }
}
template <int F> DI void k1_qw(K1C& c) {
  constexpr int T = F >> 5, ks = (F >> 3) & 3, nb = F & 7, sl = T % 3, o = (sl == 2 ? 32768 : 0) + ks * 8192 + (nb >> 2) * 256;
  const lds_cptr p = sl == 1 ? c.wq1[nb & 3] : c.wq[nb & 3];
  c.ql[F & 15] = lds_tr(p + o); c.qh[F & 15] = lds_tr(p + o + 2048);
}
template <int T, int ks> DI void k1_qx(K1C& c) { c.qx[ks] = lds_rd128(c.xq[ks] + qxs(T % 3)); }
template <int m> DI void k1_pre(K1C& c, const Params& P) {
  if constexpr (m == 0) {
    asm volatile("" : "+v"(c.kw[0]), "+v"(c.kw[1]), "+v"(c.kw[2]), "+v"(c.kw[3]), "+v"(c.kw[4]), "+v"(c.kw[5]), "+v"(c.kw[6]), "+v"(c.kw[7]), "+v"(c.kw[8]), "+v"(c.kw[9]), "+v"(c.kw[10]),
                 "+v"(c.vv[0]), "+v"(c.vv[1]), "+v"(c.vv[2]), "+v"(c.vv[3]), "+v"(c.vv[4]), "+v"(c.vv[5]), "+v"(c.vv[6]), "+v"(c.vv[7]), "+v"(c.sk), "+v"(c.ep));
    const unsigned long long e0 = ((unsigned long long)rfl(c.ep[1]) << 32) | rfl(c.ep[0]);
    c.tag = (e0 ^ 0x9E3779B97F4A7C15ull) * 0xD1342543DE82EF95ull + 1ull;
  } else if constexpr (m < 12) { c.kf[m - 1] = up2(c.kw[m - 1]); PINV(c.kf[m - 1]); }
  else if constexpr (m < 14) { constexpr int i = m - 12;
    c.sc[2 * i] = (f32x2){bf2f(c.sk[i] & 0xffffu), bf2f(c.sk[2 + i] & 0xffffu)}; c.sc[2 * i + 1] = (f32x2){bf2f(c.sk[i] >> 16), bf2f(c.sk[2 + i] >> 16)}; PINV(c.sc[2 * i]); PINV(c.sc[2 * i + 1]); }
  else if constexpr (m < 78) { constexpr int i = (m - 14) >> 3, s = (m - 14) & 7;
    if constexpr (s == 0) { c.pa = rb2(c.kf[i] * c.sc[3]); PINV(c.pa); }
    else if constexpr (s == 7) { const f32x2 a = c.pa * up2(c.vv[i]); c.kv[i] = cvtpk(a[0], a[1]); PINV(c.kv[i]); }
    else if constexpr (s & 1) { c.pt = rb2(c.kf[i + (s + 1) / 2] * c.sc[3 - (s + 1) / 2]); PINV(c.pt); }
    else { c.pa = rb2(c.pa + c.pt); PINV(c.pa); }
  } else {
    constexpr unsigned sel = m == 78 ? 0x05040100u : 0x07060302u;
    const unsigned tid = c.wid * 64u + (c.vlane >> 4), xo = (((c.b / 7u) * DM + 64u * (c.b % 7u) + 2u * (tid & 31u)) * 64u + 8u * (tid >> 5)) * 2u;
    st16w<(m - 78) * 128>((u32x4){__builtin_amdgcn_perm(c.kv[6], c.kv[7], sel), __builtin_amdgcn_perm(c.kv[4], c.kv[5], sel), __builtin_amdgcn_perm(c.kv[2], c.kv[3], sel), __builtin_amdgcn_perm(c.kv[0], c.kv[1], sel)},
                          mk_srd(P.ws + WS_XT, SEQ * DM * 2), xo, 0u);
  }
}
template <int i> DI void k1_preload(K1C& c, const Params& P) {
  const unsigned tid = c.wid * 64u + (c.vlane >> 4), bc0 = 64u * (c.b % 7u) + 2u * (tid & 31u), vo = ((64u * (c.b / 7u) + 8u * (tid >> 5)) * DM + bc0) * 2u;
  if constexpr (i < 11) ld4(c.kw[i], mk_srd(P.bk, SEQ * DM * 2), vo + (unsigned)((i - 3) * DM * 2));
  else if constexpr (i < 19) ld4(c.vv[i - 11], mk_srd(P.bv, SEQ * DM * 2), vo + (unsigned)((i - 11) * DM * 2));
  else if constexpr (i == 19) asm volatile("buffer_load_dwordx4 %0, %1, %2, 0 offen" : "=v"(c.sk) : "v"(bc0 * 8u), "s"(mk_srd(P.shk, DM * 8)) : "memory");
  else asm volatile("buffer_load_dwordx2 %0, off, %1, 0 sc1" : "=v"(c.ep) : "s"(mk_srd(P.ws + WS_FL, 64)) : "memory");
}
template <bool WA, int STEP, int buf, int G> DI void k1_gap(K1C& c, const Params& P, unsigned sw, unsigned sx) {
  constexpr int ks = G >> 6, g = G & 63, nb = g >> 3, mb = g & 7;
  Frags& fc = ks ? c.f1 : c.f0; Frags& fn = ks ? c.f0 : c.f1;
  if constexpr (G == 64) {
    asm volatile("s_waitcnt vmcnt(%0) lgkmcnt(0)\n\ts_barrier" :: "n"(STEP == 0 ? 3 : STEP == 14 ? 0 : STEP == 15 ? 12 : 1) : "memory");
    SBAR();
  }
  const bf16x8 w = cat(fc.wl[nb], fc.wh[nb]);
  if constexpr (G == 0) asm volatile("s_nop 1" :: "v"(w), "v"(fc.x[0]));
  if constexpr (WA) mfma16a(c.acc[mb][nb], w, fc.x[mb]); else mfma16a(c.acc[nb][mb], fc.x[mb], w);
  SBAR();
  if constexpr (!(g & 1) && g < 48) {
    if constexpr (ks == 0) k1_read<buf, 1, g / 2>(c, fn);
    else if constexpr (STEP != 15) k1_read<buf ^ 1, 0, g / 2>(c, fn);
  }
  if constexpr (STEP == 15 && ks == 1 && g >= 36 && !(g & 3)) { if constexpr (g == 36) k1_qx<0, 0>(c); else k1_qw<(g - 40) / 4>(c); }
  if constexpr (STEP == 0 && ks == 0 && (g & 1) && g < 16) k1_piece<1, 8 + (g >> 1)>(c, c.sw_kv + 64u * WRS, c.sx_kv + 128u);
  if constexpr (STEP == 0 && (G == 17 || G == 19 || G == 21)) { k1_touch(c); c.pf += c.pfstep; }
  if constexpr (STEP == 13 && G == 64) { if (c.wid == 3) touch_lds(c.ldsb, c.scode, code_off()); }
  if constexpr (ks == 1) {
    if constexpr (STEP <= 13 && (g & 3) == 1) k1_piece<buf, (g >> 2)>(c, sw, sx);
    if constexpr (STEP == 0 && (g & 3) == 3) k1_preload<(g >> 2)>(c, P);
    if constexpr (STEP == 0 && (g & 3) == 2 && g >= 44) k1_preload<16 + ((g - 44) >> 2)>(c, P);
    if constexpr (STEP == 14 && (g & 1) && g < 60 && (g >> 1) % 5 != 4) { constexpr int j = (g >> 1) - (g >> 1) / 5; k1_qpiece<j / 12, j % 12>(c); }
    if constexpr (STEP == 15 && (g & 3) == 1 && g < 48) k1_qpiece<2, (g >> 2)>(c);
  }
  if constexpr (STEP <= 13 && G == 127) { k1_touch(c); c.pf += c.pfstep; }
  if constexpr (STEP >= 13) { constexpr int X = (STEP - 13) * 128 + G; if constexpr (X >= 56 && X % 4 == 3 && (X - 56) / 4 < 80) k1_pre<(X - 56) / 4>(c, P); }
  SBAR();
}
template <bool WA, int STEP, int buf> DI void k1_step(K1C& c, const Params& P, int sb) {
  const unsigned sw = c.sw_kv + (unsigned)sb * (64u * WRS), sx = c.sx_kv + (unsigned)sb * 128u;
  sfor<128>([&](auto G) AI { k1_gap<WA, STEP, buf, G>(c, P, sw, sx); });
}
#define LPG 3
constexpr int q_lpos(int k) { return k == 0 ? 7 : k == 1 ? 3 : 5; }
constexpr bool q_late(int u) { for (int k = 0; k < LPG; ++k) if (u % 8 == q_lpos(k)) return true; return false; }
constexpr int q_lidx(int u) { for (int k = 0; k < LPG; ++k) if (u % 8 == q_lpos(k)) return LPG * (u / 8) + k; return 0; }
constexpr int q_lunit(int k) { return 8 * (k / (LPG ? LPG : 1)) + q_lpos(k % (LPG ? LPG : 1)); }
constexpr int q_stores(int lo, int hi) { int n = 0; for (int u = 0; u < 32; ++u) n += (!q_late(u) && 6 * u + 5 > lo && 6 * u + 5 < hi); return n; }
constexpr int q_wait(int T) {
  return T == 0 ? 14 + q_stores(-1, 26) : T == 1 ? 12 + q_stores(-1, 58) : (T + 2 <= 6 ? 12 : 0) + q_stores(32 * (T - 2) + 48, 32 * T + 26); }
template <bool WA, int X> DI void k1_qgap(K1C& c, const Params& P) {
  constexpr int T = X >> 5, g = X & 31, ks = g >> 3, nb = g & 7;
  if constexpr (g == 26 && T < 6) {
    asm volatile("s_waitcnt vmcnt(%0) lgkmcnt(0)\n\ts_barrier" :: "n"(q_wait(T)) : "memory"); SBAR();
    if constexpr (T == 1) { if (c.wid == 0 && lane_id() == 0) __hip_atomic_store((gu64*)(P.ws + WS_FL) + FL_S0 + c.b * 16u, c.tag, RLX_AGENT); SBAR(); }
  }
  const bf16x8 w = cat(c.ql[X & 15], c.qh[X & 15]);
  if constexpr (X < 8) mfma32qz(c.qa[nb], w, c.qx[ks]); else mfma32q(c.qa[nb], w, c.qx[ks]);
  SBAR();
  if constexpr (g < 22) { constexpr int f = 6 + g + (g + 4) / 5 - (g > 20 ? 1 : 0); k1_qw<32 * T + f>(c); if constexpr (g % 5 == 0 && g < 20) k1_qw<32 * T + f + 1>(c); }
  if constexpr (g >= 26 && T < 6) k1_qw<32 * (T + 1) + g - 26>(c);
  if constexpr (g == 2 || g == 9 || g == 17) k1_qx<T, ks + 1>(c);
  if constexpr (g == 27 && T < 6) k1_qx<T + 1, 0>(c);
  if constexpr (X >= 26 && !(X & 1) && (X - 26) % 32 < 24 && (X - 26) / 32 + 3 <= 6) k1_qpiece<(X - 26) / 32 + 3, ((X - 26) % 32) / 2>(c);
  constexpr int u = X / 6, ph = X % 6;
  if constexpr (u < 32) {
    constexpr int i = u >> 2, j = u & 3;
    if constexpr (ph == 1) { c.ta = c.acc[i][2 * j]; PINV(c.ta); }
    if constexpr (ph == 2) { c.tb = c.acc[i][2 * j + 1]; PINV(c.tb); }
    if constexpr (ph == 3) { c.pk[0] = cvtpk(c.ta[0], c.ta[1]); c.pk[1] = cvtpk(c.ta[2], c.ta[3]); PINV(c.pk); }
    if constexpr (ph == 4) { c.pk[2] = cvtpk(c.tb[0], c.tb[1]); c.pk[3] = cvtpk(c.tb[2], c.tb[3]); PINV(c.pk); }
    if constexpr (ph == 5 && q_late(u)) { c.ss->late[q_lidx(u)] = __builtin_bit_cast(f32x4, c.pk); PINA(c.ss->late[q_lidx(u)]); }
    if constexpr (ph == 5 && !q_late(u)) st16w<(WA ? (i >> 1) * 16384u + j * 2048u : (i >> 1) * 2048u + j * 16384u) + (i & 1) * 1024u>(c.pk, c.sO, c.vlane, c.so);
  }
  if constexpr (X >= 200 && X < 205) {
    const unsigned tid = c.wid * 64u + (c.vlane >> 4), c0 = 2u * (28u * (c.b & 7u) + (c.b >> 3)); constexpr int ch = (X - 200) >> 1;
    if constexpr (X == 204) asm volatile("buffer_load_dwordx2 %0, %1, %2, 0 offen sc1" : "=v"(c.ss->f0) : "v"((FL_S0 + (7u * (tid & 31u) + (c0 >> 6)) * 16u) * 8u), "s"(mk_srd(P.ws + WS_FL, 1u << 20)) : "memory");
    else if constexpr (X & 1) asm volatile("buffer_load_dwordx2 %0, %1, %2, 0 offen offset:16" : "=v"(c.ss->hB[ch]) : "v"(tid * 16u), "s"(mk_srd(P.ssmk + (size_t)(c0 + ch) * SEQ, c.b < 224u ? SEQ * 2 : 0)) : "memory");
    else asm volatile("buffer_load_dwordx4 %0, %1, %2, 0 offen" : "=v"(c.ss->hA[ch]) : "v"(tid * 16u), "s"(mk_srd(P.ssmk + (size_t)(c0 + ch) * SEQ, c.b < 224u ? SEQ * 2 : 0)) : "memory");
  }
  if constexpr (X >= 196 && X < 200) {
    constexpr int j = X - 196; const unsigned ln = c.vlane >> 4;
    dma16<WO_LDS + j * 1024u, j * 256u>(c.lds4, (ln >> 2) * (DM * 2) + (ln & 3u) * 16u, mk_srd(P.wo, NH * DH * DM * 2), c.hb * (16u * DM * 2) + c.wid * 64u);
  }
  SBAR();
}

template <bool WA> DI void k1_main(K1C& c, const Params& P, const char* pf2, u32x4 (&qo)[16]) {
  sfor<64>([&](auto I) AI { c.acc[I >> 3][I & 7] = f32x4{}; PINA(c.acc[I >> 3][I & 7]); });
  SBAR();
  sfor<8>([&](auto J) AI { k1_piece<1, J>(c, c.sw_kv + 64u * WRS, c.sx_kv + 128u); });
  asm volatile("s_waitcnt vmcnt(8)\n\ts_barrier" ::: "memory");
  sfor<24>([&](auto I) AI { k1_read<0, 0, I>(c, c.f0); });
  SBAR();
  k1_step<WA, 0, 0>(c, P, 2);
#pragma nounroll
  for (int t = 1; t < 13; t += 2) {
    if (t >= 3 && t <= 7) {
      const unsigned l = c.vlane >> 4, w = (c.b * 4u + c.wid) * 32u + (l & 31u);
      const unsigned short* p0 = t == 3 ? P.wq : t == 5 ? P.x : P.ssmk; const unsigned short* p1 = t == 3 ? P.wo : t == 5 ? P.bq : P.ssmk;
      const unsigned n = t == 3 ? DM * WRS : SEQ * DM * 2, o = w * 128u < n ? w * 128u : (w * 128u) & 0xfffffu;
      const char* a = (const char*)(l < 32u ? p0 : p1) + o;
      asm volatile("s_add_u32 m0, %0, 147456\n\ts_nop 0\n\tglobal_load_lds_dword %1, off" :: "s"(c.ldsb), "v"(a) : "memory", "scc");
    }
#define TLBPF 65536u
    if (t == 1) {
      const unsigned i = ((c.b >> 3) * 4u + c.wid) * 64u + (c.vlane >> 4);
      asm volatile("s_add_u32 m0, %0, 147456\n\ts_nop 0\n\tbuffer_load_dword %1, %2, 0 offen sc1 lds" :: "s"(c.ldsb), "v"(i * TLBPF), "s"(mk_srd(P.ws, (unsigned)WS_PF + NH * 64u * 28672u)) : "memory", "scc");
    }
    k1_step<WA, 1, 1>(c, P, t + 2);
    c.pf = t == 9 ? pf2 : c.pf;
    k1_step<WA, 1, 0>(c, P, t + 3);
  }
  k1_step<WA, 13, 1>(c, P, 15);
  {
    const unsigned lane = lane_id(), r = lane & 31u, hh = lane >> 5, q = (lane & 15u) >> 2, p = lane & 3u, blk = (lane >> 4) & 1u;
    sfor<2>([&](auto I) AI { c.vx_q[I] = (lane >> 3) * (DM * 2) + 16u * ((lane & 7u) ^ (4u * I + (lane >> 4))); c.vwq[I] = hh * WRS + 16u * (r ^ ((2u * I + hh) << 2)); });
    sfor<4>([&](auto I) AI {
      c.xq[I] = (lds_cptr)(size_t)(c.ldsb + (32u * c.wid + r) * 128u + 16u * ((2u * I + hh) ^ ((r >> 1) & 7u))); PINV(c.xq[I]);
      c.wq[I] = (lds_cptr)(size_t)(c.ldsb + 65536u + (8u * hh + q) * 512u + ((unsigned)I ^ q) * 64u + 8u * (4u * (p >> 1) + 2u * blk + (p & 1u))); PINV(c.wq[I]);
      c.wq1[I] = c.wq[I] + 65536; PINV(c.wq1[I]); });
  }
  k1_step<WA, 14, 0>(c, P, 0);
  k1_step<WA, 15, 1>(c, P, 0);
  sfor<224>([&](auto X) AI { k1_qgap<WA, X>(c, P); });
  MFMA_DRAIN(); SBAR();
  sfor<8>([&](auto D) AI { PINV(c.qa[D]); qo[2 * D] = pack8w(c.qa[D], 0, QSCALE); qo[2 * D + 1] = pack8w(c.qa[D], 1, QSCALE); PINV(qo[2 * D]); PINV(qo[2 * D + 1]); SBAR(); });
  asm volatile("s_waitcnt vmcnt(0)" : "+v"(c.ss->hA[0]), "+v"(c.ss->hA[1]), "+v"(c.ss->hB[0]), "+v"(c.ss->hB[1]), "+v"(c.ss->f0) :: "memory");
}

DI unsigned long long phase_proj(const Params& P, char* lds, const unsigned wid, const unsigned qblk, u32x4 (&qo)[16], SsmS& ss, srd_t scode) {
  const unsigned lane = lane_id(), wr = wid >> 1, wc = wid & 1;
  const unsigned b = blockIdx.x, xcd = b & 7u, idx = b >> 3;
  const unsigned pm = idx & 7u, which = idx >> 3, h = 2u * xcd + (which >> 1), isV = which & 1u;
  K1C c; c.ss = &ss; c.scode = scode;
  c.ldsb = (unsigned)(size_t)lds; c.lds8 = c.ldsb + wid * 8192u; c.lds4 = c.ldsb + wid * 4096u; c.wid = wid; c.vlane = lane * 16u; c.b = b; c.hb = 16u * h + (idx & 15u);
  const unsigned short* wkv = isV ? P.wuv : P.wuk;
  const bool pw = lane >= 32u;
  unsigned pfa = (192u + 8u * pm + 2u * wid + ((lane >> 2) & 1u)) * WRS + h * (DH * 2) + (lane & 3u) * 128u, pfb = (256u * pm + 64u * which + 16u * wid + (lane & 15u)) * (DC * 2) + 384u;
  PINV(pfa); PINV(pfb);
  c.pf = (pw ? (const char*)wkv : (const char*)P.ckv) + (pw ? pfa : pfb);
  c.pfstep = pw ? 64u * WRS : 128u;
  c.sX = mk_srd(P.ckv, SEQ * DC * 2); c.sW = mk_srd(wkv, DC * WRS); c.sXq = mk_srd(P.x, SEQ * DM * 2); c.sWq = mk_srd(P.wq, DM * WRS);
  sfor<2>([&](auto I) AI { c.vx_kv[I] = (lane >> 3) * (DC * 2) + 16u * ((lane & 7u) ^ (4u * I + (lane >> 4))); });
  sfor<4>([&](auto I) AI { const unsigned f = (2u * (I & 1) + (lane >> 5)) | (4u * (I >> 1)); c.vw[I] = (lane >> 5) * WRS + 16u * ((lane & 31u) ^ (f << 1)); });
  c.sx_kv = (256u * pm + 64u * wid) * (DC * 2); c.sw_kv = 16u * wid * WRS + h * (DH * 2);
  c.sx_q = (128u * qblk + 32u * wid) * (DM * 2);  c.sw_q = c.sw_kv;
  k1_stage<0>(c, 0);
    const unsigned rho = lane & 15u, g = lane >> 4;
  sfor<2>([&](auto I) AI { c.xr_kv[I] = (lds_cptr)lds + (wr * 128u + 16u * (rho >> 3) + (rho & 7u)) * 128u + 16u * ((4u * I + g) ^ (rho >> 1)); PINV(c.xr_kv[I]); });
  { const unsigned q = (lane & 15u) >> 2, p = lane & 3u, f = q | ((g & 1u) << 2);
    sfor<8>([&](auto I) AI { c.wr[I] = (lds_cptr)lds + 65536u + (8u * g + q) * 512u + (8u * wc + ((unsigned)I ^ f)) * 32u + 8u * p; PINV(c.wr[I]); }); }
  c.sO = mk_srd(P.ws + WS_K + (isV ? MB16 : 0u) + (size_t)h * (SEQ * DH * 2), SEQ * DH * 2);
  c.so = (8u * pm + 4u * wr) * 16384u + wc * 8192u;
  pfa = (192u + 4u * (idx & 15u) + wid) * WRS + h * (DH * 2) + (lane & 3u) * 128u; pfb = (128u * qblk + 64u * (idx >> 4) + 16u * wid + (lane & 15u)) * (DM * 2) + 384u;
  PINV(pfa); PINV(pfb);
  const char* pf2 = (pw ? (const char*)P.wq : (const char*)P.x) + (pw ? pfa : pfb);
  ss.sO = c.sO; ss.so = c.so; ss.sA = isV ? 2048u : 16384u; ss.sB = isV ? 16384u : 2048u;
  if (isV) k1_main<false>(c, P, pf2, qo); else k1_main<true>(c, P, pf2, qo);
  return c.tag;
}

constexpr float THR = 8.0f;
constexpr int AO = 0, AQ = 128, AQ2 = 192;
template <int D> DI void pv_mfma(const bf16x8& a, const u32x4& b) { asm volatile("v_mfma_f32_32x32x16_bf16 a[%c2:%c3], %0, %1, a[%c2:%c3]" :: "v"(a), "v"(b), "n"(16 * D), "n"(16 * D + 15)); }
template <int D> DI void pv_mfma4(const bf16x8& a, const u32x4& b, const bf16x8& x, const bf16x8& y, const bf16x8& z) { asm volatile("v_mfma_f32_32x32x16_bf16 a[%c2:%c3], %0, %1, a[%c2:%c3]" :: "v"(a), "v"(b), "n"(16 * D), "n"(16 * D + 15), "v"(x), "v"(y), "v"(z)); }
template <int D> DI void pv_mfmaz(const bf16x8& a, const u32x4& b, const bf16x8& x, const bf16x8& y, const bf16x8& z) { asm volatile("v_mfma_f32_32x32x16_bf16 a[%c2:%c3], %0, %1, 0" :: "v"(a), "v"(b), "n"(16 * D), "n"(16 * D + 15), "v"(x), "v"(y), "v"(z)); }
template <int S> DI void qk_mfma(f32x16& c, const bf16x8& a) { asm volatile("v_mfma_f32_32x32x16_bf16 %0, %1, a[%c2:%c3], %0" : "+v"(c) : "v"(a), "n"(AQ + 4 * S), "n"(AQ + 4 * S + 3)); }
template <int S> DI void qk_mfma4(f32x16& c, const bf16x8& a, const bf16x8& x, const bf16x8& y, const bf16x8& z) { asm volatile("v_mfma_f32_32x32x16_bf16 %0, %1, a[%c2:%c3], %0" : "+v"(c) : "v"(a), "n"(AQ + 4 * S), "n"(AQ + 4 * S + 3), "v"(x), "v"(y), "v"(z)); }
DI void qk_init4(f32x16& d, const bf16x8& a, const f32x16& c, const bf16x8& x, const bf16x8& y, const bf16x8& z) { asm volatile("v_mfma_f32_32x32x16_bf16 %0, %1, a[128:131], %2" : "=&v"(d) : "v"(a), "v"(c), "v"(x), "v"(y), "v"(z)); }
template <int R> DI void acc_zero4() { asm volatile("v_accvgpr_write_b32 a[%c0], 0\n\tv_accvgpr_write_b32 a[%c1], 0\n\tv_accvgpr_write_b32 a[%c2], 0\n\tv_accvgpr_write_b32 a[%c3], 0" :: "n"(R), "n"(R + 1), "n"(R + 2), "n"(R + 3)); }
template <int R> DI void acc_write4(const u32x4& w) { asm volatile("v_accvgpr_write_b32 a[%c4], %0\n\tv_accvgpr_write_b32 a[%c5], %1\n\tv_accvgpr_write_b32 a[%c6], %2\n\tv_accvgpr_write_b32 a[%c7], %3" :: "v"(w[0]), "v"(w[1]), "v"(w[2]), "v"(w[3]), "n"(R), "n"(R + 1), "n"(R + 2), "n"(R + 3)); }
template <int R, int S> DI void acc_mov4() { asm volatile("v_accvgpr_mov_b32 a[%c0], a[%c4]\n\tv_accvgpr_mov_b32 a[%c1], a[%c5]\n\tv_accvgpr_mov_b32 a[%c2], a[%c6]\n\tv_accvgpr_mov_b32 a[%c3], a[%c7]" :: "n"(R), "n"(R + 1), "n"(R + 2), "n"(R + 3), "n"(S), "n"(S + 1), "n"(S + 2), "n"(S + 3)); }
template <int R> DI f32x4 acc_read4() { float a, b, c, d; asm volatile("v_accvgpr_read_b32 %0, a[%c4]\n\tv_accvgpr_read_b32 %1, a[%c5]\n\tv_accvgpr_read_b32 %2, a[%c6]\n\tv_accvgpr_read_b32 %3, a[%c7]" : "=v"(a), "=v"(b), "=v"(c), "=v"(d) : "n"(R), "n"(R + 1), "n"(R + 2), "n"(R + 3)); return (f32x4){a, b, c, d}; }
template <int R> DI void acc_scale2(float f) { float t, u; asm volatile("v_accvgpr_read_b32 %0, a[%c3]\n\tv_accvgpr_read_b32 %1, a[%c4]\n\tv_mul_f32 %0, %2, %0\n\tv_mul_f32 %1, %2, %1\n\tv_accvgpr_write_b32 a[%c3], %0\n\tv_accvgpr_write_b32 a[%c4], %1" : "=&v"(t), "=&v"(u) : "v"(f), "n"(R), "n"(R + 1)); }
template <int R, unsigned SI> DI void acc_store4(srd_t srd, unsigned voff) { unsigned t; asm volatile("s_mov_b32 %0, %3\n\tbuffer_store_dwordx4 a[%c4:%c5], %1, %2, %0 offen sc1" : "=&s"(t) : "v"(voff), "s"(srd), "n"(SI), "n"(R), "n"(R + 3) : "memory"); }
template <int R, unsigned SI> DI void acc_load4(srd_t srd, unsigned voff) { unsigned t; asm volatile("s_mov_b32 %0, %3\n\tbuffer_load_dwordx4 a[%c4:%c5], %1, %2, %0 offen sc1" : "=&s"(t) : "v"(voff), "s"(srd), "n"(SI), "n"(R), "n"(R + 3) : "memory"); }
#define A8(n) "a" #n "0", "a" #n "1", "a" #n "2", "a" #n "3", "a" #n "4", "a" #n "5", "a" #n "6", "a" #n "7", "a" #n "8", "a" #n "9"
#define ACC_CLAIM() asm volatile("" ::: "a0", "a1", "a2", "a3", "a4", "a5", "a6", "a7", "a8", "a9", A8(1), A8(2), A8(3), A8(4), A8(5), A8(6), A8(7), A8(8), A8(9), A8(10), A8(11), A8(12), A8(13), A8(14), \
  A8(15), A8(16), A8(17), A8(18), A8(19), A8(20), A8(21), A8(22), A8(23), A8(24), "a250", "a251", "a252", "a253", "a254", "a255")
DI float max3f(float a, float b, float c) { float r; asm("v_max3_f32 %0, %1, %2, %3" : "=v"(r) : "v"(a), "v"(b), "v"(c)); return r; }
DI float max2f(float a, float b) { float r; asm("v_max_f32_e32 %0, %1, %2" : "=v"(r) : "v"(a), "v"(b)); return r; }
DI float fadd_s(float a, float b) { float r; asm volatile("v_add_f32_e32 %0, %1, %2" : "=v"(r) : "v"(a), "v"(b)); return r; }
DI float fsub_s(float a, float b) { float r; asm("v_sub_f32_e32 %0, %1, %2" : "=v"(r) : "v"(a), "v"(b)); return r; }
constexpr int koff(int f) { return (f >> 1) * 2048 + (f & 1) * 512; }
constexpr int voff(int f) { return (f >> 4) * 16384 + (f & 7) * 2048 + ((f >> 3) & 1) * 512; }
struct K2C {
  unsigned ldsb, vlane;
  srd_t sK;
  lds_cptr kr, vr;
  unsigned kslot_w, vslot_w, vslot_r;
  unsigned ksoff, vsoff, dt;
  f32x16 s0, s1, negm; u32x4 p[4]; bf16x8 fr[16];
  float m, l0, l1, mx0, mx1, fct, thr, mS, lS; bool pend;
  f32x4 ml, ob[32];
  int sw; srd_t sP;
  int dq, rel;
};
template <int J> DI void k2_dma(unsigned& t, unsigned m0v, unsigned voff, srd_t srd, unsigned soff) {
  if constexpr (J == 0) asm volatile("s_mov_b32 m0, %1\n\ts_mov_b32 %0, %4\n\tbuffer_load_dwordx4 %2, %3, %0 offen sc1 lds" : "=&s"(t) : "s"(m0v), "v"(voff), "s"(srd), "s"(soff) : "memory");
  else if constexpr (J == 4) asm volatile("s_add_u32 m0, m0, 4096\n\ts_add_u32 %0, %0, 4096\n\tbuffer_load_dwordx4 %1, %2, %0 offen sc1 lds" : "+s"(t) : "v"(voff), "s"(srd) : "memory", "scc");
  else asm volatile("buffer_load_dwordx4 %0, %1, %2 offen offset:%3 sc1 lds" :: "v"(voff), "s"(srd), "s"(t), "n"((J & 3) * 1024) : "memory");
}
DI void k2_dma8(unsigned m0v, unsigned voff, srd_t srd, unsigned soff) { unsigned t; sfor<8>([&](auto J) AI { k2_dma<J>(t, m0v, voff, srd, soff); }); }
template <int I> DI void k2_mask4(f32x16& s, int d, float ninf) {
  constexpr int r = 4 * I; float a = s[r], b = s[r + 1], c = s[r + 2], e = s[r + 3];
  asm volatile("v_cmp_gt_i32 vcc, %5, %4\n\tv_cmp_gt_i32 s[4:5], %6, %4\n\tv_cmp_gt_i32 s[6:7], %7, %4\n\tv_cmp_gt_i32 s[8:9], %8, %4\n\t"
               "v_cndmask_b32 %0, %0, %9, vcc\n\tv_cndmask_b32 %1, %1, %9, s[4:5]\n\tv_cndmask_b32 %2, %2, %9, s[6:7]\n\tv_cndmask_b32 %3, %3, %9, s[8:9]"
               : "+v"(a), "+v"(b), "+v"(c), "+v"(e) : "v"(d), "n"(8 * I), "n"(8 * I + 1), "n"(8 * I + 2), "n"(8 * I + 3), "v"(ninf) : "vcc", "s4", "s5", "s6", "s7", "s8", "s9");
  s[r] = a; s[r + 1] = b; s[r + 2] = c; s[r + 3] = e;
}
DI void k2_mask(f32x16& s, int rel, int dq) {
  const float ninf = -__builtin_inff(); const int d = rel > 0 ? -100 : dq;
  k2_mask4<0>(s, d, ninf); k2_mask4<1>(s, d, ninf); k2_mask4<2>(s, d, ninf); k2_mask4<3>(s, d, ninf);
}
DI void max4(float& m, float a, float b, float c, float d) { asm volatile("v_max3_f32 %0, %0, %1, %2\n\tv_max3_f32 %0, %0, %3, %4" : "+v"(m) : "v"(a), "v"(b), "v"(c), "v"(d)); }
DI void max6(float& m, float a, float b, float c, float d, float e, float f) { asm volatile("v_max3_f32 %0, %0, %1, %2\n\tv_max3_f32 %0, %0, %3, %4\n\tv_max3_f32 %0, %0, %5, %6" : "+v"(m) : "v"(a), "v"(b), "v"(c), "v"(d), "v"(e), "v"(f)); }
DI float max4i(float a, float b, float c, float d) { float m; asm volatile("v_max_f32_e32 %0, %1, %2\n\tv_max3_f32 %0, %0, %3, %4" : "=&v"(m) : "v"(a), "v"(b), "v"(c), "v"(d)); return m; }
template <int e> DI void k2_lsum(K2C& c, float v) { if constexpr (e & 1) c.l1 = fadd_s(c.l1, v); else c.l0 = fadd_s(c.l0, v); }
template <bool FIN, bool QK, bool FL, int g> DI void k2_gapA(K2C& c) {
  if constexpr (QK) {
    if constexpr (g == 0) qk_init4(c.s0, c.fr[0], c.negm, c.fr[1], c.fr[2], c.fr[3]);
    else if constexpr (g == 16) qk_init4(c.s1, c.fr[0], c.negm, c.fr[1], c.fr[2], c.fr[3]);
    else if constexpr ((g & 3) == 0) qk_mfma4<g & 15>(g < 16 ? c.s0 : c.s1, c.fr[g & 15], c.fr[(g + 1) & 15], c.fr[(g + 2) & 15], c.fr[(g + 3) & 15]);
    else qk_mfma<g & 15>(g < 16 ? c.s0 : c.s1, c.fr[g & 15]);
    SBAR();
    if constexpr (g < 24) c.fr[(g + 8) & 15] = lds_rd128(c.kr + koff((g + 8)));
  }
  if constexpr (!QK && g <= 9) { asm volatile("s_nop 0"); SBAR(); }
  if constexpr (!FIN) acc_zero4<4 * g>();
  if constexpr (FIN && (QK ? g >= 24 : g < 8)) c.fr[g & 7] = lds_rd128(c.vr + voff((g & 7)));
  if constexpr (FIN && g <= 8) {
    if constexpr (g < 8) c.s1[8 + g] = __builtin_amdgcn_exp2f(c.s1[8 + g]);
    k2_lsum<g>(c, c.s1[7 + g]);
    if constexpr (g < 4) c.p[2][g] = cvtpk(c.s1[2 * g], c.s1[2 * g + 1]);
    else if constexpr (g < 7) c.p[3][g - 4] = cvtpk(c.s1[2 * g], c.s1[2 * g + 1]);
    else if constexpr (g == 8) c.p[3][3] = cvtpk(c.s1[14], c.s1[15]);
    if constexpr (g < 8) PINV(c.s1);
    if constexpr (g < 4) PINV(c.p[2]); else PINV(c.p[3]);
  }
  if constexpr (FIN && (QK ? g < 24 && (g & 3) == 1 : g < 6)) k2_dma<(QK ? g / 4 : g) + 2>(c.dt, 0u, c.vlane, c.sK, 0u);
  if constexpr (FL) {
    acc_store4<4 * g, 1024 * g>(c.sP, c.vlane);
    if constexpr (g == 12) { float a = c.mS, b = c.lS; PINV(a); PINV(b); const u32x4 v = {__float_as_uint(a), __float_as_uint(b), 0u, 0u}; st16w<32 * 1024>(v, c.sP, c.vlane, 0u); }
  }
  if constexpr (QK) {
    if constexpr (g >= 24 && (g & 3) == 1) k2_dma<(g - 24) / 4>(c.dt, c.ldsb + 65536u + c.vslot_w, c.vlane, c.sK, c.vsoff);
    if constexpr (g == 18) { if (c.rel >= 0) k2_mask(c.s0, c.rel, c.dq); }
    if constexpr (g == 20) { c.mx0 = max4i(c.s0[0], c.s0[1], c.s0[2], c.s0[3]); }
    if constexpr (g >= 21 && g < 24) { constexpr int r = 4 * (g - 20); max4(c.mx0, c.s0[r], c.s0[r + 1], c.s0[r + 2], c.s0[r + 3]); }
  }
  SBAR();
}
template <bool FIN, bool QK, bool FL = false> DI void k2_phaseA(K2C& c) {
  if constexpr (QK && !FIN) {
#pragma unroll
    for (int i = 0; i < 8; ++i) c.fr[i] = lds_rd128(c.kr + koff(i));
  }
  sfor<32>([&](auto G) AI { k2_gapA<FIN, QK, FL, G>(c); });
}
template <bool PV, bool SM, bool FL, int g> DI void k2_gapB(K2C& c) {
  if constexpr (SM && g == 24) {
    asm volatile("s_waitcnt vmcnt(%0) lgkmcnt(0)\n\ts_barrier" :: "n"(FL ? 6 : 8) : "memory");
    c.kr = (lds_cptr)(size_t)((unsigned)(size_t)c.kr ^ 32768u);
    SBAR();
  }
  if constexpr (PV) {
    if constexpr (SM && FL && g < 8) pv_mfmaz<g>(c.fr[g], c.p[0], c.fr[g | 1], c.fr[g | 2], c.fr[g | 3]);
    else if constexpr ((g & 3) == 0) pv_mfma4<g & 7>(c.fr[g & 15], c.p[g >> 3], c.fr[(g + 1) & 15], c.fr[(g + 2) & 15], c.fr[(g + 3) & 15]);
    else pv_mfma<g & 7>(c.fr[g & 15], c.p[g >> 3]);
    SBAR();
    if constexpr (g < 2) { c.fr[(8 + 2 * g) & 15] = lds_rd128(c.vr + voff((8 + 2 * g))); c.fr[(9 + 2 * g) & 15] = lds_rd128(c.vr + voff((9 + 2 * g))); }
    else if constexpr (g < 8) c.fr[(g + 10) & 15] = lds_rd128(c.vr + voff((g + 10)));
    else if constexpr (g < 10) { c.fr[(2 * g + 2) & 15] = lds_rd128(c.vr + voff((2 * g + 2))); c.fr[(2 * g + 3) & 15] = lds_rd128(c.vr + voff((2 * g + 3))); }
    else if constexpr (g < 20) c.fr[(g + 12) & 15] = lds_rd128(c.vr + voff((g + 12)));
  }
  if constexpr (!SM && FL) c.ob[g] = __builtin_bit_cast(f32x4, __builtin_amdgcn_raw_buffer_load_b128(c.sP, c.vlane, g * 1024, 16));
  if constexpr (!PV && g >= 7) { asm volatile("s_nop 0"); SBAR(); }
  if constexpr (SM) {
    if constexpr (g >= 24) c.fr[(g + 8) & 15] = lds_rd128(c.kr + koff((g - 24)));
    if constexpr ((g & 3) == 1) { if constexpr (g < 24) k2_dma<g / 4 + 2>(c.dt, 0u, c.vlane, c.sK, 0u); else k2_dma<(g - 24) / 4>(c.dt, c.ldsb + c.kslot_w, c.vlane, c.sK, c.ksoff); }
    if constexpr (g == 2) { if (c.rel + 32 >= 0) k2_mask(c.s1, c.rel + 32, c.dq); }
    if constexpr (g == 3) { max6(c.mx0, c.s1[0], c.s1[1], c.s1[2], c.s1[3], c.s1[4], c.s1[5]); }
    if constexpr (g == 4) { max6(c.mx0, c.s1[6], c.s1[7], c.s1[8], c.s1[9], c.s1[10], c.s1[11]); }
    if constexpr (g == 5) { max4(c.mx0, c.s1[12], c.s1[13], c.s1[14], c.s1[15]); c.mx1 = c.mx0; PINV(c.mx1); }
    if constexpr (g == 6) {
      if constexpr (!PV) asm volatile("s_nop 1");
      auto pr = __builtin_amdgcn_permlane32_swap(__float_as_uint(c.mx0), __float_as_uint(c.mx1), false, false);
      const float rm = max2f(__uint_as_float(pr[0]), __uint_as_float(pr[1]));
      if constexpr (!PV) {
        const float dl = max2f(rm, -1e30f); c.m = dl;
#pragma unroll
        for (int r = 0; r < 16; ++r) { c.s0[r] = fsub_s(c.s0[r], dl); c.s1[r] = fsub_s(c.s1[r], dl); c.negm[r] = -dl; }
        PINV(c.negm);
      } else if (__builtin_expect(__any(rm > c.thr), 0)) {
        float dl;
        if (!FL && c.sw == 1) {
          float l = c.l0 + c.l1; auto pl = __builtin_amdgcn_permlane32_swap(__float_as_uint(l), __float_as_uint(l), false, false);
          c.mS = c.m; c.lS = __uint_as_float(pl[0]) + __uint_as_float(pl[1]); c.l0 = 0.f; c.l1 = 0.f;
          dl = max2f(rm, -1e30f); c.m = dl;
        } else {
          dl = max2f(rm, 0.f); c.m = fadd_s(c.m, dl); c.fct = __builtin_amdgcn_exp2f(-dl);
          c.l0 *= c.fct; c.l1 *= c.fct; c.pend = true;
        }
#pragma unroll
        for (int r = 0; r < 16; ++r) { c.s0[r] = fsub_s(c.s0[r], dl); c.s1[r] = fsub_s(c.s1[r], dl); c.negm[r] = -c.m; }
      }
    }
    if constexpr (g >= 7 && g < 23) c.s0[g - 7] = __builtin_amdgcn_exp2f(c.s0[g - 7]);
    if constexpr (g >= 8 && g < 24) k2_lsum<g>(c, c.s0[g - 8]);
    if constexpr (g >= 24) c.s1[g - 24] = __builtin_amdgcn_exp2f(c.s1[g - 24]);
    if constexpr (g >= 25) k2_lsum<g>(c, c.s1[g - 25]);
    if constexpr (g >= 16 && g < 24) { constexpr int w = g - 16; c.p[w >> 2][w & 3] = cvtpk(c.s0[2 * w], c.s0[2 * w + 1]); PINV(c.p[w >> 2]); }
    if constexpr (g >= 7 && g < 23) PINV(c.s0);
    if constexpr (g >= 24) PINV(c.s1);
  }
  SBAR();
}
template <bool PV, bool SM, bool FL = false> DI void k2_phaseB(K2C& c) {
  if constexpr (!PV) { MFMA_DRAIN(); SBAR(); }
  sfor<32>([&](auto G) AI { k2_gapB<PV, SM, FL, G>(c); });
  if constexpr (PV && SM) {
    if (__builtin_expect(c.pend, 0)) {
      MFMA_DRAIN(); SBAR();
      sfor<64>([&](auto I) AI { acc_scale2<2 * I>(c.fct); });
      c.pend = false;
    }
    SBAR();
  }
}

DI void mfma32_z4(f32x16& d, const bf16x8& a, const u32x4& b, const bf16x8& x, const bf16x8& y, const bf16x8& z) { asm volatile("v_mfma_f32_32x32x16_bf16 %0, %1, %2, 0" : "=&v"(d) : "v"(a), "v"(b), "v"(x), "v"(y), "v"(z)); }
DI void mfma32_vv4(f32x16& c, const bf16x8& a, const u32x4& b, const bf16x8& x, const bf16x8& y, const bf16x8& z) { asm volatile("v_mfma_f32_32x32x16_bf16 %0, %1, %2, %0" : "+v"(c) : "v"(a), "v"(b), "v"(x), "v"(y), "v"(z)); }
DI void mfma32_vv(f32x16& c, const bf16x8& a, const u32x4& b) { asm volatile("v_mfma_f32_32x32x16_bf16 %0, %1, %2, %0" : "+v"(c) : "v"(a), "v"(b)); }
struct K2E { u32x4 cx[16]; lds_cptr rc, rn; unsigned slotw, wsoff; srd_t sP; u32x4 pk; };
template <int CH, int g> DI void k2_gapC(K2C& c, K2E& e) {
  if constexpr (CH == 3 && g == 24) { asm volatile("s_waitcnt vmcnt(6)\n\ts_barrier" ::: "memory"); SBAR(); }
  if constexpr ((g & 15) == 0) mfma32_z4(g < 16 ? c.s0 : c.s1, c.fr[0], e.cx[0], c.fr[1], c.fr[2], c.fr[3]);
  else if constexpr ((g & 3) == 0) mfma32_vv4(g < 16 ? c.s0 : c.s1, c.fr[g & 15], e.cx[g & 15], c.fr[(g + 1) & 15], c.fr[(g + 2) & 15], c.fr[(g + 3) & 15]);
  else mfma32_vv(g < 16 ? c.s0 : c.s1, c.fr[g & 15], e.cx[g & 15]);
  SBAR();
  if constexpr (CH < 2 && g == 28) { asm volatile("s_barrier" ::: "memory"); SBAR(); }
  if constexpr (g < 24) c.fr[(g + 8) & 15] = lds_rd128(e.rc + (g + 8) * 1024);
  else if constexpr (CH < 6) c.fr[(g + 8) & 15] = lds_rd128(e.rn + (g - 24) * 1024);
  if constexpr (CH < 3 && g >= 8 && g < 16) k2_dma<g - 8>(c.dt, c.ldsb + e.slotw, c.vlane, c.sK, e.wsoff);
  constexpr int u = (g & 15) - 2;
  if constexpr (u >= 0 && u < 6 && (g >= 16 || CH > 0)) {
    const f32x16& t = g < 16 ? c.s1 : c.s0; constexpr int n = 2 * CH + (g < 16 ? -1 : 0), a = u / 3, i = u % 3;
    if constexpr (i < 2) { e.pk[2 * i] = cvtpk(t[8 * a + 4 * i], t[8 * a + 4 * i + 1]); e.pk[2 * i + 1] = cvtpk(t[8 * a + 4 * i + 2], t[8 * a + 4 * i + 3]); PINV(e.pk); }
    else st16w<(2 * n + a) * 1024>(e.pk, e.sP, c.vlane, 0u);
  }
  SBAR();
}
template <int CH, int... Gs> DI void k2_phaseC_(K2C& c, K2E& e, iseq<Gs...>) { (k2_gapC<CH, Gs>(c, e), ...); }
template <int CH> DI void k2_phaseC(K2C& c, K2E& e) { k2_phaseC_<CH>(c, e, std::make_integer_sequence<int, 32>{}); }

DI void phase_attn(const Params& P, char* lds, unsigned long long tag, const unsigned wid, u32x4 (&qo)[16], srd_t scode) {
  const char *KVW = P.ws + WS_K; char *Pf = P.ws + WS_PF, *part = P.ws + WS_PART; gu64* flags = (gu64*)(P.ws + WS_FL);
  const unsigned b = blockIdx.x, xcd = b & 7u, idx = b >> 3, h = 2u * xcd + (idx >> 4), pi = (idx >> 1) & 7u, roleB = idx & 1u;
  const unsigned pair = h * 8u + pi;
  const int NA = pi == 0 ? 18 : 17;
  const int n1 = roleB ? 32 - 2 * (int)pi - NA : NA, tb1 = roleB ? NA : 0, ntot = roleB ? 34 - NA : NA;
  const unsigned wbase = 2u * MB16 + h * (DH * DM * 2) + wid * 8192u;
  auto tile_off = [&](int p) AI -> unsigned { return h * (SEQ * DH * 2) + (unsigned)(p < n1 ? tb1 + p : p - n1) * 32768u + wid * 8192u; };
  auto k_off = [&](int p) AI -> unsigned { return p >= ntot ? wbase + (unsigned)(p - ntot) * 32768u : tile_off(p); };
  auto v_off = [&](int p) AI -> unsigned { return p >= ntot ? wbase + 2u * 32768u : MB16 + tile_off(p); };
  K2C c;
  c.ldsb = (unsigned)(size_t)lds + wid * 8192u; c.vlane = lane_id() * 16u;
  c.sK = mk_srd(KVW, 2u * MB16 + NH * DH * DM * 2);
  unsigned qb = 15u - pi;
  k2_dma8(c.ldsb, c.vlane, c.sK, k_off(0));
  ACC_CLAIM();
  if (roleB) {
    const srd_t sq = mk_srd(P.ws + ((size_t)pair * 4u + wid) * 16384u, 16384u);
    sfor<16>([&](auto I) AI { acc_load4<AQ + 4 * I, I * 1024>(sq, c.vlane); });
  }
  k2_dma8(c.ldsb + 32768u, c.vlane, c.sK, k_off(1)); k2_dma8(c.ldsb + 65536u, c.vlane, c.sK, v_off(0));
  if (roleB) sfor<16>([&](auto I) AI { acc_write4<AQ2 + 4 * I>(qo[I]); }); else sfor<16>([&](auto I) AI { acc_write4<AQ + 4 * I>(qo[I]); });
  SBAR();
  const unsigned lane = lane_id(), hi = lane >> 5, r32 = lane & 31u;
  const unsigned klane = ((r32 >> 3) & 1u) * 1024u + hi * 256u + ((r32 & 7u) + 8u * (r32 >> 4)) * 16u, vlane_r = (r32 >> 4) * 1024u + hi * 256u + (r32 & 15u) * 16u;
  c.dq = (int)r32 - 4 * (int)hi;
  c.kr = (lds_cptr)lds + klane; c.kslot_w = 0u; c.vslot_w = 32768u; c.vslot_r = 0u;
  asm volatile("s_waitcnt vmcnt(16)\n\ts_barrier" ::: "memory");
  int p = 0;
  c.sP = mk_srd(part + ((size_t)pair * 4u + wid) * 33792u, 33792u);
  const int p1 = roleB ? n1 - 1 : -8;
  int rel = 64 * tb1 - (int)(qb * 128u + wid * 32u);
  auto setup = [&]() AI { c.vsoff = v_off(p + 1); c.ksoff = k_off(p + 2); c.rel = rel; c.sw = p - p1; c.thr = c.sw == 1 ? -3e38f : THR; };
  auto advance = [&]() AI {
    c.kslot_w ^= 32768u; c.vslot_w = c.vslot_w == 65536u ? 0u : c.vslot_w + 32768u; rel += 64;
    c.vr = (lds_cptr)lds + 65536u + c.vslot_r + vlane_r; c.vslot_r = c.vslot_r == 65536u ? 0u : c.vslot_r + 32768u;
    if (p == p1) {
      sfor<16>([&](auto I) AI { acc_mov4<AQ + 4 * I, AQ2 + 4 * I>(); });
      c.negm = f32x16{}; PINV(c.negm); asm volatile("s_nop 1");
      rel = -(int)(pi * 128u + wid * 32u);
    }
    ++p;
  };
  c.negm = f32x16{}; PINV(c.negm); c.l0 = 0.f; c.l1 = 0.f; c.pend = false; c.fct = 1.f; c.mS = 0.f; c.lS = 0.f;
  asm volatile("s_nop 1"); SBAR();
  setup();
  k2_phaseA<false, true>(c); k2_phaseB<false, true>(c);
  advance();
  u32x2 f2 = {0u, 0u}; unsigned sink = 0u;
  int stop = roleB ? p1 + 2 : ntot;
  for (;;) {
#pragma nounroll
    while (p < stop) {
      setup();
      if (!roleB && p == ntot - 1) asm volatile("buffer_load_dwordx2 %0, off, %1, 0 sc1" : "=v"(f2) : "s"(mk_srd(P.ws + WS_FL + (FL_S2 + pair * 16u) * 8u, 8u)) : "memory");
      if (p == ntot - 3 && wid == 3) ld4(sink, scode, code_off());
      k2_phaseA<true, true>(c); k2_phaseB<true, true>(c);
      PINV(f2); PINV(sink);
      advance();
    }
    if (p >= ntot) break;
    setup();
    k2_phaseA<true, true, true>(c); k2_phaseB<true, true, true>(c);
    if (wid == 0 && lane == 0) __hip_atomic_store(flags + FL_S2 + pair * 16u, tag, RLX_AGENT);
    advance();
    stop = ntot;
  }
  qb = roleB ? pi : qb;
  const bool early = !roleB && rfl(f2[0]) == (unsigned)tag && rfl(f2[1]) == (unsigned)(tag >> 32);
  SBAR();
  k2_phaseA<true, false>(c);
  k2_dma8(c.ldsb + 65536u + c.vslot_w, c.vlane, c.sK, wbase + 3u * 32768u);
  if (early) { c.ml = __builtin_bit_cast(f32x4, __builtin_amdgcn_raw_buffer_load_b128(c.sP, c.vlane, 32 * 1024, 16)); SBAR(); k2_phaseB<true, false, true>(c); } else k2_phaseB<true, false>(c);
  MFMA_DRAIN(); SBAR();
  float l = c.l0 + c.l1;
  { auto pr = __builtin_amdgcn_permlane32_swap(__float_as_uint(l), __float_as_uint(l), false, false); l = __uint_as_float(pr[0]) + __uint_as_float(pr[1]); }
  K2E e;
  if (roleB) {
    const float rl = 1.0f / l;
    sfor<16>([&](auto I) AI { e.cx[I] = pack8(acc_read4<8 * I>(), acc_read4<8 * I + 4>(), rl); PINV(e.cx[I]); });
  } else {
    if (!early) {
      wait_flags(flags + FL_S2 + pair * 16u, tag);
      c.ml = __builtin_bit_cast(f32x4, __builtin_amdgcn_raw_buffer_load_b128(c.sP, c.vlane, 32 * 1024, 16));
      sfor<32>([&](auto K) AI { c.ob[K] = __builtin_bit_cast(f32x4, __builtin_amdgcn_raw_buffer_load_b128(c.sP, c.vlane, K * 1024, 16)); });
    }
    const f32x4 ml = c.ml;
    SBAR();
    const float mm = fmaxf(c.m, ml[0]), wa = __builtin_amdgcn_exp2f(c.m - mm), wb = __builtin_amdgcn_exp2f(ml[0] - mm);
    const float rl = 1.0f / (wa * l + wb * ml[1]), fa = wa * rl, fb = wb * rl;
    sfor<16>([&](auto I) AI { e.cx[I] = pack8(acc_read4<8 * I>() * fa + c.ob[2 * I] * fb, acc_read4<8 * I + 4>() * fa + c.ob[2 * I + 1] * fb, 1.0f); PINV(e.cx[I]); });
  }
  e.sP = mk_srd(Pf + ((size_t)h * 64u + qb * 4u + wid) * 28672u, 28672u);
  const unsigned kA = c.kslot_w, kB = c.kslot_w ^ 32768u;
  const unsigned v2 = 65536u + c.vslot_r, v3 = 65536u + c.vslot_w, v4 = 65536u + (98304u - c.vslot_r - c.vslot_w);
  const lds_cptr lb = (lds_cptr)lds + c.vlane;
  asm volatile("s_waitcnt vmcnt(0)\n\ts_barrier" ::: "memory");
  e.rc = lb + kA;
#pragma unroll
  for (int i = 0; i < 8; ++i) c.fr[i] = lds_rd128(e.rc + i * 1024);
  e.rn = lb + kB; e.slotw = v4; e.wsoff = wbase + 4u * 32768u; SBAR(); k2_phaseC<0>(c, e);
  e.rc = e.rn; e.rn = lb + v2; e.slotw = kA; e.wsoff = wbase + 5u * 32768u; SBAR(); k2_phaseC<1>(c, e);
  e.rc = e.rn; e.rn = lb + v3; e.slotw = kB; e.wsoff = wbase + 6u * 32768u; SBAR(); k2_phaseC<2>(c, e);
  e.rc = e.rn; e.rn = lb + v4; SBAR(); k2_phaseC<3>(c, e);
  e.rc = e.rn; e.rn = lb + kA; SBAR(); k2_phaseC<4>(c, e);
  e.rc = e.rn; e.rn = lb + kB; SBAR(); k2_phaseC<5>(c, e);
  e.rc = e.rn; SBAR(); k2_phaseC<6>(c, e);
  MFMA_DRAIN(); PINV(c.s1); SBAR();
  st16w<26 * 1024>(pack8w(c.s1, 0, 1.0f), e.sP, c.vlane, 0u); st16w<27 * 1024>(pack8w(c.s1, 1, 1.0f), e.sP, c.vlane, 0u);
}
constexpr int OUTW = 2 * DM;
DI void reduce_load(u32x4 (&v)[16], unsigned task, srd_t sP, unsigned lane) {
#pragma unroll
  for (int h = 0; h < 16; ++h) v[h] = __builtin_amdgcn_raw_buffer_load_b128(sP, lane * 16u, task * 1024u + h * (64u * 28672u), 16);
}
DI void reduce_sum(const u32x4 (&v)[16], unsigned task, unsigned lane, unsigned short* out) {
  const unsigned rb = task / 28u, f = task % 28u;
  float s[8];
#pragma unroll
  for (int i = 0; i < 8; ++i) {
    float a[16];
#pragma unroll
    for (int h = 0; h < 16; ++h) a[h] = (i & 1) ? __uint_as_float(v[h][i >> 1] & 0xffff0000u) : __uint_as_float(v[h][i >> 1] << 16);
    s[i] = (((a[0] + a[1]) + (a[2] + a[3])) + ((a[4] + a[5]) + (a[6] + a[7]))) + (((a[8] + a[9]) + (a[10] + a[11])) + ((a[12] + a[13]) + (a[14] + a[15])));
  }
  *(u32x4*)(out + (size_t)(32u * rb + (lane & 31u)) * OUTW + 16u * f + 8u * (lane >> 5)) = (u32x4){cvtpk(s[0], s[1]), cvtpk(s[2], s[3]), cvtpk(s[4], s[5]), cvtpk(s[6], s[7])};
}

constexpr int HCOPY = 4160, HCH = 4 * HCOPY, XROW = 80, XCH = 96 * XROW;
constexpr int SSM_LDS = 98304, XOFF = SSM_LDS + 2 * HCH;
struct K3S { lds_cptr ha, xa; f32x16 y0, y1; s16x4 al[4][2], ah[4][2]; bf16x8 bx[4][2]; };
template <int e> DI void k3_ssm_rd(K3S& c) {
  constexpr int s = e & 3;
  c.al[s][0] = *(const volatile LAS s16x4*)(c.ha + e * 64); c.ah[s][0] = *(const volatile LAS s16x4*)(c.ha + e * 64 + 8);
  c.bx[s][0] = lds_rd128(c.xa + (63 - e) * XROW); c.bx[s][1] = lds_rd128(c.xa + (31 - e) * XROW);
  c.al[s][1] = *(const volatile LAS s16x4*)(c.ha + (e + 32) * 64); c.ah[s][1] = *(const volatile LAS s16x4*)(c.ha + (e + 32) * 64 + 8);
}
template <int e, class F> DI void k3_ssm_step(K3S& c, F&& f) {
  constexpr int s = e & 3;
  if constexpr (e + 2 < 32) k3_ssm_rd<e + 2>(c);
  SBAR();
  const bf16x8 a0 = cat(c.al[s][0], c.ah[s][0]), a1 = cat(c.al[s][1], c.ah[s][1]);
  c.y1 = MFMA32(a0, c.bx[s][0], c.y1); c.y0 = MFMA32(a0, c.bx[s][1], c.y0); c.y1 = MFMA32(a1, c.bx[s][1], c.y1);
  SBAR();
  f(std::integral_constant<int, e>{});
  SBAR();
}
DI void ssm_load(SsmS& s, const Params& P, unsigned blk, unsigned long long tag, const unsigned wid) {
  const unsigned lane = lane_id(), tid = wid * 64u + lane;
  s.c0 = 2u * (28u * (blk & 7u) + (blk >> 3));
  if (!__all((((unsigned long long)s.f0[1] << 32) | s.f0[0]) == tag))
    wait_flags((gu64*)(P.ws + WS_FL) + FL_S0 + (7u * (lane & 31u) + (s.c0 >> 6)) * 16u, tag);
  const srd_t sx = mk_srd(P.ws + WS_XT, SEQ * DM * 2);
#pragma unroll
  for (int ch = 0; ch < 2; ++ch) s.xw[ch] = __builtin_amdgcn_raw_buffer_load_b128(sx, (((tid >> 3) * DM + s.c0 + ch) * 64u + 8u * (tid & 7u)) * 2u, 0, 16);
  s.dw = *(const unsigned*)(P.ssmd + s.c0);
}
DI void ssm_build(SsmS& s, char* lds, const unsigned wid) {
  const unsigned tid = wid * 64u + lane_id();
#pragma unroll
  for (int ch = 0; ch < 2; ++ch) {
    const unsigned w[6] = {s.hA[ch][0], s.hA[ch][1], s.hA[ch][2], s.hA[ch][3], s.hB[ch][0], s.hB[ch][1]};
    unsigned o[5];
#pragma unroll
    for (int k = 0; k < 5; ++k) o[k] = __builtin_amdgcn_alignbyte(w[k + 1], w[k], 2);
    LAS char* dst = (LAS char*)lds + SSM_LDS + ch * HCH + tid * 16u + 64u;
    *(LAS u32x4*)(dst) = (u32x4){o[0], o[1], o[2], o[3]};
    *(LAS u32x4*)(dst + HCOPY) = (u32x4){w[1], w[2], w[3], w[4]};
    *(LAS u32x4*)(dst + 2 * HCOPY) = (u32x4){o[1], o[2], o[3], o[4]};
    *(LAS u32x4*)(dst + 3 * HCOPY) = (u32x4){w[2], w[3], w[4], w[5]};
    if (tid < 4u) {
      const unsigned z0 = tid ? 0u : w[0], z1 = tid ? 0u : w[1], zo = tid ? 0u : o[0];
      LAS char* d2 = (LAS char*)lds + SSM_LDS + ch * HCH + (3u - tid) * 16u;
      *(LAS u32x4*)(d2) = (u32x4){0u, 0u, 0u, z0 << 16};
      *(LAS u32x4*)(d2 + HCOPY) = (u32x4){0u, 0u, 0u, z0};
      *(LAS u32x4*)(d2 + 2 * HCOPY) = (u32x4){0u, 0u, z0 << 16, zo};
      *(LAS u32x4*)(d2 + 3 * HCOPY) = (u32x4){0u, 0u, z0, z1};
    }
    LAS char* xb = (LAS char*)lds + XOFF + ch * XCH;
    if (tid < 160u) *(LAS u32x4*)(xb + tid * 16u) = (u32x4){0u, 0u, 0u, 0u};
  }
#pragma unroll
  for (int ch = 0; ch < 2; ++ch) *(LAS u32x4*)((LAS char*)lds + XOFF + ch * XCH + (32u + (tid >> 2)) * XROW + (tid & 3u) * 16u) = s.xw[ch];
}
template <class F> DI void ssm_conv(SsmS& s, char* lds, const unsigned wid, F&& f) {
  const unsigned lane = lane_id(), hi = lane >> 5, r = lane & 31u;
  const unsigned ch = wid >> 1, dh = wid & 1u;
  K3S c;
  c.ha = (lds_cptr)lds + SSM_LDS + ch * HCH + (r & 3u) * HCOPY + (24u - 16u * dh - 8u * hi + 4u * (r >> 2)) * 2u; PINV(c.ha);
  c.xa = (lds_cptr)lds + XOFF + ch * XCH + (r + 1u) * XROW + (2u * dh + hi) * 16u; PINV(c.xa);
  c.y0 = f32x16{}; c.y1 = f32x16{};
  k3_ssm_rd<0>(c); k3_ssm_rd<1>(c);
  sfor<32>([&](auto E) AI { k3_ssm_step<E>(c, f); });
  s.y0 = c.y0; s.y1 = c.y1;
}
DI void ssm_finish(SsmS& s, const Params& P, char* lds, const unsigned wid) {
  const unsigned lane = lane_id(), tid = wid * 64u + lane, hi = lane >> 5, r = lane & 31u;
  LAS char* ys = (LAS char*)lds + SSM_LDS + wid * 8192u;
#pragma unroll
  for (int k = 0; k < 4; ++k) {
    const unsigned o = r * 128u + (((2u * k + hi) ^ (r & 7u)) << 4);
    *(LAS f32x4*)(ys + o) = (f32x4){s.y0[4 * k], s.y0[4 * k + 1], s.y0[4 * k + 2], s.y0[4 * k + 3]};
    *(LAS f32x4*)(ys + 4096u + o) = (f32x4){s.y1[4 * k], s.y1[4 * k + 1], s.y1[4 * k + 2], s.y1[4 * k + 3]};
  }
  asm volatile("s_waitcnt lgkmcnt(0)\n\ts_barrier" ::: "memory");
  const unsigned row = tid >> 2, yo0 = row * 128u + (((2u * (tid & 3u)) ^ (row & 7u)) << 4), yo1 = row * 128u + (((2u * (tid & 3u) + 1u) ^ (row & 7u)) << 4);
  const srd_t sz = mk_srd(P.ws + WS_ZT, SEQ * DM * 2);
  sfor<2>([&](auto C) AI {
    const LAS char* yp = (const LAS char*)lds + SSM_LDS + C * 16384;
    const f32x4 a = *(const LAS f32x4*)(yp + yo0) + *(const LAS f32x4*)(yp + 8192 + yo0), bb = *(const LAS f32x4*)(yp + yo1) + *(const LAS f32x4*)(yp + 8192 + yo1);
    const float dd = bf2f(C ? s.dw >> 16 : s.dw & 0xffffu);
    u32x4 z;
#pragma unroll
    for (int i = 0; i < 4; ++i) {
      const f32x2 kv = up2(s.xw[C][3 - i]), y = rb2(i < 2 ? (f32x2){a[2 * i], a[2 * i + 1]} : (f32x2){bb[2 * i - 4], bb[2 * i - 3]}), kd = rb2((f32x2){kv[1] * dd, kv[0] * dd});
      z[i] = cvtpk(y[0] + kd[0], y[1] + kd[1]);
    }
    st16w<C * 128>(z, sz, (((tid >> 3) * DM + s.c0) * 64u + 8u * (tid & 7u)) * 2u, 0u);
  });
}
DI void ssm_gate(const Params& P, unsigned blk, unsigned long long tag, const unsigned wid) {
  const unsigned lane = lane_id(), tid = wid * 64u + lane, bc0 = 64u * (blk % 7u) + 2u * (tid & 31u), t0 = 64u * (blk / 7u) + 8u * (tid >> 5);
  unsigned qw[8];
#pragma unroll
  for (int i = 0; i < 8; ++i) qw[i] = *(const unsigned*)(P.bq + (size_t)(t0 + i) * DM + bc0);
  { const unsigned pr = 32u * (blk % 7u) + (lane & 31u);
    wait_flags((gu64*)(P.ws + WS_FL) + FL_S1 + (pr / 28u + 8u * (pr % 28u)) * 16u, tag); }
  const srd_t sz = mk_srd(P.ws + WS_ZT, SEQ * DM * 2);
  const unsigned zo = (((blk / 7u) * DM + bc0) * 64u + 8u * (tid >> 5)) * 2u;
  const u32x4 z0 = __builtin_amdgcn_raw_buffer_load_b128(sz, zo, 0, 16), z1 = __builtin_amdgcn_raw_buffer_load_b128(sz, zo + 128u, 0, 16);
#pragma unroll
  for (int i = 0; i < 8; ++i) {
    const f32x2 q = up2(qw[i]), a = up2(z0[i >> 1]), bb = up2(z1[i >> 1]);
    *(unsigned*)(P.out + (size_t)(t0 + i) * OUTW + DM + bc0) = cvtpk(q[0] * a[i & 1], q[1] * bb[i & 1]);
  }
}

__global__ __launch_bounds__(256, 1) void k1_fused(Params P) {
  extern __shared__ __attribute__((aligned(16))) char lds[];
  const unsigned wid = rfl(threadIdx.x >> 6);
  const unsigned b = blockIdx.x, xcd = b & 7u, idx = b >> 3, h = 2u * xcd + (idx >> 4), j = idx & 15u;
  srd_t scode;
  { unsigned long long pc; unsigned len;
    asm volatile("s_getpc_b64 %0\n.Lcode_begin:\n\ts_mov_b32 %1, .Lcode_end-.Lcode_begin" : "=s"(pc), "=s"(len));
    scode = mk_srd((const void*)pc, len); }
  if (wid == 3) touch_lds((unsigned)(size_t)lds, scode, code_off());
  gu64* flags = (gu64*)(P.ws + WS_FL);
  const unsigned qb = (j & 1u) ? (j >> 1) : 15u - (j >> 1);
  const bool ssm = b < 224u;
  u32x4 qo[16]; SsmS s;
  const unsigned long long tag = phase_proj(P, lds, wid, qb, qo, s, scode);
  if (ssm) ssm_load(s, P, b, tag, wid);
  SBAR();
  {
    const unsigned lane = lane_id(), vl = lane * 16u;
    const unsigned q = (lane & 15u) >> 2, p = lane & 3u;
    const lds_cptr rp = (lds_cptr)lds + WO_LDS + wid * 4096u + (4u * (lane >> 5) + q) * 64u + 32u * ((lane >> 4) & 1u) + 16u * (p & 1u) + 8u * (p >> 1);
    bf16x8 wf[4];
    sfor<4>([&](auto J) AI { wf[J] = cat(lds_tr(rp + J * 1024), lds_tr(rp + J * 1024 + 512)); });
    const srd_t sw_ = mk_srd(P.ws + WS_W + (size_t)h * (DH * DM * 2), DH * DM * 2);
    const srd_t sq = mk_srd(P.ws + ((size_t)(h * 8u + (j >> 1)) * 4u + wid) * 16384u, (j & 1u) ? 0u : 16384u);
    auto deferred = [&](auto E) AI {
      constexpr int k = E >> 1;
      if constexpr (E & 1) st16w<k * 1024>(qo[k], sq, vl, 0u);
      else if constexpr (k < 12) { if constexpr (k < 4 * LPG) { constexpr int u = q_lunit(k), i = u >> 2, jj = u & 3; st16wa<(i & 1) * 1024>(s.late[k], s.sO, vl, s.so + (i >> 1) * s.sA + jj * s.sB); } }
      else st16w<(k - 12) * 65536u>(__builtin_bit_cast(u32x4, wf[k - 12]), sw_, vl, (wid * 16u + j) * 1024u);
    };
    if (ssm) {
      ssm_build(s, lds, wid);
      asm volatile("s_waitcnt lgkmcnt(0)\n\ts_barrier" ::: "memory");
      ssm_conv(s, lds, wid, deferred);
      asm volatile("s_barrier" ::: "memory");
      ssm_finish(s, P, lds, wid);
    } else sfor<32>(deferred);
  }
  publish(flags + FL_S1 + b * 16u, tag, wid);
  if (wid == 0) wait_flags(flags + FL_S1 + (xcd + 8u * ((idx & 16u) + (lane_id() & 15u))) * 16u, tag);
  asm volatile("s_barrier" ::: "memory");
  phase_attn(P, lds, tag, wid, qo, scode);
  publish(flags + FL_S3 + b * 16u, tag, wid);
  if (!(j & 1u) && j >= 2u) { const unsigned g0 = 2u * (7u * h + (j >> 1) - 1u); ssm_gate(P, g0, tag, wid); ssm_gate(P, g0 + 1u, tag, wid); }
  const unsigned lane = lane_id();
  if (wid == 0) wait_flags(flags + FL_S3 + ((lane & 7u) + 8u * (16u * ((lane >> 3) & 1u) + j)) * 16u, tag);
  asm volatile("s_barrier" ::: "memory");
  if (b == 0 && wid == 0 && lane == 0) __hip_atomic_store(flags + FL_EPOCH, tag, RLX_AGENT);
  const srd_t sP = mk_srd(P.ws + WS_PF, NH * 64u * 28672u);
  const unsigned task = qb * 112u + h * 7u + wid;
  u32x4 va[16], vb[16];
  reduce_load(va, task, sP, lane);
  if (wid < 3u) reduce_load(vb, task + 4u, sP, lane);
  SBAR();
  reduce_sum(va, task, lane, P.out);
  if (wid < 3u) reduce_sum(vb, task + 4u, lane, P.out);
  asm volatile(".Lcode_end:");
}

extern "C" void kernel_launch(void* const* d_in, const int* in_sizes, int n_in, void* d_out, int out_size, void* d_ws, size_t ws_size, hipStream_t stream) {
  typedef const unsigned short* cu16;
  Params P = {(cu16)d_in[0], (cu16)d_in[1], (cu16)d_in[2], (cu16)d_in[3], (cu16)d_in[4], (cu16)d_in[5], (cu16)d_in[6], (cu16)d_in[7], (cu16)d_in[8], (cu16)d_in[9], (cu16)d_in[10], (cu16)d_in[11],
              (unsigned short*)d_out, (char*)d_ws};
  (void)hipFuncSetAttribute((const void*)k1_fused, hipFuncAttributeMaxDynamicSharedMemorySize, 163840);
  k1_fused<<<256, 256, 163840, stream>>>(P);
}
```

```cpp
#include <hip/hip_runtime.h>
#include <hip/hip_bf16.h>
#include <utility>

typedef short    bf16x8 __attribute__((ext_vector_type(8)));
typedef short    s16x4  __attribute__((ext_vector_type(4)));
typedef float    f32x16 __attribute__((ext_vector_type(16)));
typedef float    f32x4  __attribute__((ext_vector_type(4)));
typedef float    f32x2  __attribute__((ext_vector_type(2)));
typedef __bf16   bf16x2 __attribute__((ext_vector_type(2)));
typedef unsigned u32x4  __attribute__((ext_vector_type(4)));
typedef unsigned u32x2  __attribute__((ext_vector_type(2)));
#define DI __device__ __forceinline__
#define AI __attribute__((always_inline))
#define LAS __attribute__((address_space(3)))
#define SBAR() __builtin_amdgcn_sched_barrier(0)
template <class T> DI void PINV(T& x) { asm volatile("" : "+v"(x)); }
template <class T> DI void PINA(T& x) { asm volatile("" : "+a"(x)); }
#define MFMA32(a, b, c) __builtin_amdgcn_mfma_f32_32x32x16_bf16((a), (b), (c), 0, 0, 0)
#define MFMA_DRAIN() asm volatile("s_nop 7\n\ts_nop 7" ::: "memory")
typedef LAS const char* lds_cptr;
typedef __amdgpu_buffer_rsrc_t srd_t;
template <int... Is> using iseq = std::integer_sequence<int, Is...>;
template <int N> using mkseq = std::make_integer_sequence<int, N>;

DI unsigned rfl(unsigned x) { return __builtin_amdgcn_readfirstlane(x); }
DI unsigned lane_id() { unsigned l; asm volatile("v_mbcnt_lo_u32_b32 %0, -1, 0\n\tv_mbcnt_hi_u32_b32 %0, -1, %0" : "=v"(l)); return l; }
DI unsigned cvtpk(float lo, float hi) { f32x2 v = {lo, hi}; bf16x2 b = __builtin_convertvector(v, bf16x2); return __builtin_bit_cast(unsigned, b); }
DI float bf2f(unsigned h) { return __uint_as_float(h << 16); }
DI float rbf(float x) { return __uint_as_float(cvtpk(x, x) << 16); }
DI bf16x8 lds_rd128(lds_cptr p) { return *(const LAS bf16x8*)p; }
DI s16x4 lds_tr(lds_cptr p) { return __builtin_amdgcn_ds_read_tr16_b64_v4i16((LAS s16x4*)p); }
DI bf16x8 cat(s16x4 l, s16x4 h) { return __builtin_shufflevector(l, h, 0, 1, 2, 3, 4, 5, 6, 7); }
DI srd_t mk_srd(const void* p, unsigned bytes) { return __builtin_amdgcn_make_buffer_rsrc((void*)p, (short)0, (int)bytes, 0x00020000); }
template <unsigned MI, unsigned SI> DI void dma16(unsigned mb, unsigned voff, srd_t srd, unsigned sb) {
  unsigned t;
  asm volatile("s_add_u32 m0, %1, %5\n\ts_add_u32 %0, %4, %6\n\tbuffer_load_dwordx4 %2, %3, %0 offen lds" : "=&s"(t) : "s"(mb), "v"(voff), "s"(srd), "s"(sb), "n"(MI), "n"(SI) : "memory", "scc");
}
template <unsigned SI> DI void st16w(u32x4 v, srd_t srd, unsigned voff, unsigned sb) {
  unsigned t; asm volatile("s_add_u32 %0, %4, %5\n\tbuffer_store_dwordx4 %1, %2, %3, %0 offen sc1\n\ts_nop 1" : "=&s"(t) : "v"(v), "v"(voff), "s"(srd), "s"(sb), "n"(SI) : "memory", "scc"); }
template <unsigned SI> DI void st16wa(f32x4 v, srd_t srd, unsigned voff, unsigned sb) {
  unsigned t; asm volatile("s_add_u32 %0, %4, %5\n\tbuffer_store_dwordx4 %1, %2, %3, %0 offen sc1\n\ts_nop 1" : "=&s"(t) : "a"(v), "v"(voff), "s"(srd), "s"(sb), "n"(SI) : "memory", "scc"); }
DI void ld4(unsigned& d, srd_t srd, unsigned voff) { asm volatile("buffer_load_dword %0, %1, %2, 0 offen" : "=v"(d) : "v"(voff), "s"(srd) : "memory"); }
template <unsigned SI> DI void ld16a(bf16x8& d, srd_t srd, unsigned voff) { unsigned t; asm volatile("s_mov_b32 %1, %4\n\tbuffer_load_dwordx4 %0, %2, %3, %1 offen sc1" : "=a"(d), "=&s"(t) : "v"(voff), "s"(srd), "n"(SI) : "memory"); }
template <int N, class F> DI void sfor_(F&& f, iseq<>) {}
template <int N, class F, int I, int... Is> DI void sfor_(F&& f, iseq<I, Is...>) { f(std::integral_constant<int, I>{}); sfor_<N>(f, iseq<Is...>{}); }
template <int N, class F> DI void sfor(F&& f) { sfor_<N>(f, mkseq<N>{}); }
DI u32x4 pack8(const f32x4& a, const f32x4& b, float sc) { u32x4 w = {cvtpk(a[0] * sc, a[1] * sc), cvtpk(a[2] * sc, a[3] * sc), cvtpk(b[0] * sc, b[1] * sc), cvtpk(b[2] * sc, b[3] * sc)}; return w; }
DI u32x4 pack8w(const f32x16& a, int s, float sc) {
  u32x4 w = {cvtpk(a[8 * s] * sc, a[8 * s + 1] * sc), cvtpk(a[8 * s + 2] * sc, a[8 * s + 3] * sc), cvtpk(a[8 * s + 4] * sc, a[8 * s + 5] * sc), cvtpk(a[8 * s + 6] * sc, a[8 * s + 7] * sc)};
  return w;
}
constexpr int SEQ = 2048, NH = 16, DH = 256, DM = 448, DC = 1024;
constexpr unsigned WRS = NH * DH * 2;
constexpr unsigned MB16 = SEQ * NH * DH * 2;
constexpr float QSCALE = 1.4426950408889634f / 16.0f;

DI unsigned code_off() { return ((blockIdx.x >> 3) * 64u + lane_id()) * 64u; }
typedef __attribute__((address_space(1))) unsigned long long gu64;
#define RLX_AGENT __ATOMIC_RELAXED, __HIP_MEMORY_SCOPE_AGENT
struct Params {
  const unsigned short *x, *ckv, *wq, *wuk, *wuv, *wo, *bq, *bk, *bv, *shk, *ssmk, *ssmd;
  unsigned short* out; char* ws;
};
struct Ctx { srd_t code; };
constexpr size_t WS_K = MB16, WS_W = 3 * (size_t)MB16, WS_XT = WS_W + (4u << 20), WS_ZT = WS_XT + (2u << 20), WS_FL = WS_ZT + (2u << 20), WS_PART = WS_FL + (1u << 20), WS_PF = WS_PART + (17u << 20);
constexpr int FL_EPOCH = 0, FL_S0 = 16, FL_S1 = FL_S0 + 256 * 16, FL_S2 = FL_S1 + 256 * 16, FL_S3 = FL_S2 + 128 * 16, FL_S4 = FL_S3 + 256 * 16;
DI void flag_store(gu64* flag, unsigned long long tag) {
  const u32x2 v = {(unsigned)tag, (unsigned)(tag >> 32)};
  asm volatile("s_nop 4\n\tbuffer_store_dwordx2 %0, %1, %2, 0 offen sc1" :: "v"(v), "v"(lane_id() * 16u), "s"(mk_srd((const void*)flag, 8u)) : "memory");
}
DI void publish(gu64* flag, unsigned long long tag, unsigned wid) {
  asm volatile("s_waitcnt vmcnt(0)\n\ts_barrier" ::: "memory");
  if (wid == 0) flag_store(flag, tag);
}
DI void wait_flags(gu64* lane_flag, unsigned long long tag) {
  for (int spin = 0; spin < (1 << 20); ++spin) {
    if (__all(__hip_atomic_load(lane_flag, RLX_AGENT) == tag)) break;
    __builtin_amdgcn_s_sleep(1);
  }
}

DI void mfma16a(f32x4& c, const bf16x8& a, const bf16x8& b) { asm volatile("v_mfma_f32_16x16x32_bf16 %0, %1, %2, %0" : "+a"(c) : "v"(a), "v"(b)); }
DI void mfma16v(f32x4& c, const bf16x8& a, const bf16x8& b) { asm volatile("v_mfma_f32_16x16x32_bf16 %0, %1, %2, %0" : "+v"(c) : "v"(a), "v"(b)); }
DI void mfma16z(f32x4& c, const bf16x8& a, const bf16x8& b) { asm volatile("v_mfma_f32_16x16x32_bf16 %0, %1, %2, 0" : "=&v"(c) : "v"(a), "v"(b)); }
DI f32x2 up2(unsigned w) { f32x2 r = {__uint_as_float(w << 16), __uint_as_float(w & 0xffff0000u)}; return r; }
DI f32x2 rb2(f32x2 v) { return up2(cvtpk(v[0], v[1])); }
constexpr unsigned qxs(int s) { return 16384u * s; }
constexpr unsigned qws(int s) { return s == 0 ? 65536u : s == 1 ? 131072u : 98304u; }
constexpr unsigned WO_LDS = 49152u;
struct SsmS { u32x4 xw[2], hA[2]; u32x2 hB[2], f0; unsigned dw, c0; f32x16 y0, y1; f32x4 late[12]; srd_t sO; unsigned so, sA, sB; };
struct Frags { bf16x8 x[8]; s16x4 wl[8], wh[8]; };
struct K1C {
  unsigned ldsb, lds8, lds4, wid, vlane, b, hb;
  unsigned vx_kv[2], vx_q[2], vw[4];
  srd_t sX, sW, sXq, sWq, sO;
  unsigned sx_kv, sw_kv, sx_q, sw_q, so;
  lds_cptr xr_kv[2], wr[8], xq[2], wq[8];
  const char* pf; unsigned pfstep;
  f32x4 acc[8][8]; Frags f0, f1; f32x4 ta, tb; u32x4 pk;
  f32x4 qa[16][2]; s16x4 ql[16], qh[16]; bf16x8 qx[2][2];
  unsigned kw[11], vv[8], kv[8]; u32x4 sk; u32x2 ep; f32x2 kf[11], sc[4], pa, pt;
  unsigned long long tag; SsmS* ss; srd_t scode;
};
DI void touch_lds(unsigned ldsb, srd_t srd, unsigned voff) {
  asm volatile("s_add_u32 m0, %0, 147456\n\ts_nop 0\n\tbuffer_load_dword %1, %2, 0 offen lds" :: "s"(ldsb), "v"(voff), "s"(srd) : "memory", "scc"); }
DI void k1_touch(const K1C& c) {
  asm volatile("s_add_u32 m0, %0, 147456\n\ts_nop 0\n\tglobal_load_lds_dword %1, off" :: "s"(c.ldsb), "v"(c.pf) : "memory", "scc"); }
template <int buf, int j> DI void k1_piece(const K1C& c, unsigned sw, unsigned sx) {
  constexpr int p = j >> 1;
  if constexpr (!(j & 1)) dma16<buf * 32768u + 65536u + p * 1024u, 2u * p * WRS>(c.lds8, c.vw[(p & 1) + 2 * (p >> 2)], c.sW, sw);
  else dma16<buf * 32768u + p * 1024u, 8u * p * (DC * 2)>(c.lds8, c.vx_kv[(p >> 1) & 1], c.sX, sx);
}
template <int S, int j> DI void k1_qpiece(const K1C& c) {
  if constexpr (j % 3 != 1) { constexpr int p = 2 * (j / 3) + (j % 3 == 2); dma16<qws(S % 3) + p * 1024u, (64u * S + 2u * p) * WRS>(c.lds8, c.vw[(p & 1) + 2 * (p >> 2)], c.sWq, c.sw_q); }
  else { constexpr int p = j / 3; dma16<qxs(S % 3) + p * 1024u, 8u * p * (DM * 2) + 128u * S>(c.lds4, c.vx_q[p & 1], c.sXq, c.sx_q); }
}
template <int buf> DI void k1_stage(const K1C& c, int st) { sfor<16>([&](auto J) AI { k1_piece<buf, J>(c, c.sw_kv + (unsigned)st * (64u * WRS), c.sx_kv + (unsigned)st * 128u); }); }
template <int buf, int ks, int i> DI void k1_read(const K1C& c, Frags& f) {
  constexpr int so = buf * 32768;
  if constexpr (i < 2 || i >= 10) { constexpr int k = i < 2 ? i : i - 8, nb = k >> 1, t = k & 1;
    const s16x4 v = lds_tr(c.wr[nb] + so + ks * 16384 + t * 2048); if constexpr (t) f.wh[nb] = v; else f.wl[nb] = v; }
  else { constexpr int mb = i - 2; f.x[mb] = lds_rd128(c.xr_kv[ks] + so + (mb >> 1) * 4096 + (mb & 1) * 1024); }
}
template <int F> DI void k1_qw(K1C& c) {
  constexpr int T = F >> 5, ks = (F >> 4) & 1, nb = F & 15, sl = T % 3, o = (sl == 2 ? 32768 : 0) + ks * 16384 + (nb >> 3) * 256;
  if constexpr ((F & 31) < 8 && sl != 0) { c.wq[nb] += sl == 1 ? 65536 : -65536; PINV(c.wq[nb]); }
  c.ql[F & 15] = lds_tr(c.wq[nb & 7] + o); c.qh[F & 15] = lds_tr(c.wq[nb & 7] + o + 2048);
}
template <int T, int ks> DI void k1_qx(K1C& c) { c.qx[ks][0] = lds_rd128(c.xq[ks] + qxs(T % 3)); c.qx[ks][1] = lds_rd128(c.xq[ks] + qxs(T % 3) + 2048); }
template <int m> DI void k1_pre(K1C& c, const Params& P) {
  if constexpr (m == 0) {
    asm volatile("" : "+v"(c.kw[0]), "+v"(c.kw[1]), "+v"(c.kw[2]), "+v"(c.kw[3]), "+v"(c.kw[4]), "+v"(c.kw[5]), "+v"(c.kw[6]), "+v"(c.kw[7]), "+v"(c.kw[8]), "+v"(c.kw[9]), "+v"(c.kw[10]),
                 "+v"(c.vv[0]), "+v"(c.vv[1]), "+v"(c.vv[2]), "+v"(c.vv[3]), "+v"(c.vv[4]), "+v"(c.vv[5]), "+v"(c.vv[6]), "+v"(c.vv[7]), "+v"(c.sk), "+v"(c.ep));
    const unsigned long long e0 = ((unsigned long long)rfl(c.ep[1]) << 32) | rfl(c.ep[0]);
    c.tag = (e0 ^ 0x9E3779B97F4A7C15ull) * 0xD1342543DE82EF95ull + 1ull;
  } else if constexpr (m < 12) { c.kf[m - 1] = up2(c.kw[m - 1]); PINV(c.kf[m - 1]); }
  else if constexpr (m < 14) { constexpr int i = m - 12;
    c.sc[2 * i] = (f32x2){bf2f(c.sk[i] & 0xffffu), bf2f(c.sk[2 + i] & 0xffffu)}; c.sc[2 * i + 1] = (f32x2){bf2f(c.sk[i] >> 16), bf2f(c.sk[2 + i] >> 16)}; PINV(c.sc[2 * i]); PINV(c.sc[2 * i + 1]); }
  else if constexpr (m < 78) { constexpr int i = (m - 14) >> 3, s = (m - 14) & 7;
    if constexpr (s == 0) { c.pa = rb2(c.kf[i] * c.sc[3]); PINV(c.pa); }
    else if constexpr (s == 7) { const f32x2 a = c.pa * up2(c.vv[i]); c.kv[i] = cvtpk(a[0], a[1]); PINV(c.kv[i]); }
    else if constexpr (s & 1) { c.pt = rb2(c.kf[i + (s + 1) / 2] * c.sc[3 - (s + 1) / 2]); PINV(c.pt); }
    else { c.pa = rb2(c.pa + c.pt); PINV(c.pa); }
  } else {
    constexpr unsigned sel = m == 78 ? 0x05040100u : 0x07060302u;
    const unsigned tid = c.wid * 64u + (c.vlane >> 4), xo = (((c.b / 7u) * DM + 64u * (c.b % 7u) + 2u * (tid & 31u)) * 64u + 8u * (tid >> 5)) * 2u;
    st16w<(m - 78) * 128>((u32x4){__builtin_amdgcn_perm(c.kv[6], c.kv[7], sel), __builtin_amdgcn_perm(c.kv[4], c.kv[5], sel), __builtin_amdgcn_perm(c.kv[2], c.kv[3], sel), __builtin_amdgcn_perm(c.kv[0], c.kv[1], sel)},
                          mk_srd(P.ws + WS_XT, SEQ * DM * 2), xo, 0u);
  }
}
template <int i> DI void k1_preload(K1C& c, const Params& P) {
  const unsigned tid = c.wid * 64u + (c.vlane >> 4), bc0 = 64u * (c.b % 7u) + 2u * (tid & 31u), vo = ((64u * (c.b / 7u) + 8u * (tid >> 5)) * DM + bc0) * 2u;
  if constexpr (i < 11) ld4(c.kw[i], mk_srd(P.bk, SEQ * DM * 2), vo + (unsigned)((i - 3) * DM * 2));
  else if constexpr (i < 19) ld4(c.vv[i - 11], mk_srd(P.bv, SEQ * DM * 2), vo + (unsigned)((i - 11) * DM * 2));
  else if constexpr (i == 19) asm volatile("buffer_load_dwordx4 %0, %1, %2, 0 offen" : "=v"(c.sk) : "v"(bc0 * 8u), "s"(mk_srd(P.shk, DM * 8)) : "memory");
  else asm volatile("buffer_load_dwordx2 %0, off, %1, 0 sc1" : "=v"(c.ep) : "s"(mk_srd(P.ws + WS_FL, 64)) : "memory");
}
template <bool WA, int STEP, int buf, int G> DI void k1_gap(K1C& c, const Params& P, unsigned sw, unsigned sx) {
  constexpr int ks = G >> 6, g = G & 63, nb = g >> 3, mb = g & 7;
  Frags& fc = ks ? c.f1 : c.f0; Frags& fn = ks ? c.f0 : c.f1;
  if constexpr (G == 64) {
    asm volatile("s_waitcnt vmcnt(%0) lgkmcnt(0)\n\ts_barrier" :: "n"(STEP == 0 ? 3 : STEP == 14 ? 0 : STEP == 15 ? 12 : 1) : "memory");
    SBAR();
  }
  const bf16x8 w = cat(fc.wl[nb], fc.wh[nb]);
  if constexpr (G == 0) asm volatile("s_nop 1" :: "v"(w), "v"(fc.x[0]));
  if constexpr (WA) mfma16a(c.acc[mb][nb], w, fc.x[mb]); else mfma16a(c.acc[nb][mb], fc.x[mb], w);
  SBAR();
  if constexpr (!(g & 1) && g < 48) {
    if constexpr (ks == 0) k1_read<buf, 1, g / 2>(c, fn);
    else if constexpr (STEP != 15) k1_read<buf ^ 1, 0, g / 2>(c, fn);
  }
  if constexpr (STEP == 15 && ks == 1 && g >= 36 && !(g & 3)) { if constexpr (g == 36) k1_qx<0, 0>(c); else k1_qw<(g - 40) / 4>(c); }
  if constexpr (STEP == 0 && ks == 0 && (g & 1) && g < 16) k1_piece<1, 8 + (g >> 1)>(c, c.sw_kv + 64u * WRS, c.sx_kv + 128u);
  if constexpr (STEP == 0 && (G == 17 || G == 19 || G == 21)) { k1_touch(c); c.pf += c.pfstep; }
  if constexpr (STEP == 13 && G == 64) { if (c.wid == 3) touch_lds(c.ldsb, c.scode, code_off()); }
  if constexpr (ks == 1) {
    if constexpr (STEP <= 13 && (g & 3) == 1) k1_piece<buf, (g >> 2)>(c, sw, sx);
    if constexpr (STEP == 0 && (g & 3) == 3) k1_preload<(g >> 2)>(c, P);
    if constexpr (STEP == 0 && (g & 3) == 2 && g >= 44) k1_preload<16 + ((g - 44) >> 2)>(c, P);
    if constexpr (STEP == 14 && (g & 1) && g < 60 && (g >> 1) % 5 != 4) { constexpr int j = (g >> 1) - (g >> 1) / 5; k1_qpiece<j / 12, j % 12>(c); }
    if constexpr (STEP == 15 && (g & 3) == 1 && g < 48) k1_qpiece<2, (g >> 2)>(c);
  }
  if constexpr (STEP <= 13 && G == 127) { k1_touch(c); c.pf += c.pfstep; }
  if constexpr (STEP >= 13) { constexpr int X = (STEP - 13) * 128 + G; if constexpr (X >= 56 && X % 4 == 3 && (X - 56) / 4 < 80) k1_pre<(X - 56) / 4>(c, P); }
  SBAR();
}
template <bool WA, int STEP, int buf> DI void k1_step(K1C& c, const Params& P, int sb) {
  const unsigned sw = c.sw_kv + (unsigned)sb * (64u * WRS), sx = c.sx_kv + (unsigned)sb * 128u;
  sfor<128>([&](auto G) AI { k1_gap<WA, STEP, buf, G>(c, P, sw, sx); });
}
#define LPG 3
constexpr int q_lpos(int k) { return k == 0 ? 7 : k == 1 ? 3 : 5; }
constexpr bool q_late(int u) { for (int k = 0; k < LPG; ++k) if (u % 8 == q_lpos(k)) return true; return false; }
constexpr int q_lidx(int u) { for (int k = 0; k < LPG; ++k) if (u % 8 == q_lpos(k)) return LPG * (u / 8) + k; return 0; }
constexpr int q_lunit(int k) { return 8 * (k / (LPG ? LPG : 1)) + q_lpos(k % (LPG ? LPG : 1)); }
constexpr int q_stores(int lo, int hi) { int n = 0; for (int u = 0; u < 32; ++u) n += (!q_late(u) && 6 * u + 5 > lo && 6 * u + 5 < hi); return n; }
constexpr int q_wait(int T) {
  return T == 0 ? 14 + q_stores(-1, 26) : T == 1 ? 12 + q_stores(-1, 58) : (T + 2 <= 6 ? 12 : 0) + q_stores(32 * (T - 2) + 48, 32 * T + 26); }
template <bool WA, int X> DI void k1_qgap(K1C& c, const Params& P) {
  constexpr int T = X >> 5, g = X & 31, ks = g >> 4, nb = g & 15;
  if constexpr (g == 26 && T < 6) {
    asm volatile("s_waitcnt vmcnt(%0) lgkmcnt(0)\n\ts_barrier" :: "n"(q_wait(T)) : "memory"); SBAR();
    if constexpr (T == 1) { if (c.wid == 0) flag_store((gu64*)(P.ws + WS_FL) + FL_S0 + c.b * 16u, c.tag); SBAR(); }
  }
  const bf16x8 w = cat(c.ql[X & 15], c.qh[X & 15]);
  if constexpr (X < 16) mfma16z(c.qa[nb][0], w, c.qx[ks][0]); else mfma16v(c.qa[nb][0], w, c.qx[ks][0]);
  SBAR();
  if constexpr (g < 22) { constexpr int f = 6 + g + (g + 4) / 5 - (g > 20 ? 1 : 0); k1_qw<32 * T + f>(c); if constexpr (g % 5 == 0 && g < 20) k1_qw<32 * T + f + 1>(c); }
  if constexpr (g >= 26 && T < 6) k1_qw<32 * (T + 1) + g - 26>(c);
  SBAR();
  if constexpr (X < 16) mfma16z(c.qa[nb][1], w, c.qx[ks][1]); else mfma16v(c.qa[nb][1], w, c.qx[ks][1]);
  SBAR();
  if constexpr (g == 7) k1_qx<T, 1>(c);
  if constexpr (g == 27 && T < 6) k1_qx<T + 1, 0>(c);
  if constexpr (X >= 26 && !(X & 1) && (X - 26) % 32 < 24 && (X - 26) / 32 + 3 <= 6) k1_qpiece<(X - 26) / 32 + 3, ((X - 26) % 32) / 2>(c);
  constexpr int u = X / 6, ph = X % 6;
  if constexpr (u < 32) {
    constexpr int i = u >> 2, j = u & 3;
    if constexpr (ph == 1) { c.ta = c.acc[i][2 * j]; PINV(c.ta); }
    if constexpr (ph == 2) { c.tb = c.acc[i][2 * j + 1]; PINV(c.tb); }
    if constexpr (ph == 3) { c.pk[0] = cvtpk(c.ta[0], c.ta[1]); c.pk[1] = cvtpk(c.ta[2], c.ta[3]); PINV(c.pk); }
    if constexpr (ph == 4) { c.pk[2] = cvtpk(c.tb[0], c.tb[1]); c.pk[3] = cvtpk(c.tb[2], c.tb[3]); PINV(c.pk); }
    if constexpr (ph == 5 && q_late(u)) { c.ss->late[q_lidx(u)] = __builtin_bit_cast(f32x4, c.pk); PINA(c.ss->late[q_lidx(u)]); }
    if constexpr (ph == 5 && !q_late(u)) st16w<(WA ? (i >> 1) * 16384u + j * 2048u : (i >> 1) * 2048u + j * 16384u) + (i & 1) * 1024u>(c.pk, c.sO, c.vlane, c.so);
  }
  if constexpr (X >= 200 && X < 205) {
    const unsigned tid = c.wid * 64u + (c.vlane >> 4), c0 = 2u * (28u * (c.b & 7u) + (c.b >> 3)); constexpr int ch = (X - 200) >> 1;
    if constexpr (X == 204) asm volatile("buffer_load_dwordx2 %0, %1, %2, 0 offen sc1" : "=v"(c.ss->f0) : "v"((FL_S0 + (7u * (tid & 31u) + (c0 >> 6)) * 16u) * 8u), "s"(mk_srd(P.ws + WS_FL, 1u << 20)) : "memory");
    else if constexpr (X & 1) asm volatile("buffer_load_dwordx2 %0, %1, %2, 0 offen offset:16" : "=v"(c.ss->hB[ch]) : "v"(tid * 16u), "s"(mk_srd(P.ssmk + (size_t)(c0 + ch) * SEQ, c.b < 224u ? SEQ * 2 : 0)) : "memory");
    else asm volatile("buffer_load_dwordx4 %0, %1, %2, 0 offen" : "=v"(c.ss->hA[ch]) : "v"(tid * 16u), "s"(mk_srd(P.ssmk + (size_t)(c0 + ch) * SEQ, c.b < 224u ? SEQ * 2 : 0)) : "memory");
  }
  if constexpr (X >= 196 && X < 200) {
    constexpr int j = X - 196; const unsigned ln = c.vlane >> 4;
    dma16<WO_LDS + j * 1024u, (j >> 1) * 256u + (j & 1) * (16u * DM * 2)>(c.lds4, (ln >> 2) * (DM * 2) + (ln & 3u) * 16u, mk_srd(P.wo, NH * DH * DM * 2), (c.hb >> 1) * (32u * DM * 2) + (7u * (c.hb & 1u) + c.wid) * 64u);
  }
  SBAR();
}

template <bool WA> DI void k1_main(K1C& c, const Params& P, const char* pf2, u32x4 (&qo)[16]) {
  sfor<64>([&](auto I) AI { c.acc[I >> 3][I & 7] = f32x4{}; PINA(c.acc[I >> 3][I & 7]); });
  SBAR();
  sfor<8>([&](auto J) AI { k1_piece<1, J>(c, c.sw_kv + 64u * WRS, c.sx_kv + 128u); });
  asm volatile("s_waitcnt vmcnt(8)\n\ts_barrier" ::: "memory");
  sfor<24>([&](auto I) AI { k1_read<0, 0, I>(c, c.f0); });
  SBAR();
  k1_step<WA, 0, 0>(c, P, 2);
#pragma nounroll
  for (int t = 1; t < 13; t += 2) {
    if (t >= 3 && t <= 7) {
      const unsigned l = c.vlane >> 4, w = (c.b * 4u + c.wid) * 32u + (l & 31u);
      const unsigned short* p0 = t == 3 ? P.wq : t == 5 ? P.x : P.ssmk; const unsigned short* p1 = t == 3 ? P.wo : t == 5 ? P.bq : P.ssmk;
      const unsigned n = t == 3 ? DM * WRS : SEQ * DM * 2, o = w * 128u < n ? w * 128u : (w * 128u) & 0xfffffu;
      const char* a = (const char*)(l < 32u ? p0 : p1) + o;
      asm volatile("s_add_u32 m0, %0, 147456\n\ts_nop 0\n\tglobal_load_lds_dword %1, off" :: "s"(c.ldsb), "v"(a) : "memory", "scc");
    }
    k1_step<WA, 1, 1>(c, P, t + 2);
    c.pf = t == 9 ? pf2 : c.pf;
    k1_step<WA, 1, 0>(c, P, t + 3);
  }
  k1_step<WA, 13, 1>(c, P, 15);
  {
    const unsigned lane = lane_id(), rho = lane & 15u, g = lane >> 4, q = rho >> 2, p = lane & 3u, f = q | ((g & 1u) << 2);
    sfor<2>([&](auto I) AI { c.vx_q[I] = (lane >> 3) * (DM * 2) + 16u * ((lane & 7u) ^ (4u * I + (lane >> 4)));
      c.xq[I] = (lds_cptr)(size_t)(c.ldsb + (32u * c.wid + rho) * 128u + 16u * ((4u * I + g) ^ (rho >> 1))); PINV(c.xq[I]); });
    sfor<8>([&](auto I) AI { c.wq[I] = (lds_cptr)(size_t)(c.ldsb + 65536u + (8u * g + q) * 512u + ((unsigned)I ^ f) * 32u + 8u * p); PINV(c.wq[I]); });
  }
  k1_step<WA, 14, 0>(c, P, 0);
  k1_step<WA, 15, 1>(c, P, 0);
  sfor<224>([&](auto X) AI { k1_qgap<WA, X>(c, P); });
  MFMA_DRAIN(); SBAR();
  sfor<16>([&](auto I) AI { constexpr int db = I >> 1, q = I & 1; PINV(c.qa[2 * db][q]); PINV(c.qa[2 * db + 1][q]); qo[I] = pack8(c.qa[2 * db][q], c.qa[2 * db + 1][q], QSCALE); PINV(qo[I]); SBAR(); });
  asm volatile("s_waitcnt vmcnt(0)" : "+v"(c.ss->hA[0]), "+v"(c.ss->hA[1]), "+v"(c.ss->hB[0]), "+v"(c.ss->hB[1]), "+v"(c.ss->f0) :: "memory");
}

DI unsigned long long phase_proj(const Params& P, char* lds, const unsigned wid, const unsigned qblk, u32x4 (&qo)[16], SsmS& ss, srd_t scode) {
  const unsigned lane = lane_id(), wr = wid >> 1, wc = wid & 1;
  const unsigned b = blockIdx.x, xcd = b & 7u, idx = b >> 3;
  const unsigned pm = idx & 7u, which = idx >> 3, h = 2u * xcd + (which >> 1), isV = which & 1u;
  K1C c; c.ss = &ss; c.scode = scode;
  c.ldsb = (unsigned)(size_t)lds; c.lds8 = c.ldsb + wid * 8192u; c.lds4 = c.ldsb + wid * 4096u; c.wid = wid; c.vlane = lane * 16u; c.b = b; c.hb = 16u * h + (idx & 15u);
  const unsigned short* wkv = isV ? P.wuv : P.wuk;
  const bool pw = lane >= 32u;
  unsigned pfa = (192u + 8u * pm + 2u * wid + ((lane >> 2) & 1u)) * WRS + h * (DH * 2) + (lane & 3u) * 128u, pfb = (256u * pm + 64u * which + 16u * wid + (lane & 15u)) * (DC * 2) + 384u;
  PINV(pfa); PINV(pfb);
  c.pf = (pw ? (const char*)wkv : (const char*)P.ckv) + (pw ? pfa : pfb);
  c.pfstep = pw ? 64u * WRS : 128u;
  c.sX = mk_srd(P.ckv, SEQ * DC * 2); c.sW = mk_srd(wkv, DC * WRS); c.sXq = mk_srd(P.x, SEQ * DM * 2); c.sWq = mk_srd(P.wq, DM * WRS);
  sfor<2>([&](auto I) AI { c.vx_kv[I] = (lane >> 3) * (DC * 2) + 16u * ((lane & 7u) ^ (4u * I + (lane >> 4))); });
  sfor<4>([&](auto I) AI { const unsigned f = (2u * (I & 1) + (lane >> 5)) | (4u * (I >> 1)); c.vw[I] = (lane >> 5) * WRS + 16u * ((lane & 31u) ^ (f << 1)); });
  c.sx_kv = (256u * pm + 64u * wid) * (DC * 2); c.sw_kv = 16u * wid * WRS + h * (DH * 2);
  c.sx_q = (128u * qblk + 32u * wid) * (DM * 2);  c.sw_q = c.sw_kv;
  k1_stage<0>(c, 0);
    const unsigned rho = lane & 15u, g = lane >> 4;
  sfor<2>([&](auto I) AI { c.xr_kv[I] = (lds_cptr)lds + (wr * 128u + 16u * (rho >> 3) + (rho & 7u)) * 128u + 16u * ((4u * I + g) ^ (rho >> 1)); PINV(c.xr_kv[I]); });
  { const unsigned q = (lane & 15u) >> 2, p = lane & 3u, f = q | ((g & 1u) << 2);
    sfor<8>([&](auto I) AI { c.wr[I] = (lds_cptr)lds + 65536u + (8u * g + q) * 512u + (8u * wc + ((unsigned)I ^ f)) * 32u + 8u * p; PINV(c.wr[I]); }); }
  c.sO = mk_srd(P.ws + WS_K + (isV ? MB16 : 0u) + (size_t)h * (SEQ * DH * 2), SEQ * DH * 2);
  c.so = (8u * pm + 4u * wr) * 16384u + wc * 8192u;
  pfa = (192u + 4u * (idx & 15u) + wid) * WRS + h * (DH * 2) + (lane & 3u) * 128u; pfb = (128u * qblk + 64u * (idx >> 4) + 16u * wid + (lane & 15u)) * (DM * 2) + 384u;
  PINV(pfa); PINV(pfb);
  const char* pf2 = (pw ? (const char*)P.wq : (const char*)P.x) + (pw ? pfa : pfb);
  ss.sO = c.sO; ss.so = c.so; ss.sA = isV ? 2048u : 16384u; ss.sB = isV ? 16384u : 2048u;
  if (isV) k1_main<false>(c, P, pf2, qo); else k1_main<true>(c, P, pf2, qo);
  return c.tag;
}

constexpr float THR = 8.0f;
constexpr int AQ = 128, AQ2 = 192;
template <int R> DI void pv_mfma(const bf16x8& a, const u32x4& b) { asm volatile("v_mfma_f32_16x16x32_bf16 a[%c2:%c3], %0, %1, a[%c2:%c3]" :: "v"(a), "v"(b), "n"(R), "n"(R + 3)); }
template <int R> DI void pv_mfmaz(const bf16x8& a, const u32x4& b) { asm volatile("v_mfma_f32_16x16x32_bf16 a[%c2:%c3], %0, %1, 0" :: "v"(a), "v"(b), "n"(R), "n"(R + 3)); }
template <int R> DI void qk_mfma(f32x4& c, const bf16x8& a) { asm volatile("v_mfma_f32_16x16x32_bf16 %0, %1, a[%c2:%c3], %0" : "+v"(c) : "v"(a), "n"(R), "n"(R + 3)); }
template <int R> DI void qk_init(f32x4& d, const bf16x8& a, const f32x4& c) { asm volatile("v_mfma_f32_16x16x32_bf16 %0, %1, a[%c3:%c4], %2" : "=&v"(d) : "v"(a), "v"(c), "n"(R), "n"(R + 3)); }
DI void mfma_vv(f32x4& c, const bf16x8& a, const u32x4& b) { asm volatile("v_mfma_f32_16x16x32_bf16 %0, %1, %2, %0" : "+v"(c) : "v"(a), "v"(b)); }
DI void mfma_vz(f32x4& d, const bf16x8& a, const u32x4& b) { asm volatile("v_mfma_f32_16x16x32_bf16 %0, %1, %2, 0" : "=&v"(d) : "v"(a), "v"(b)); }
DI void use4(const bf16x8& w, const bf16x8& x, const bf16x8& y, const bf16x8& z) { asm volatile("" :: "v"(w), "v"(x), "v"(y), "v"(z)); }
template <int R> DI void acc_zero4() { asm volatile("v_accvgpr_write_b32 a[%c0], 0\n\tv_accvgpr_write_b32 a[%c1], 0\n\tv_accvgpr_write_b32 a[%c2], 0\n\tv_accvgpr_write_b32 a[%c3], 0" :: "n"(R), "n"(R + 1), "n"(R + 2), "n"(R + 3)); }
template <int R> DI void acc_write4(const u32x4& w) { asm volatile("v_accvgpr_write_b32 a[%c4], %0\n\tv_accvgpr_write_b32 a[%c5], %1\n\tv_accvgpr_write_b32 a[%c6], %2\n\tv_accvgpr_write_b32 a[%c7], %3" :: "v"(w[0]), "v"(w[1]), "v"(w[2]), "v"(w[3]), "n"(R), "n"(R + 1), "n"(R + 2), "n"(R + 3)); }
template <int R, int S> DI void acc_mov4() { asm volatile("v_accvgpr_mov_b32 a[%c0], a[%c4]\n\tv_accvgpr_mov_b32 a[%c1], a[%c5]\n\tv_accvgpr_mov_b32 a[%c2], a[%c6]\n\tv_accvgpr_mov_b32 a[%c3], a[%c7]" :: "n"(R), "n"(R + 1), "n"(R + 2), "n"(R + 3), "n"(S), "n"(S + 1), "n"(S + 2), "n"(S + 3)); }
template <int R> DI f32x4 acc_read4() { float a, b, c, d; asm volatile("v_accvgpr_read_b32 %0, a[%c4]\n\tv_accvgpr_read_b32 %1, a[%c5]\n\tv_accvgpr_read_b32 %2, a[%c6]\n\tv_accvgpr_read_b32 %3, a[%c7]" : "=v"(a), "=v"(b), "=v"(c), "=v"(d) : "n"(R), "n"(R + 1), "n"(R + 2), "n"(R + 3)); return (f32x4){a, b, c, d}; }
template <int R> DI void acc_scale2(float f) { float t, u; asm volatile("v_accvgpr_read_b32 %0, a[%c3]\n\tv_accvgpr_read_b32 %1, a[%c4]\n\tv_mul_f32 %0, %2, %0\n\tv_mul_f32 %1, %2, %1\n\tv_accvgpr_write_b32 a[%c3], %0\n\tv_accvgpr_write_b32 a[%c4], %1" : "=&v"(t), "=&v"(u) : "v"(f), "n"(R), "n"(R + 1)); }
template <int R, unsigned SI> DI void acc_store4(srd_t srd, unsigned voff) { unsigned t; asm volatile("s_mov_b32 %0, %3\n\tbuffer_store_dwordx4 a[%c4:%c5], %1, %2, %0 offen sc1" : "=&s"(t) : "v"(voff), "s"(srd), "n"(SI), "n"(R), "n"(R + 3) : "memory"); }
template <int R, unsigned SI> DI void acc_load4(srd_t srd, unsigned voff) { unsigned t; asm volatile("s_mov_b32 %0, %3\n\tbuffer_load_dwordx4 a[%c4:%c5], %1, %2, %0 offen sc1" : "=&s"(t) : "v"(voff), "s"(srd), "n"(SI), "n"(R), "n"(R + 3) : "memory"); }
#define A8(n) "a" #n "0", "a" #n "1", "a" #n "2", "a" #n "3", "a" #n "4", "a" #n "5", "a" #n "6", "a" #n "7", "a" #n "8", "a" #n "9"
#define ACC_CLAIM() asm volatile("; acc claim" ::: "a0", "a1", "a2", "a3", "a4", "a5", "a6", "a7", "a8", "a9", A8(1), A8(2), A8(3), A8(4), A8(5), A8(6), A8(7), A8(8), A8(9), A8(10), A8(11), A8(12), A8(13), A8(14), \
  A8(15), A8(16), A8(17), A8(18), A8(19), A8(20), A8(21), A8(22), A8(23), A8(24), "a250", "a251", "a252", "a253", "a254", "a255")
DI float max2f(float a, float b) { float r; asm("v_max_f32_e32 %0, %1, %2" : "=v"(r) : "v"(a), "v"(b)); return r; }
DI void max4(float& m, const f32x4& v) { asm volatile("v_max3_f32 %0, %0, %1, %2\n\tv_max3_f32 %0, %0, %3, %4" : "+v"(m) : "v"(v[0]), "v"(v[1]), "v"(v[2]), "v"(v[3])); }
DI float max4i(const f32x4& v) { float m; asm volatile("v_max_f32_e32 %0, %1, %2\n\tv_max3_f32 %0, %0, %3, %4" : "=&v"(m) : "v"(v[0]), "v"(v[1]), "v"(v[2]), "v"(v[3])); return m; }
DI float row_max(float x) {
  asm volatile("s_nop 1" : "+v"(x));
  auto a = __builtin_amdgcn_permlane32_swap(__float_as_uint(x), __float_as_uint(x), false, false); x = max2f(__uint_as_float(a[0]), __uint_as_float(a[1]));
  asm volatile("s_nop 1" : "+v"(x));
  auto b = __builtin_amdgcn_permlane16_swap(__float_as_uint(x), __float_as_uint(x), false, false); return max2f(__uint_as_float(b[0]), __uint_as_float(b[1]));
}
struct K2C {
  unsigned ldsb, vlane;
  srd_t sK;
  lds_cptr kr, vr;
  unsigned kslot_w, vslot_w, vslot_r;
  unsigned ksoff, vsoff, dt;
  f32x4 s[2][4], ci[2][4]; u32x4 p[4]; bf16x8 fr[16];
  f32x4 L[2]; bf16x8 ones;
  float m[2], fct[2], mS[2], mx, thr; bool pend, first;
  unsigned spec, t0off, wbase; int pos, kn1, kend; lds_cptr vbase;
  f32x4 ml, ob[32];
  srd_t sP;
  f32x4 tri[4];
};
template <int J> DI void k2_dma(unsigned& t, unsigned m0v, unsigned voff, srd_t srd, unsigned soff) {
  if constexpr (J == 0) asm volatile("s_mov_b32 m0, %1\n\ts_mov_b32 %0, %4\n\tbuffer_load_dwordx4 %2, %3, %0 offen sc1 lds" : "=&s"(t) : "s"(m0v), "v"(voff), "s"(srd), "s"(soff) : "memory");
  else if constexpr (J == 4) asm volatile("s_add_u32 m0, m0, 4096\n\ts_add_u32 %0, %0, 4096\n\tbuffer_load_dwordx4 %1, %2, %0 offen sc1 lds" : "+s"(t) : "v"(voff), "s"(srd) : "memory", "scc");
  else asm volatile("buffer_load_dwordx4 %0, %1, %2 offen offset:%3 sc1 lds" :: "v"(voff), "s"(srd), "s"(t), "n"((J & 3) * 1024) : "memory");
}
DI void k2_dma8(unsigned m0v, unsigned voff, srd_t srd, unsigned soff) { unsigned t; sfor<8>([&](auto J) AI { k2_dma<J>(t, m0v, voff, srd, soff); }); }
DI void k2_cinit(K2C& c, float r0, float r1, int rel) {
  sfor<2>([&](auto KB) AI {
    const int rb = rel + 32 * KB;
    const float cap = rb > 0 ? -__builtin_inff() : __builtin_inff(), lift = rb < 0 ? __builtin_inff() : -__builtin_inff();
    const float a0 = __builtin_fminf(r0, cap), a1 = __builtin_fminf(r1, cap), b0 = __builtin_fminf(a0, lift), b1 = __builtin_fminf(a1, lift);
    sfor<16>([&](auto E) AI { constexpr int t = E >> 2, i = E & 3; c.ci[KB][t][i] = __builtin_amdgcn_fmed3f(c.tri[t][i], t >> 1 ? a1 : a0, t >> 1 ? b1 : b0); });
    sfor<4>([&](auto T) AI { PINV(c.ci[KB][T]); });
  });
}
DI void s_seleq(unsigned& x, int a, int b, unsigned y) { asm volatile("s_cmp_eq_u32 %1, %2\n\ts_cselect_b32 %0, %3, %0" : "+s"(x) : "s"(a), "s"(b), "s"(y) : "scc"); }
DI void s_rot3(unsigned& x) { asm volatile("s_add_u32 %0, %0, 0x8000\n\ts_cmp_lg_u32 %0, 0x18000\n\ts_cselect_b32 %0, %0, 0" : "+s"(x) :: "scc"); }
template <int kb, int e> DI void k2_exp(K2C& c) { c.s[kb][e >> 2][e & 3] = __builtin_amdgcn_exp2f(c.s[kb][e >> 2][e & 3]); PINV(c.s[kb][e >> 2]); }
template <int kb, int w> DI void k2_pack(K2C& c) { c.p[2 * kb + (w >> 2)][w & 3] = cvtpk(c.s[kb][w >> 1][2 * (w & 1)], c.s[kb][w >> 1][2 * (w & 1) + 1]); PINV(c.p[2 * kb + (w >> 2)]); }
template <bool FIN, bool QK, bool FL, int g> DI void k2_gapA(K2C& c) {
  constexpr int kb = g >> 4, db = (g >> 1) & 7, m = g & 1;
  if constexpr (QK) {
    if constexpr ((g & 3) == 0) use4(c.fr[g & 15], c.fr[(g + 1) & 15], c.fr[(g + 2) & 15], c.fr[(g + 3) & 15]);
    if constexpr (db == 0) qk_init<AQ>(c.s[kb][m], c.fr[g & 15], c.ci[kb][m]); else qk_mfma<AQ + 8 * db>(c.s[kb][m], c.fr[g & 15]);
    SBAR();
    if constexpr (g < 24) c.fr[(g + 8) & 15] = lds_rd128(c.kr + (g + 8) * 1024);
  }
  if constexpr (FIN && (QK ? g >= 24 : g < 8)) c.fr[g & 7] = lds_rd128(c.vr + (g & 7) * 1024);
  if constexpr (FIN && g < 10 && g != 1 && g != 5) k2_pack<1, (g < 1 ? 0 : g < 5 ? g - 1 : g - 2)>(c);
  if constexpr (!FIN) acc_zero4<4 * g>();
  if constexpr (FIN && (QK ? g < 24 && (g & 3) == 1 : g < 6)) k2_dma<(QK ? g / 4 : g) + 2>(c.dt, 0u, c.vlane, c.sK, 0u);
  if constexpr (QK && g >= 24 && (g & 3) == 1) k2_dma<(g - 24) / 4>(c.dt, c.ldsb + 65536u + c.vslot_w, c.vlane, c.sK, c.vsoff);
  SBAR();
  if constexpr (QK) {
    if constexpr (db == 0) qk_init<AQ + 4>(c.s[kb][2 + m], c.fr[g & 15], c.ci[kb][2 + m]); else qk_mfma<AQ + 8 * db + 4>(c.s[kb][2 + m], c.fr[g & 15]);
    SBAR();
  }
  if constexpr (FIN && g < 8) k2_exp<1, 8 + g>(c);
  if constexpr (!QK && g < 8) asm volatile("s_nop 0");
  if constexpr (FL) {
    acc_store4<4 * g, 1024 * g>(c.sP, c.vlane);
    if constexpr (g == 12) { float a = c.mS[0], b = c.mS[1], d = c.L[0][0], e = c.L[1][0]; PINV(a); PINV(b); PINV(d); PINV(e);
      st16w<32 * 1024>((u32x4){__float_as_uint(a), __float_as_uint(b), __float_as_uint(d), __float_as_uint(e)}, c.sP, c.vlane, 0u); }
  }
  if constexpr (QK) {
    if constexpr (g == 12) asm volatile("s_add_u32 %0, %1, %2\n\ts_add_u32 %1, %1, 0x8000" : "=&s"(c.vsoff), "+s"(c.ksoff) : "n"(MB16) : "scc");
    if constexpr (g == 13) s_seleq(c.vsoff, c.pos, c.kend - 1, c.wbase + 65536u);
    if constexpr (g == 14) s_seleq(c.ksoff, c.pos, c.kn1 - 2, c.t0off);
    if constexpr (g == 15) s_seleq(c.ksoff, c.pos, c.kend - 2, c.wbase);
    if constexpr (g == 26) s_rot3(c.vslot_w);
    if constexpr (g == 20) c.mx = max4i(c.s[0][0]);
    if constexpr (g >= 21 && g < 24) max4(c.mx, c.s[0][g - 20]);
  }
  SBAR();
}
template <bool FIN, bool QK, bool FL = false> DI void k2_phaseA(K2C& c) {
  if constexpr (QK && !FIN) sfor<8>([&](auto I) AI { c.fr[I] = lds_rd128(c.kr + I * 1024); });
  sfor<(QK ? 32 : 10)>([&](auto G) AI { k2_gapA<FIN, QK, FL, G>(c); });
}
DI void k2_rebase(K2C& c, bool first) {
  sfor<2>([&](auto Q) AI {
    float x = max4i(c.s[0][2 * Q]); max4(x, c.s[0][2 * Q + 1]); max4(x, c.s[1][2 * Q]); max4(x, c.s[1][2 * Q + 1]);
    const float rm = row_max(x), dl = max2f(rm, first ? -1e30f : 0.f);
    float om = c.m[Q], oms = c.mS[Q]; PINV(om); PINV(oms);
    c.mS[Q] = first ? om : oms; c.fct[Q] = __builtin_amdgcn_exp2f(-dl); c.m[Q] = (first ? 0.f : om) + dl;
    sfor<4>([&](auto E) AI { c.s[E >> 1][2 * Q + (E & 1)] -= dl; });
  });
  c.pend = !first; c.thr = THR; c.first = false; c.spec |= 2u << c.pos;
}
template <bool PV, bool SM, bool FL, int g> DI void k2_gapB(K2C& c) {
  constexpr int kb = g >> 4, nb = g & 15;
  if constexpr (SM && g == 24) {
    asm volatile("s_waitcnt vmcnt(%0) lgkmcnt(0)\n\ts_barrier" :: "n"(FL ? 6 : 8) : "memory");
    c.kr = (lds_cptr)(size_t)((unsigned)(size_t)c.kr ^ 32768u);
    SBAR();
  }
  if constexpr (PV) {
    if constexpr ((g & 3) == 0) use4(c.fr[g & 15], c.fr[(g + 1) & 15], c.fr[(g + 2) & 15], c.fr[(g + 3) & 15]);
    if constexpr (SM && FL && kb == 0) pv_mfmaz<8 * nb>(c.fr[g & 15], c.p[0]); else pv_mfma<8 * nb>(c.fr[g & 15], c.p[2 * kb]);
    SBAR();
    if constexpr (g < 2) { c.fr[(8 + 2 * g) & 15] = lds_rd128(c.vr + (8 + 2 * g) * 1024); c.fr[(9 + 2 * g) & 15] = lds_rd128(c.vr + (9 + 2 * g) * 1024); }
    else if constexpr (g < 8) c.fr[(g + 10) & 15] = lds_rd128(c.vr + (g + 10) * 1024);
    else if constexpr (g < 10) { c.fr[(2 * g + 2) & 15] = lds_rd128(c.vr + (2 * g + 2) * 1024); c.fr[(2 * g + 3) & 15] = lds_rd128(c.vr + (2 * g + 3) * 1024); }
    else if constexpr (g < 20) c.fr[(g + 12) & 15] = lds_rd128(c.vr + (g + 12) * 1024);
  }
  if constexpr (SM) {
    if constexpr (g >= 24) c.fr[(g + 8) & 15] = lds_rd128(c.kr + (g - 24) * 1024);
    if constexpr (g >= 16 && g < 24) k2_pack<0, g - 16>(c);
    if constexpr ((g & 3) == 1) { if constexpr (g < 24) k2_dma<g / 4 + 2>(c.dt, 0u, c.vlane, c.sK, 0u); else k2_dma<(g - 24) / 4>(c.dt, c.ldsb + c.kslot_w, c.vlane, c.sK, c.ksoff); }
  }
  SBAR();
  if constexpr (PV) {
    if constexpr (SM && FL && kb == 0) pv_mfmaz<8 * nb + 4>(c.fr[g & 15], c.p[1]); else pv_mfma<8 * nb + 4>(c.fr[g & 15], c.p[2 * kb + 1]);
    SBAR();
  }
  if constexpr (PV && (nb == 13 || nb == 14)) { if constexpr (SM && FL && kb == 0) mfma_vz(c.L[nb - 13], c.ones, c.p[nb - 13]); else mfma_vv(c.L[nb - 13], c.ones, c.p[2 * kb + nb - 13]); SBAR(); }
  if constexpr (!SM && FL) c.ob[g] = __builtin_bit_cast(f32x4, __builtin_amdgcn_raw_buffer_load_b128(c.sP, c.vlane, g * 1024, 16));
  if constexpr (SM) {
    if constexpr (PV && g >= 2 && g < 6) max4(c.mx, c.s[1][g - 2]);
    if constexpr (g == 6) {
      if constexpr (!PV) k2_rebase(c, true);
      else if (__builtin_expect(__any(c.mx > c.thr), 0)) k2_rebase(c, c.first);
    }
    if constexpr (g == 23) { c.vr = c.vbase + c.vslot_r; PINV(c.vr); s_rot3(c.vslot_r); }
    if constexpr (g == 26) asm volatile("s_xor_b32 %0, %0, 0x8000" : "+s"(c.kslot_w) :: "scc");
    if constexpr (g >= 7 && g < 23) k2_exp<0, g - 7>(c);
    if constexpr (g >= 24) k2_exp<1, g - 24>(c);
    if constexpr (!PV && g >= 7) asm volatile("s_nop 0");
  }
  SBAR();
}
template <bool PV, bool SM, bool FL = false> DI void k2_phaseB(K2C& c) {
  if constexpr (!PV) { MFMA_DRAIN(); SBAR(); }
  if constexpr (!SM) { PINV(c.L[0]); PINV(c.L[1]); }
  sfor<32>([&](auto G) AI { k2_gapB<PV, SM, FL, G>(c); });
}
DI void k2_scaleO(K2C& c) { MFMA_DRAIN(); SBAR(); sfor<64>([&](auto I) AI { acc_scale2<2 * I>(c.fct[(I >> 1) & 1]); });
  PINV(c.L[0]); PINV(c.L[1]); c.L[0] *= c.fct[0]; c.L[1] *= c.fct[1]; PINV(c.L[0]); PINV(c.L[1]); c.pend = false; }

struct K2E { u32x4 cx[16]; lds_cptr rc, rn; unsigned slotw, wsoff; srd_t sP; u32x4 pk; };
template <int CH, int g> DI void k2_gapC(K2C& c, K2E& e) {
  constexpr int kb = g >> 4, j = (g >> 1) & 7, m = g & 1;
  if constexpr (CH == 3 && g == 24) { asm volatile("s_waitcnt vmcnt(6)\n\ts_barrier" ::: "memory"); SBAR(); }
  if constexpr ((g & 3) == 0) use4(c.fr[g & 15], c.fr[(g + 1) & 15], c.fr[(g + 2) & 15], c.fr[(g + 3) & 15]);
  if constexpr (j == 0) mfma_vz(c.s[kb][m], c.fr[g & 15], e.cx[0]); else mfma_vv(c.s[kb][m], c.fr[g & 15], e.cx[2 * j]);
  SBAR();
  if constexpr (CH < 2 && g == 28) { asm volatile("s_barrier" ::: "memory"); SBAR(); }
  if constexpr (g < 24) c.fr[(g + 8) & 15] = lds_rd128(e.rc + (g + 8) * 1024);
  else if constexpr (CH < 6) c.fr[(g + 8) & 15] = lds_rd128(e.rn + (g - 24) * 1024);
  if constexpr (CH < 3 && g >= 8 && g < 16) k2_dma<g - 8>(c.dt, c.ldsb + e.slotw, c.vlane, c.sK, e.wsoff);
  SBAR();
  if constexpr (j == 0) mfma_vz(c.s[kb][2 + m], c.fr[g & 15], e.cx[1]); else mfma_vv(c.s[kb][2 + m], c.fr[g & 15], e.cx[2 * j + 1]);
  SBAR();
  constexpr int u = (g & 15) - 2;
  if constexpr (u >= 0 && u < 6 && (g >= 16 || CH > 0)) {
    constexpr int n = 2 * CH + (g < 16 ? -1 : 0), a = u / 3, i = u % 3;
    if constexpr (i < 2) { const f32x4& t = c.s[kb ^ 1][2 * a + i]; e.pk[2 * i] = cvtpk(t[0], t[1]); e.pk[2 * i + 1] = cvtpk(t[2], t[3]); PINV(e.pk); }
    else st16w<(2 * n + a) * 1024>(e.pk, e.sP, c.vlane, 0u);
  }
  SBAR();
}
template <int CH> DI void k2_phaseC(K2C& c, K2E& e) { sfor<32>([&](auto G) AI { k2_gapC<CH, G>(c, e); }); }

template <class F> DI void phase_attn(const Params& P, char* lds, unsigned long long tag, const unsigned wid, u32x4 (&qo)[16], srd_t scode, F&& filler) {
  const char *KVW = P.ws + WS_K; char *Pf = P.ws + WS_PF, *part = P.ws + WS_PART; gu64* flags = (gu64*)(P.ws + WS_FL);
  const unsigned b = blockIdx.x, xcd = b & 7u, idx = b >> 3, h = 2u * xcd + (idx >> 4), pi = (idx >> 1) & 7u, roleB = idx & 1u;
  const unsigned pair = h * 8u + pi;
  const int NA = pi == 0 ? 18 : 17;
  const int n1 = roleB ? 32 - 2 * (int)pi - NA : NA, tb1 = roleB ? NA : 0, ntot = roleB ? 34 - NA : NA;
  const unsigned wbase = 2u * MB16 + h * (DH * DM * 2) + wid * 8192u;
  const unsigned t0off = h * (SEQ * DH * 2) + wid * 8192u, k0off = t0off + (unsigned)tb1 * 32768u, k1off = n1 > 1 ? k0off + 32768u : t0off;
  K2C c;
  c.ldsb = (unsigned)(size_t)lds + wid * 8192u; c.vlane = lane_id() * 16u;
  c.sK = mk_srd(KVW, 2u * MB16 + NH * DH * DM * 2);
  unsigned qb = 15u - pi;
  ACC_CLAIM();
  if (roleB) sfor<16>([&](auto I) AI { acc_write4<AQ2 + 4 * I>(qo[I]); }); else sfor<16>([&](auto I) AI { acc_write4<AQ + 4 * I>(qo[I]); });
  SBAR();
  filler();
  wait_flags(flags + FL_S1 + (xcd + 8u * ((idx & 16u) + (lane_id() & 15u))) * 16u, tag);
  k2_dma8(c.ldsb, c.vlane, c.sK, k0off);
  if (roleB) {
    const srd_t sq = mk_srd(P.ws + ((size_t)pair * 4u + wid) * 16384u, 16384u);
    sfor<16>([&](auto I) AI { acc_load4<AQ + 4 * I, I * 1024>(sq, c.vlane); });
  }
  k2_dma8(c.ldsb + 32768u, c.vlane, c.sK, k1off); k2_dma8(c.ldsb + 65536u, c.vlane, c.sK, k0off + MB16);
  SBAR();
  const unsigned lane = lane_id();
  { const int dq = (int)(lane & 15u) - 16 * (int)(lane >> 5) - 4 * (int)((lane >> 4) & 1u);
    sfor<16>([&](auto E) AI { constexpr int t = E >> 2, i = E & 3; c.tri[t][i] = (8 * (t & 1) + i - 16 * (t >> 1) > dq) ? -__builtin_inff() : __builtin_inff(); });
    sfor<4>([&](auto T) AI { PINV(c.tri[T]); }); }
  c.kr = (lds_cptr)lds + c.vlane; c.kslot_w = 0u; c.vslot_w = 32768u; c.vslot_r = 0u;
  c.sP = mk_srd(part + ((size_t)pair * 4u + wid) * 33792u, 33792u);
  c.pos = 0; c.kn1 = roleB ? n1 : 99; c.kend = ntot; c.t0off = t0off; c.wbase = wbase; c.ksoff = k1off; c.vbase = (lds_cptr)lds + 65536u + c.vlane;
  c.spec = (7u << (ntot - 3)) | (roleB ? (n1 > 1 ? 7u << (n1 - 2) : 3u) : 0u);
  int relb = 64 * tb1 - (int)(qb * 128u + wid * 32u);
  u32x2 f2 = {0u, 0u}; unsigned sink = 0u;
  auto prelude = [&]() AI {
    if (__builtin_expect((c.spec >> c.pos) & 1u, 0)) {
      if (c.pend) k2_scaleO(c);
      float r0 = -c.m[0], r1 = -c.m[1];
      if (c.pos == c.kn1) {
        sfor<16>([&](auto I) AI { acc_mov4<AQ + 4 * I, AQ2 + 4 * I>(); });
        r0 = 0.f; r1 = 0.f; c.thr = -__builtin_inff(); c.first = true; relb = -64 * n1 - (int)(pi * 128u + wid * 32u);
      }
      k2_cinit(c, r0, r1, relb + 64 * c.pos);
      if (!roleB && c.pos == ntot - 1) asm volatile("buffer_load_dwordx2 %0, off, %1, 0 sc1" : "=v"(f2) : "s"(mk_srd(P.ws + WS_FL + (FL_S2 + pair * 16u) * 8u, 8u)) : "memory");
      if (c.pos == ntot - 3 && wid == 3) ld4(sink, scode, code_off());
      asm volatile("s_nop 1");
    }
    SBAR();
  };
  sfor<2>([&](auto Q) AI { c.L[Q] = f32x4{}; PINV(c.L[Q]); c.m[Q] = 0.f; c.fct[Q] = 1.f; c.mS[Q] = 0.f; });
  c.ones = (bf16x8){0x3f80, 0x3f80, 0x3f80, 0x3f80, 0x3f80, 0x3f80, 0x3f80, 0x3f80}; PINV(c.ones);
  c.pend = false; c.thr = THR; c.first = false;
  k2_cinit(c, 0.f, 0.f, relb);
  SBAR();
  asm volatile("s_waitcnt vmcnt(16) lgkmcnt(0)\n\ts_barrier" ::: "memory");
  k2_phaseA<false, true>(c); k2_phaseB<false, true>(c);
  if (wid == 0) flag_store(flags + FL_S4 + b * 16u, tag);
  ++c.pos;
  auto run = [&](int stop) AI {
#pragma nounroll
    while (c.pos < stop) {
      prelude();
      k2_phaseA<true, true>(c); k2_phaseB<true, true>(c);
      PINV(f2); PINV(sink);
      ++c.pos;
    }
  };
  run(roleB ? n1 + 1 : ntot);
  if (roleB) {
    prelude();
    k2_phaseA<true, true, true>(c); k2_phaseB<true, true, true>(c);
    PINV(sink);
    if (wid == 0) flag_store(flags + FL_S2 + pair * 16u, tag);
    ++c.pos;
    run(ntot);
  }
  if (c.pend) k2_scaleO(c);
  qb = roleB ? pi : qb;
  const bool early = !roleB && rfl(f2[0]) == (unsigned)tag && rfl(f2[1]) == (unsigned)(tag >> 32);
  SBAR();
  k2_phaseA<true, false>(c);
  k2_dma8(c.ldsb + 65536u + c.vslot_w, c.vlane, c.sK, wbase + 3u * 32768u);
  if (early) { c.ml = __builtin_bit_cast(f32x4, __builtin_amdgcn_raw_buffer_load_b128(c.sP, c.vlane, 32 * 1024, 16)); SBAR(); k2_phaseB<true, false, true>(c); } else k2_phaseB<true, false>(c);
  MFMA_DRAIN(); SBAR();
  PINV(c.L[0]); PINV(c.L[1]);
  const float l[2] = {c.L[0][0], c.L[1][0]};
  K2E e;
  if (roleB) {
    const float rl[2] = {1.0f / l[0], 1.0f / l[1]};
    sfor<16>([&](auto I) AI { constexpr int r = 16 * (I >> 1) + 4 * (I & 1); e.cx[I] = pack8(acc_read4<r>(), acc_read4<r + 8>(), rl[I & 1]); PINV(e.cx[I]); });
  } else {
    if (!early) {
      wait_flags(flags + FL_S2 + pair * 16u, tag);
      c.ml = __builtin_bit_cast(f32x4, __builtin_amdgcn_raw_buffer_load_b128(c.sP, c.vlane, 32 * 1024, 16));
      sfor<32>([&](auto K) AI { c.ob[K] = __builtin_bit_cast(f32x4, __builtin_amdgcn_raw_buffer_load_b128(c.sP, c.vlane, K * 1024, 16)); });
    }
    const f32x4 ml = c.ml;
    SBAR();
    float fa[2], fb[2];
    sfor<2>([&](auto Q) AI { const float mm = fmaxf(c.m[Q], ml[0 + Q]), wa = __builtin_amdgcn_exp2f(c.m[Q] - mm), wb = __builtin_amdgcn_exp2f(ml[0 + Q] - mm), rl = 1.0f / (wa * l[Q] + wb * ml[2 + Q]); fa[Q] = wa * rl; fb[Q] = wb * rl; });
    sfor<16>([&](auto I) AI { constexpr int r = 16 * (I >> 1) + 4 * (I & 1), q = I & 1;
      e.cx[I] = pack8(acc_read4<r>() * fa[q] + c.ob[r / 4] * fb[q], acc_read4<r + 8>() * fa[q] + c.ob[r / 4 + 2] * fb[q], 1.0f); PINV(e.cx[I]); });
  }
  e.sP = mk_srd(Pf + ((size_t)h * 64u + qb * 4u + wid) * 28672u, 28672u);
  const unsigned kA = c.kslot_w, kB = c.kslot_w ^ 32768u;
  const unsigned v2 = 65536u + c.vslot_r, v3 = 65536u + c.vslot_w, v4 = 65536u + (98304u - c.vslot_r - c.vslot_w);
  const lds_cptr lb = (lds_cptr)lds + c.vlane;
  asm volatile("s_waitcnt vmcnt(0)\n\ts_barrier" ::: "memory");
  e.rc = lb + kA;
#pragma unroll
  for (int i = 0; i < 8; ++i) c.fr[i] = lds_rd128(e.rc + i * 1024);
  e.rn = lb + kB; e.slotw = v4; e.wsoff = wbase + 4u * 32768u; SBAR(); k2_phaseC<0>(c, e);
  e.rc = e.rn; e.rn = lb + v2; e.slotw = kA; e.wsoff = wbase + 5u * 32768u; SBAR(); k2_phaseC<1>(c, e);
  e.rc = e.rn; e.rn = lb + v3; e.slotw = kB; e.wsoff = wbase + 6u * 32768u; SBAR(); k2_phaseC<2>(c, e);
  e.rc = e.rn; e.rn = lb + v4; SBAR(); k2_phaseC<3>(c, e);
  e.rc = e.rn; e.rn = lb + kA; SBAR(); k2_phaseC<4>(c, e);
  e.rc = e.rn; e.rn = lb + kB; SBAR(); k2_phaseC<5>(c, e);
  e.rc = e.rn; SBAR(); k2_phaseC<6>(c, e);
  MFMA_DRAIN(); sfor<4>([&](auto I) AI { PINV(c.s[1][I]); }); SBAR();
  st16w<26 * 1024>(pack8(c.s[1][0], c.s[1][1], 1.0f), e.sP, c.vlane, 0u); st16w<27 * 1024>(pack8(c.s[1][2], c.s[1][3], 1.0f), e.sP, c.vlane, 0u);
}
constexpr int OUTW = 2 * DM;
DI void reduce_load(u32x4 (&v)[16], unsigned task, srd_t sP, unsigned lane) {
#pragma unroll
  for (int h = 0; h < 16; ++h) v[h] = __builtin_amdgcn_raw_buffer_load_b128(sP, lane * 16u, task * 1024u + h * (64u * 28672u), 16);
}
DI void reduce_sum(const u32x4 (&v)[16], unsigned task, unsigned lane, unsigned short* out) {
  const unsigned rb = task / 28u, f = task % 28u;
  float s[8];
#pragma unroll
  for (int i = 0; i < 8; ++i) {
    float a[16];
#pragma unroll
    for (int h = 0; h < 16; ++h) a[h] = (i & 1) ? __uint_as_float(v[h][i >> 1] & 0xffff0000u) : __uint_as_float(v[h][i >> 1] << 16);
    s[i] = (((a[0] + a[1]) + (a[2] + a[3])) + ((a[4] + a[5]) + (a[6] + a[7]))) + (((a[8] + a[9]) + (a[10] + a[11])) + ((a[12] + a[13]) + (a[14] + a[15])));
  }
  *(u32x4*)(out + (size_t)(32u * rb + 16u * (f & 1u) + (lane & 15u)) * OUTW + 32u * (f >> 1) + 8u * (lane >> 4)) = (u32x4){cvtpk(s[0], s[1]), cvtpk(s[2], s[3]), cvtpk(s[4], s[5]), cvtpk(s[6], s[7])};
}

constexpr int HCOPY = 4160, HCH = 4 * HCOPY, XROW = 80, XCH = 96 * XROW;
constexpr int SSM_LDS = 98304, XOFF = SSM_LDS + 2 * HCH;
struct K3S { lds_cptr ha, xa; f32x16 y0, y1; s16x4 al[4][2], ah[4][2]; bf16x8 bx[4][2]; };
template <int e> DI void k3_ssm_rd(K3S& c) {
  constexpr int s = e & 3;
  c.al[s][0] = *(const volatile LAS s16x4*)(c.ha + e * 64); c.ah[s][0] = *(const volatile LAS s16x4*)(c.ha + e * 64 + 8);
  c.bx[s][0] = lds_rd128(c.xa + (63 - e) * XROW); c.bx[s][1] = lds_rd128(c.xa + (31 - e) * XROW);
  c.al[s][1] = *(const volatile LAS s16x4*)(c.ha + (e + 32) * 64); c.ah[s][1] = *(const volatile LAS s16x4*)(c.ha + (e + 32) * 64 + 8);
}
template <int e, class F> DI void k3_ssm_step(K3S& c, F&& f) {
  constexpr int s = e & 3;
  if constexpr (e + 2 < 32) k3_ssm_rd<e + 2>(c);
  SBAR();
  const bf16x8 a0 = cat(c.al[s][0], c.ah[s][0]), a1 = cat(c.al[s][1], c.ah[s][1]);
  c.y1 = MFMA32(a0, c.bx[s][0], c.y1); c.y0 = MFMA32(a0, c.bx[s][1], c.y0); c.y1 = MFMA32(a1, c.bx[s][1], c.y1);
  SBAR();
  f(std::integral_constant<int, e>{});
  SBAR();
}
DI void ssm_load(SsmS& s, const Params& P, unsigned blk, unsigned long long tag, const unsigned wid) {
  const unsigned lane = lane_id(), tid = wid * 64u + lane;
  s.c0 = 2u * (28u * (blk & 7u) + (blk >> 3));
  if (!__all((((unsigned long long)s.f0[1] << 32) | s.f0[0]) == tag))
    wait_flags((gu64*)(P.ws + WS_FL) + FL_S0 + (7u * (lane & 31u) + (s.c0 >> 6)) * 16u, tag);
  const srd_t sx = mk_srd(P.ws + WS_XT, SEQ * DM * 2);
#pragma unroll
  for (int ch = 0; ch < 2; ++ch) s.xw[ch] = __builtin_amdgcn_raw_buffer_load_b128(sx, (((tid >> 3) * DM + s.c0 + ch) * 64u + 8u * (tid & 7u)) * 2u, 0, 16);
  s.dw = *(const unsigned*)(P.ssmd + s.c0);
}
DI void ssm_build(SsmS& s, char* lds, const unsigned wid) {
  const unsigned tid = wid * 64u + lane_id();
#pragma unroll
  for (int ch = 0; ch < 2; ++ch) {
    const unsigned w[6] = {s.hA[ch][0], s.hA[ch][1], s.hA[ch][2], s.hA[ch][3], s.hB[ch][0], s.hB[ch][1]};
    unsigned o[5];
#pragma unroll
    for (int k = 0; k < 5; ++k) o[k] = __builtin_amdgcn_alignbyte(w[k + 1], w[k], 2);
    LAS char* dst = (LAS char*)lds + SSM_LDS + ch * HCH + tid * 16u + 64u;
    *(LAS u32x4*)(dst) = (u32x4){o[0], o[1], o[2], o[3]};
    *(LAS u32x4*)(dst + HCOPY) = (u32x4){w[1], w[2], w[3], w[4]};
    *(LAS u32x4*)(dst + 2 * HCOPY) = (u32x4){o[1], o[2], o[3], o[4]};
    *(LAS u32x4*)(dst + 3 * HCOPY) = (u32x4){w[2], w[3], w[4], w[5]};
    if (tid < 4u) {
      const unsigned z0 = tid ? 0u : w[0], z1 = tid ? 0u : w[1], zo = tid ? 0u : o[0];
      LAS char* d2 = (LAS char*)lds + SSM_LDS + ch * HCH + (3u - tid) * 16u;
      *(LAS u32x4*)(d2) = (u32x4){0u, 0u, 0u, z0 << 16};
      *(LAS u32x4*)(d2 + HCOPY) = (u32x4){0u, 0u, 0u, z0};
      *(LAS u32x4*)(d2 + 2 * HCOPY) = (u32x4){0u, 0u, z0 << 16, zo};
      *(LAS u32x4*)(d2 + 3 * HCOPY) = (u32x4){0u, 0u, z0, z1};
    }
    LAS char* xb = (LAS char*)lds + XOFF + ch * XCH;
    if (tid < 160u) *(LAS u32x4*)(xb + tid * 16u) = (u32x4){0u, 0u, 0u, 0u};
  }
#pragma unroll
  for (int ch = 0; ch < 2; ++ch) *(LAS u32x4*)((LAS char*)lds + XOFF + ch * XCH + (32u + (tid >> 2)) * XROW + (tid & 3u) * 16u) = s.xw[ch];
}
template <class F> DI void ssm_conv(SsmS& s, char* lds, const unsigned wid, F&& f) {
  const unsigned lane = lane_id(), hi = lane >> 5, r = lane & 31u;
  const unsigned ch = wid >> 1, dh = wid & 1u;
  K3S c;
  c.ha = (lds_cptr)lds + SSM_LDS + ch * HCH + (r & 3u) * HCOPY + (24u - 16u * dh - 8u * hi + 4u * (r >> 2)) * 2u; PINV(c.ha);
  c.xa = (lds_cptr)lds + XOFF + ch * XCH + (r + 1u) * XROW + (2u * dh + hi) * 16u; PINV(c.xa);
  c.y0 = f32x16{}; c.y1 = f32x16{};
  k3_ssm_rd<0>(c); k3_ssm_rd<1>(c);
  sfor<32>([&](auto E) AI { k3_ssm_step<E>(c, f); });
  s.y0 = c.y0; s.y1 = c.y1;
}
DI void ssm_finish(SsmS& s, const Params& P, char* lds, const unsigned wid) {
  const unsigned lane = lane_id(), tid = wid * 64u + lane, hi = lane >> 5, r = lane & 31u;
  LAS char* ys = (LAS char*)lds + SSM_LDS + wid * 8192u;
#pragma unroll
  for (int k = 0; k < 4; ++k) {
    const unsigned o = r * 128u + (((2u * k + hi) ^ (r & 7u)) << 4);
    *(LAS f32x4*)(ys + o) = (f32x4){s.y0[4 * k], s.y0[4 * k + 1], s.y0[4 * k + 2], s.y0[4 * k + 3]};
    *(LAS f32x4*)(ys + 4096u + o) = (f32x4){s.y1[4 * k], s.y1[4 * k + 1], s.y1[4 * k + 2], s.y1[4 * k + 3]};
  }
  asm volatile("s_waitcnt lgkmcnt(0)\n\ts_barrier" ::: "memory");
  const unsigned row = tid >> 2, yo0 = row * 128u + (((2u * (tid & 3u)) ^ (row & 7u)) << 4), yo1 = row * 128u + (((2u * (tid & 3u) + 1u) ^ (row & 7u)) << 4);
  const srd_t sz = mk_srd(P.ws + WS_ZT, SEQ * DM * 2);
  sfor<2>([&](auto C) AI {
    const LAS char* yp = (const LAS char*)lds + SSM_LDS + C * 16384;
    const f32x4 a = *(const LAS f32x4*)(yp + yo0) + *(const LAS f32x4*)(yp + 8192 + yo0), bb = *(const LAS f32x4*)(yp + yo1) + *(const LAS f32x4*)(yp + 8192 + yo1);
    const float dd = bf2f(C ? s.dw >> 16 : s.dw & 0xffffu);
    u32x4 z;
#pragma unroll
    for (int i = 0; i < 4; ++i) {
      const f32x2 kv = up2(s.xw[C][3 - i]), y = rb2(i < 2 ? (f32x2){a[2 * i], a[2 * i + 1]} : (f32x2){bb[2 * i - 4], bb[2 * i - 3]}), kd = rb2((f32x2){kv[1] * dd, kv[0] * dd});
      z[i] = cvtpk(y[0] + kd[0], y[1] + kd[1]);
    }
    st16w<C * 128>(z, sz, (((tid >> 3) * DM + s.c0) * 64u + 8u * (tid & 7u)) * 2u, 0u);
  });
}
DI void ssm_gate(const Params& P, unsigned blk, unsigned long long tag, const unsigned wid) {
  const unsigned lane = lane_id(), tid = wid * 64u + lane, bc0 = 64u * (blk % 7u) + 2u * (tid & 31u), t0 = 64u * (blk / 7u) + 8u * (tid >> 5);
  unsigned qw[8];
#pragma unroll
  for (int i = 0; i < 8; ++i) qw[i] = *(const unsigned*)(P.bq + (size_t)(t0 + i) * DM + bc0);
  { const unsigned pr = 32u * (blk % 7u) + (lane & 31u);
    wait_flags((gu64*)(P.ws + WS_FL) + FL_S4 + (pr / 28u + 8u * (pr % 28u)) * 16u, tag); }
  const srd_t sz = mk_srd(P.ws + WS_ZT, SEQ * DM * 2);
  const unsigned zo = (((blk / 7u) * DM + bc0) * 64u + 8u * (tid >> 5)) * 2u;
  const u32x4 z0 = __builtin_amdgcn_raw_buffer_load_b128(sz, zo, 0, 16), z1 = __builtin_amdgcn_raw_buffer_load_b128(sz, zo + 128u, 0, 16);
#pragma unroll
  for (int i = 0; i < 8; ++i) {
    const f32x2 q = up2(qw[i]), a = up2(z0[i >> 1]), bb = up2(z1[i >> 1]);
    *(unsigned*)(P.out + (size_t)(t0 + i) * OUTW + DM + bc0) = cvtpk(q[0] * a[i & 1], q[1] * bb[i & 1]);
  }
}

__global__ __launch_bounds__(256, 1) void k1_fused(Params P) {
  extern __shared__ __attribute__((aligned(16))) char lds[];
  const unsigned wid = rfl(threadIdx.x >> 6);
  const unsigned b = blockIdx.x, xcd = b & 7u, idx = b >> 3, h = 2u * xcd + (idx >> 4), j = idx & 15u;
  srd_t scode;
  { unsigned long long pc; unsigned len;
    asm volatile("s_getpc_b64 %0\n.Lcode_begin:\n\ts_mov_b32 %1, .Lcode_end-.Lcode_begin" : "=s"(pc), "=s"(len));
    scode = mk_srd((const void*)pc, len); }
  if (wid == 3) touch_lds((unsigned)(size_t)lds, scode, code_off());
  gu64* flags = (gu64*)(P.ws + WS_FL);
  const unsigned qb = (j & 1u) ? (j >> 1) : 15u - (j >> 1);
  const bool ssm = b < 224u;
  u32x4 qo[16]; SsmS s;
  const unsigned long long tag = phase_proj(P, lds, wid, qb, qo, s, scode);
  if (ssm) ssm_load(s, P, b, tag, wid);
  SBAR();
  {
    const unsigned lane = lane_id(), vl = lane * 16u;
    const lds_cptr rp = (lds_cptr)lds + WO_LDS + wid * 4096u + (lane >> 2) * 64u + 16u * (lane & 3u);
    bf16x8 wf[4];
    sfor<4>([&](auto J) AI { wf[J] = cat(lds_tr(rp + (J >> 1) * 2048 + (J & 1) * 8), lds_tr(rp + (J >> 1) * 2048 + (J & 1) * 8 + 1024)); });
    const srd_t sw_ = mk_srd(P.ws + WS_W + (size_t)h * (DH * DM * 2), DH * DM * 2);
    const srd_t sq = mk_srd(P.ws + ((size_t)(h * 8u + (j >> 1)) * 4u + wid) * 16384u, (j & 1u) ? 0u : 16384u);
    auto deferred = [&](auto E) AI {
      constexpr int k = E & 15;
      if constexpr (E < 16) st16w<k * 1024>(qo[k], sq, vl, 0u);
      else if constexpr (k < 12) { if constexpr (k < 4 * LPG) { constexpr int u = q_lunit(k), i = u >> 2, jj = u & 3; st16wa<(i & 1) * 1024>(s.late[k], s.sO, vl, s.so + (i >> 1) * s.sA + jj * s.sB); } }
      else st16w<((k - 12) >> 1) * 65536u + ((k - 12) & 1) * 1024u>(__builtin_bit_cast(u32x4, wf[k - 12]), sw_, vl, ((7u * (j & 1u) + wid) * 16u + (j & 14u)) * 1024u);
    };
    if (ssm) {
      ssm_build(s, lds, wid);
      asm volatile("s_waitcnt lgkmcnt(0)\n\ts_barrier" ::: "memory");
      ssm_conv(s, lds, wid, deferred);
    } else sfor<32>(deferred);
  }
  publish(flags + FL_S1 + b * 16u, tag, wid);
  PINV(s.y0); PINV(s.y1);
  phase_attn(P, lds, tag, wid, qo, scode, [&]() AI { if (ssm) ssm_finish(s, P, lds, wid); });
  publish(flags + FL_S3 + b * 16u, tag, wid);
  if (!(j & 1u) && j >= 2u) { const unsigned g0 = 2u * (7u * h + (j >> 1) - 1u); ssm_gate(P, g0, tag, wid); ssm_gate(P, g0 + 1u, tag, wid); }
  const unsigned lane = lane_id();
  wait_flags(flags + FL_S3 + ((lane & 7u) + 8u * (16u * ((lane >> 3) & 1u) + j)) * 16u, tag);
  if (b == 0 && wid == 0) flag_store(flags + FL_EPOCH, tag);
  const srd_t sP = mk_srd(P.ws + WS_PF, NH * 64u * 28672u);
  const unsigned task = qb * 112u + h * 7u + wid;
  u32x4 va[16], vb[16];
  reduce_load(va, task, sP, lane);
  if (wid < 3u) reduce_load(vb, task + 4u, sP, lane);
  SBAR();
  reduce_sum(va, task, lane, P.out);
  if (wid < 3u) reduce_sum(vb, task + 4u, lane, P.out);
  asm volatile(".Lcode_end:");
}

extern "C" void kernel_launch(void* const* d_in, const int* in_sizes, int n_in, void* d_out, int out_size, void* d_ws, size_t ws_size, hipStream_t stream) {
  typedef const unsigned short* cu16;
  Params P = {(cu16)d_in[0], (cu16)d_in[1], (cu16)d_in[2], (cu16)d_in[3], (cu16)d_in[4], (cu16)d_in[5], (cu16)d_in[6], (cu16)d_in[7], (cu16)d_in[8], (cu16)d_in[9], (cu16)d_in[10], (cu16)d_in[11],
              (unsigned short*)d_out, (char*)d_ws};
  (void)hipFuncSetAttribute((const void*)k1_fused, hipFuncAttributeMaxDynamicSharedMemorySize, 163840);
  k1_fused<<<256, 256, 163840, stream>>>(P);
}
```

```cpp
#include <hip/hip_runtime.h>
#include <hip/hip_bf16.h>
#include <utility>

typedef short    bf16x8 __attribute__((ext_vector_type(8)));
typedef short    s16x4  __attribute__((ext_vector_type(4)));
typedef float    f32x16 __attribute__((ext_vector_type(16)));
typedef float    f32x4  __attribute__((ext_vector_type(4)));
typedef float    f32x2  __attribute__((ext_vector_type(2)));
typedef __bf16   bf16x2 __attribute__((ext_vector_type(2)));
typedef unsigned u32x4  __attribute__((ext_vector_type(4)));
typedef unsigned u32x2  __attribute__((ext_vector_type(2)));
#define DI __device__ __forceinline__
#define AI __attribute__((always_inline))
#define LAS __attribute__((address_space(3)))
#define SBAR() __builtin_amdgcn_sched_barrier(0)
template <class T> DI void PINV(T& x) { asm volatile("" : "+v"(x)); }
template <class T> DI void PINA(T& x) { asm volatile("" : "+a"(x)); }
#define MFMA32(a, b, c) __builtin_amdgcn_mfma_f32_32x32x16_bf16((a), (b), (c), 0, 0, 0)
#define MFMA_DRAIN() asm volatile("s_nop 7\n\ts_nop 7" ::: "memory")
typedef LAS const char* lds_cptr;
typedef __amdgpu_buffer_rsrc_t srd_t;
template <int... Is> using iseq = std::integer_sequence<int, Is...>;
template <int N> using mkseq = std::make_integer_sequence<int, N>;

DI unsigned rfl(unsigned x) { return __builtin_amdgcn_readfirstlane(x); }
DI unsigned lane_id() { unsigned l; asm volatile("v_mbcnt_lo_u32_b32 %0, -1, 0\n\tv_mbcnt_hi_u32_b32 %0, -1, %0" : "=v"(l)); return l; }
DI unsigned cvtpk(float lo, float hi) { f32x2 v = {lo, hi}; bf16x2 b = __builtin_convertvector(v, bf16x2); return __builtin_bit_cast(unsigned, b); }
DI float bf2f(unsigned h) { return __uint_as_float(h << 16); }
DI float rbf(float x) { return __uint_as_float(cvtpk(x, x) << 16); }
DI bf16x8 lds_rd128(lds_cptr p) { return *(const LAS bf16x8*)p; }
DI s16x4 lds_tr(lds_cptr p) { return __builtin_amdgcn_ds_read_tr16_b64_v4i16((LAS s16x4*)p); }
DI bf16x8 cat(s16x4 l, s16x4 h) { return __builtin_shufflevector(l, h, 0, 1, 2, 3, 4, 5, 6, 7); }
DI srd_t mk_srd(const void* p, unsigned bytes) { return __builtin_amdgcn_make_buffer_rsrc((void*)p, (short)0, (int)bytes, 0x00020000); }
template <unsigned MI, unsigned SI> DI void dma16(unsigned mb, unsigned voff, srd_t srd, unsigned sb) {
  unsigned t;
  asm volatile("s_add_u32 m0, %1, %5\n\ts_add_u32 %0, %4, %6\n\tbuffer_load_dwordx4 %2, %3, %0 offen lds" : "=&s"(t) : "s"(mb), "v"(voff), "s"(srd), "s"(sb), "n"(MI), "n"(SI) : "memory", "scc");
}
template <unsigned SI> DI void st16w(u32x4 v, srd_t srd, unsigned voff, unsigned sb) {
  unsigned t; asm volatile("s_add_u32 %0, %4, %5\n\tbuffer_store_dwordx4 %1, %2, %3, %0 offen sc1\n\ts_nop 1" : "=&s"(t) : "v"(v), "v"(voff), "s"(srd), "s"(sb), "n"(SI) : "memory", "scc"); }
template <unsigned SI> DI void st16wa(f32x4 v, srd_t srd, unsigned voff, unsigned sb) {
  unsigned t; asm volatile("s_add_u32 %0, %4, %5\n\tbuffer_store_dwordx4 %1, %2, %3, %0 offen sc1\n\ts_nop 1" : "=&s"(t) : "a"(v), "v"(voff), "s"(srd), "s"(sb), "n"(SI) : "memory", "scc"); }
DI void ld4(unsigned& d, srd_t srd, unsigned voff) { asm volatile("buffer_load_dword %0, %1, %2, 0 offen" : "=v"(d) : "v"(voff), "s"(srd) : "memory"); }
template <unsigned SI> DI void ld16a(bf16x8& d, srd_t srd, unsigned voff) { unsigned t; asm volatile("s_mov_b32 %1, %4\n\tbuffer_load_dwordx4 %0, %2, %3, %1 offen sc1" : "=a"(d), "=&s"(t) : "v"(voff), "s"(srd), "n"(SI) : "memory"); }
template <int N, class F> DI void sfor_(F&& f, iseq<>) {}
template <int N, class F, int I, int... Is> DI void sfor_(F&& f, iseq<I, Is...>) { f(std::integral_constant<int, I>{}); sfor_<N>(f, iseq<Is...>{}); }
template <int N, class F> DI void sfor(F&& f) { sfor_<N>(f, mkseq<N>{}); }
DI u32x4 pack8(const f32x4& a, const f32x4& b, float sc) { u32x4 w = {cvtpk(a[0] * sc, a[1] * sc), cvtpk(a[2] * sc, a[3] * sc), cvtpk(b[0] * sc, b[1] * sc), cvtpk(b[2] * sc, b[3] * sc)}; return w; }
DI u32x4 pack8w(const f32x16& a, int s, float sc) {
  u32x4 w = {cvtpk(a[8 * s] * sc, a[8 * s + 1] * sc), cvtpk(a[8 * s + 2] * sc, a[8 * s + 3] * sc), cvtpk(a[8 * s + 4] * sc, a[8 * s + 5] * sc), cvtpk(a[8 * s + 6] * sc, a[8 * s + 7] * sc)};
  return w;
}
constexpr int SEQ = 2048, NH = 16, DH = 256, DM = 448, DC = 1024;
constexpr unsigned WRS = NH * DH * 2;
constexpr unsigned MB16 = SEQ * NH * DH * 2;
constexpr float QSCALE = 1.4426950408889634f / 16.0f;

DI unsigned code_off() { return ((blockIdx.x >> 3) * 64u + lane_id()) * 64u; }
typedef __attribute__((address_space(1))) unsigned long long gu64;
#define RLX_AGENT __ATOMIC_RELAXED, __HIP_MEMORY_SCOPE_AGENT
struct Params {
  const unsigned short *x, *ckv, *wq, *wuk, *wuv, *wo, *bq, *bk, *bv, *shk, *ssmk, *ssmd;
  unsigned short* out; char* ws;
};
struct Ctx { srd_t code; };
constexpr size_t WS_K = MB16, WS_W = 3 * (size_t)MB16, WS_XT = WS_W + (4u << 20), WS_ZT = WS_XT + (2u << 20), WS_FL = WS_ZT + (2u << 20), WS_PART = WS_FL + (1u << 20), WS_PF = WS_PART + (17u << 20);
constexpr int FL_EPOCH = 0, FL_S0 = 16, FL_S1 = FL_S0 + 256 * 16, FL_S2 = FL_S1 + 256 * 16, FL_S3 = FL_S2 + 128 * 16, FL_S4 = FL_S3 + 256 * 16;
DI void flag_store(gu64* flag, unsigned long long tag) {
  const u32x2 v = {(unsigned)tag, (unsigned)(tag >> 32)};
  asm volatile("s_nop 4\n\tbuffer_store_dwordx2 %0, %1, %2, 0 offen sc1" :: "v"(v), "v"(lane_id() * 16u), "s"(mk_srd((const void*)flag, 8u)) : "memory");
}
DI void publish(gu64* flag, unsigned long long tag, unsigned wid) {
  asm volatile("s_waitcnt vmcnt(0)\n\ts_barrier" ::: "memory");
  if (wid == 0) flag_store(flag, tag);
}
DI void wait_flags(gu64* lane_flag, unsigned long long tag) {
  for (int spin = 0; spin < (1 << 20); ++spin) {
    if (__all(__hip_atomic_load(lane_flag, RLX_AGENT) == tag)) break;
    __builtin_amdgcn_s_sleep(1);
  }
}

DI void mfma16a(f32x4& c, const bf16x8& a, const bf16x8& b) { asm volatile("v_mfma_f32_16x16x32_bf16 %0, %1, %2, %0" : "+a"(c) : "v"(a), "v"(b)); }
DI void mfma16v(f32x4& c, const bf16x8& a, const bf16x8& b) { asm volatile("v_mfma_f32_16x16x32_bf16 %0, %1, %2, %0" : "+v"(c) : "v"(a), "v"(b)); }
DI void mfma16z(f32x4& c, const bf16x8& a, const bf16x8& b) { asm volatile("v_mfma_f32_16x16x32_bf16 %0, %1, %2, 0" : "=&v"(c) : "v"(a), "v"(b)); }
DI f32x2 up2(unsigned w) { f32x2 r = {__uint_as_float(w << 16), __uint_as_float(w & 0xffff0000u)}; return r; }
DI f32x2 rb2(f32x2 v) { return up2(cvtpk(v[0], v[1])); }
constexpr unsigned qxs(int s) { return 16384u * s; }
constexpr unsigned qws(int s) { return s == 0 ? 65536u : s == 1 ? 131072u : 98304u; }
constexpr unsigned WO_LDS = 49152u;
struct SsmS { u32x4 xw[2], hA[2]; u32x2 hB[2], f0; unsigned dw, c0; f32x16 y0, y1; f32x4 late[12]; srd_t sO; unsigned so, sA, sB; };
struct Frags { bf16x8 x[8]; s16x4 wl[8], wh[8]; };
struct K1C {
  unsigned ldsb, lds8, lds4, wid, vlane, b, hb;
  unsigned vx_kv[2], vx_q[2], vw[4];
  srd_t sX, sW, sXq, sWq, sO;
  unsigned sx_kv, sw_kv, sx_q, sw_q, so;
  lds_cptr xr_kv[2], wr[8], xq[2], wq[8];
  const char* pf; unsigned pfstep, xoff; srd_t sx2;
  f32x4 acc[8][8]; Frags f0, f1; f32x4 ta, tb; u32x4 pk;
  f32x4 qa[16][2]; s16x4 ql[16], qh[16]; bf16x8 qx[2][2];
  unsigned kw[11], vv[8], kv[8]; u32x4 sk; u32x2 ep; f32x2 kf[11], sc[4], pa, pt;
  unsigned long long tag; SsmS* ss; srd_t scode;
};
DI void touch_lds(unsigned ldsb, srd_t srd, unsigned voff) {
  asm volatile("s_add_u32 m0, %0, 147456\n\ts_nop 0\n\tbuffer_load_dword %1, %2, 0 offen lds" :: "s"(ldsb), "v"(voff), "s"(srd) : "memory", "scc"); }
DI void k1_touch(const K1C& c) {
  asm volatile("s_add_u32 m0, %0, 147456\n\ts_nop 0\n\tglobal_load_lds_dword %1, off" :: "s"(c.ldsb), "v"(c.pf) : "memory", "scc"); }
template <int buf, int j> DI void k1_piece(const K1C& c, unsigned sw, unsigned sx) {
  constexpr int p = j >> 1;
  if constexpr (!(j & 1)) dma16<buf * 32768u + 65536u + p * 1024u, 2u * p * WRS>(c.lds8, c.vw[(p & 1) + 2 * (p >> 2)], c.sW, sw);
  else dma16<buf * 32768u + p * 1024u, 8u * p * (DC * 2)>(c.lds8, c.vx_kv[(p >> 1) & 1], c.sX, sx);
}
template <int S, int j> DI void k1_qpiece(const K1C& c) {
  if constexpr (j % 3 != 1) { constexpr int p = 2 * (j / 3) + (j % 3 == 2); dma16<qws(S % 3) + p * 1024u, (64u * S + 2u * p) * WRS>(c.lds8, c.vw[(p & 1) + 2 * (p >> 2)], c.sWq, c.sw_q); }
  else { constexpr int p = j / 3; dma16<qxs(S % 3) + p * 1024u, 8u * p * (DM * 2) + 128u * S>(c.lds4, c.vx_q[p & 1], c.sXq, c.sx_q); }
}
template <int buf> DI void k1_stage(const K1C& c, int st) { sfor<16>([&](auto J) AI { k1_piece<buf, J>(c, c.sw_kv + (unsigned)st * (64u * WRS), c.sx_kv + (unsigned)st * 128u); }); }
template <int buf, int ks, int i> DI void k1_read(const K1C& c, Frags& f) {
  constexpr int so = buf * 32768;
  if constexpr (i < 2 || i >= 10) { constexpr int k = i < 2 ? i : i - 8, nb = k >> 1, t = k & 1;
    const s16x4 v = lds_tr(c.wr[nb] + so + ks * 16384 + t * 2048); if constexpr (t) f.wh[nb] = v; else f.wl[nb] = v; }
  else { constexpr int mb = i - 2; f.x[mb] = lds_rd128(c.xr_kv[ks] + so + (mb >> 1) * 4096 + (mb & 1) * 1024); }
}
template <int F> DI void k1_qw(K1C& c) {
  constexpr int T = F >> 5, ks = (F >> 4) & 1, nb = F & 15, sl = T % 3, o = (sl == 2 ? 32768 : 0) + ks * 16384 + (nb >> 3) * 256;
  if constexpr ((F & 31) < 8 && sl != 0) { c.wq[nb] += sl == 1 ? 65536 : -65536; PINV(c.wq[nb]); }
  c.ql[F & 15] = lds_tr(c.wq[nb & 7] + o); c.qh[F & 15] = lds_tr(c.wq[nb & 7] + o + 2048);
}
template <int T, int ks> DI void k1_qx(K1C& c) { c.qx[ks][0] = lds_rd128(c.xq[ks] + qxs(T % 3)); c.qx[ks][1] = lds_rd128(c.xq[ks] + qxs(T % 3) + 2048); }
template <int m> DI void k1_pre(K1C& c, const Params& P) {
  if constexpr (m == 0) {
    asm volatile("" : "+v"(c.kw[0]), "+v"(c.kw[1]), "+v"(c.kw[2]), "+v"(c.kw[3]), "+v"(c.kw[4]), "+v"(c.kw[5]), "+v"(c.kw[6]), "+v"(c.kw[7]), "+v"(c.kw[8]), "+v"(c.kw[9]), "+v"(c.kw[10]),
                 "+v"(c.vv[0]), "+v"(c.vv[1]), "+v"(c.vv[2]), "+v"(c.vv[3]), "+v"(c.vv[4]), "+v"(c.vv[5]), "+v"(c.vv[6]), "+v"(c.vv[7]), "+v"(c.sk), "+v"(c.ep));
    const unsigned long long e0 = ((unsigned long long)rfl(c.ep[1]) << 32) | rfl(c.ep[0]);
    c.tag = (e0 ^ 0x9E3779B97F4A7C15ull) * 0xD1342543DE82EF95ull + 1ull;
  } else if constexpr (m < 12) { c.kf[m - 1] = up2(c.kw[m - 1]); PINV(c.kf[m - 1]); }
  else if constexpr (m < 14) { constexpr int i = m - 12;
    c.sc[2 * i] = (f32x2){bf2f(c.sk[i] & 0xffffu), bf2f(c.sk[2 + i] & 0xffffu)}; c.sc[2 * i + 1] = (f32x2){bf2f(c.sk[i] >> 16), bf2f(c.sk[2 + i] >> 16)}; PINV(c.sc[2 * i]); PINV(c.sc[2 * i + 1]); }
  else if constexpr (m < 78) { constexpr int i = (m - 14) >> 3, s = (m - 14) & 7;
    if constexpr (s == 0) { c.pa = rb2(c.kf[i] * c.sc[3]); PINV(c.pa); }
    else if constexpr (s == 7) { const f32x2 a = c.pa * up2(c.vv[i]); c.kv[i] = cvtpk(a[0], a[1]); PINV(c.kv[i]); }
    else if constexpr (s & 1) { c.pt = rb2(c.kf[i + (s + 1) / 2] * c.sc[3 - (s + 1) / 2]); PINV(c.pt); }
    else { c.pa = rb2(c.pa + c.pt); PINV(c.pa); }
  } else {
    constexpr unsigned sel = m == 78 ? 0x05040100u : 0x07060302u;
    const unsigned tid = c.wid * 64u + (c.vlane >> 4), xo = (((c.b / 7u) * DM + 64u * (c.b % 7u) + 2u * (tid & 31u)) * 64u + 8u * (tid >> 5)) * 2u;
    st16w<(m - 78) * 128>((u32x4){__builtin_amdgcn_perm(c.kv[6], c.kv[7], sel), __builtin_amdgcn_perm(c.kv[4], c.kv[5], sel), __builtin_amdgcn_perm(c.kv[2], c.kv[3], sel), __builtin_amdgcn_perm(c.kv[0], c.kv[1], sel)},
                          mk_srd(P.ws + WS_XT, SEQ * DM * 2), xo, 0u);
  }
}
template <int i> DI void k1_preload(K1C& c, const Params& P) {
  const unsigned tid = c.wid * 64u + (c.vlane >> 4), bc0 = 64u * (c.b % 7u) + 2u * (tid & 31u), vo = ((64u * (c.b / 7u) + 8u * (tid >> 5)) * DM + bc0) * 2u;
  if constexpr (i < 11) ld4(c.kw[i], mk_srd(P.bk, SEQ * DM * 2), vo + (unsigned)((i - 3) * DM * 2));
  else if constexpr (i < 19) ld4(c.vv[i - 11], mk_srd(P.bv, SEQ * DM * 2), vo + (unsigned)((i - 11) * DM * 2));
  else if constexpr (i == 19) asm volatile("buffer_load_dwordx4 %0, %1, %2, 0 offen" : "=v"(c.sk) : "v"(bc0 * 8u), "s"(mk_srd(P.shk, DM * 8)) : "memory");
  else asm volatile("buffer_load_dwordx2 %0, off, %1, 0 sc1" : "=v"(c.ep) : "s"(mk_srd(P.ws + WS_FL, 64)) : "memory");
}
template <bool WA, int STEP, int buf, int G> DI void k1_gap(K1C& c, const Params& P, unsigned sw, unsigned sx) {
  constexpr int ks = G >> 6, g = G & 63, nb = g >> 3, mb = g & 7;
  Frags& fc = ks ? c.f1 : c.f0; Frags& fn = ks ? c.f0 : c.f1;
  if constexpr (G == 64) {
    asm volatile("s_waitcnt vmcnt(%0) lgkmcnt(0)\n\ts_barrier" :: "n"(STEP == 0 ? 3 : STEP == 14 ? 0 : STEP == 15 ? 12 : buf == 1 ? 2 : 1) : "memory");
    SBAR();
  }
  const bf16x8 w = cat(fc.wl[nb], fc.wh[nb]);
  if constexpr (G == 0) asm volatile("s_nop 1" :: "v"(w), "v"(fc.x[0]));
  if constexpr (WA) mfma16a(c.acc[mb][nb], w, fc.x[mb]); else mfma16a(c.acc[nb][mb], fc.x[mb], w);
  SBAR();
  if constexpr (!(g & 1) && g < 48) {
    if constexpr (ks == 0) k1_read<buf, 1, g / 2>(c, fn);
    else if constexpr (STEP != 15) k1_read<buf ^ 1, 0, g / 2>(c, fn);
  }
  if constexpr (STEP == 15 && ks == 1 && g >= 36 && !(g & 3)) { if constexpr (g == 36) k1_qx<0, 0>(c); else k1_qw<(g - 40) / 4>(c); }
  if constexpr (STEP == 0 && ks == 0 && (g & 1) && g < 16) k1_piece<1, 8 + (g >> 1)>(c, c.sw_kv + 64u * WRS, c.sx_kv + 128u);
  if constexpr (STEP == 0 && (G == 17 || G == 19 || G == 21)) { k1_touch(c); c.pf += c.pfstep; }
  if constexpr (STEP == 13 && G == 64) { if (c.wid == 3) touch_lds(c.ldsb, c.scode, code_off()); }
  if constexpr (ks == 1) {
    if constexpr (STEP <= 13 && (g & 3) == 1) k1_piece<buf, (g >> 2)>(c, sw, sx);
    if constexpr (STEP == 0 && (g & 3) == 3) k1_preload<(g >> 2)>(c, P);
    if constexpr (STEP == 0 && (g & 3) == 2 && g >= 44) k1_preload<16 + ((g - 44) >> 2)>(c, P);
    if constexpr (STEP == 14 && (g & 1) && g < 60 && (g >> 1) % 5 != 4) { constexpr int j = (g >> 1) - (g >> 1) / 5; k1_qpiece<j / 12, j % 12>(c); }
    if constexpr (STEP == 15 && (g & 3) == 1 && g < 48) k1_qpiece<2, (g >> 2)>(c);
  }
  if constexpr (STEP <= 13 && G == 127) { k1_touch(c); c.pf += c.pfstep; if constexpr (buf == 0) touch_lds(c.ldsb, c.sx2, c.xoff); }
  if constexpr (STEP >= 13) { constexpr int X = (STEP - 13) * 128 + G; if constexpr (X >= 56 && X % 4 == 3 && (X - 56) / 4 < 80) k1_pre<(X - 56) / 4>(c, P); }
  SBAR();
}
template <bool WA, int STEP, int buf> DI void k1_step(K1C& c, const Params& P, int sb) {
  const unsigned sw = c.sw_kv + (unsigned)sb * (64u * WRS), sx = c.sx_kv + (unsigned)sb * 128u;
  sfor<128>([&](auto G) AI { k1_gap<WA, STEP, buf, G>(c, P, sw, sx); });
}
#define LPG 3
constexpr int q_lpos(int k) { return k == 0 ? 7 : k == 1 ? 3 : 5; }
constexpr bool q_late(int u) { for (int k = 0; k < LPG; ++k) if (u % 8 == q_lpos(k)) return true; return false; }
constexpr int q_lidx(int u) { for (int k = 0; k < LPG; ++k) if (u % 8 == q_lpos(k)) return LPG * (u / 8) + k; return 0; }
constexpr int q_lunit(int k) { return 8 * (k / (LPG ? LPG : 1)) + q_lpos(k % (LPG ? LPG : 1)); }
constexpr int q_stores(int lo, int hi) { int n = 0; for (int u = 0; u < 32; ++u) n += (!q_late(u) && 6 * u + 5 > lo && 6 * u + 5 < hi); return n; }
constexpr int q_wait(int T) {
  return T == 0 ? 14 + q_stores(-1, 26) : T == 1 ? 12 + q_stores(-1, 58) : (T + 2 <= 6 ? 12 : 0) + q_stores(32 * (T - 2) + 48, 32 * T + 26); }
template <bool WA, int X> DI void k1_qgap(K1C& c, const Params& P) {
  constexpr int T = X >> 5, g = X & 31, ks = g >> 4, nb = g & 15;
  if constexpr (g == 26 && T < 6) {
    asm volatile("s_waitcnt vmcnt(%0) lgkmcnt(0)\n\ts_barrier" :: "n"(q_wait(T)) : "memory"); SBAR();
    if constexpr (T == 1) { if (c.wid == 0) flag_store((gu64*)(P.ws + WS_FL) + FL_S0 + c.b * 16u, c.tag); SBAR(); }
  }
  const bf16x8 w = cat(c.ql[X & 15], c.qh[X & 15]);
  if constexpr (X < 16) mfma16z(c.qa[nb][0], w, c.qx[ks][0]); else mfma16v(c.qa[nb][0], w, c.qx[ks][0]);
  SBAR();
  if constexpr (g < 22) { constexpr int f = 6 + g + (g + 4) / 5 - (g > 20 ? 1 : 0); k1_qw<32 * T + f>(c); if constexpr (g % 5 == 0 && g < 20) k1_qw<32 * T + f + 1>(c); }
  if constexpr (g >= 26 && T < 6) k1_qw<32 * (T + 1) + g - 26>(c);
  SBAR();
  if constexpr (X < 16) mfma16z(c.qa[nb][1], w, c.qx[ks][1]); else mfma16v(c.qa[nb][1], w, c.qx[ks][1]);
  SBAR();
  if constexpr (g == 7) k1_qx<T, 1>(c);
  if constexpr (g == 27 && T < 6) k1_qx<T + 1, 0>(c);
  if constexpr (X >= 26 && !(X & 1) && (X - 26) % 32 < 24 && (X - 26) / 32 + 3 <= 6) k1_qpiece<(X - 26) / 32 + 3, ((X - 26) % 32) / 2>(c);
  constexpr int u = X / 6, ph = X % 6;
  if constexpr (u < 32) {
    constexpr int i = u >> 2, j = u & 3;
    if constexpr (ph == 1) { c.ta = c.acc[i][2 * j]; PINV(c.ta); }
    if constexpr (ph == 2) { c.tb = c.acc[i][2 * j + 1]; PINV(c.tb); }
    if constexpr (ph == 3) { c.pk[0] = cvtpk(c.ta[0], c.ta[1]); c.pk[1] = cvtpk(c.ta[2], c.ta[3]); PINV(c.pk); }
    if constexpr (ph == 4) { c.pk[2] = cvtpk(c.tb[0], c.tb[1]); c.pk[3] = cvtpk(c.tb[2], c.tb[3]); PINV(c.pk); }
    if constexpr (ph == 5 && q_late(u)) { c.ss->late[q_lidx(u)] = __builtin_bit_cast(f32x4, c.pk); PINA(c.ss->late[q_lidx(u)]); }
    if constexpr (ph == 5 && !q_late(u)) st16w<(WA ? (i >> 1) * 16384u + j * 2048u : (i >> 1) * 2048u + j * 16384u) + (i & 1) * 1024u>(c.pk, c.sO, c.vlane, c.so);
  }
  if constexpr (X >= 200 && X < 205) {
    const unsigned tid = c.wid * 64u + (c.vlane >> 4), c0 = 2u * (28u * (c.b & 7u) + (c.b >> 3)); constexpr int ch = (X - 200) >> 1;
    if constexpr (X == 204) asm volatile("buffer_load_dwordx2 %0, %1, %2, 0 offen sc1" : "=v"(c.ss->f0) : "v"((FL_S0 + (7u * (tid & 31u) + (c0 >> 6)) * 16u) * 8u), "s"(mk_srd(P.ws + WS_FL, 1u << 20)) : "memory");
    else if constexpr (X & 1) asm volatile("buffer_load_dwordx2 %0, %1, %2, 0 offen offset:16" : "=v"(c.ss->hB[ch]) : "v"(tid * 16u), "s"(mk_srd(P.ssmk + (size_t)(c0 + ch) * SEQ, c.b < 224u ? SEQ * 2 : 0)) : "memory");
    else asm volatile("buffer_load_dwordx4 %0, %1, %2, 0 offen" : "=v"(c.ss->hA[ch]) : "v"(tid * 16u), "s"(mk_srd(P.ssmk + (size_t)(c0 + ch) * SEQ, c.b < 224u ? SEQ * 2 : 0)) : "memory");
  }
  if constexpr (X >= 196 && X < 200) {
    constexpr int j = X - 196; const unsigned ln = c.vlane >> 4;
    dma16<WO_LDS + j * 1024u, (j >> 1) * 256u + (j & 1) * (16u * DM * 2)>(c.lds4, (ln >> 2) * (DM * 2) + (ln & 3u) * 16u, mk_srd(P.wo, NH * DH * DM * 2), (c.hb >> 1) * (32u * DM * 2) + (7u * (c.hb & 1u) + c.wid) * 64u);
  }
  SBAR();
}

template <bool WA> DI void k1_main(K1C& c, const Params& P, const char* pf2, u32x4 (&qo)[16]) {
  sfor<64>([&](auto I) AI { c.acc[I >> 3][I & 7] = f32x4{}; PINA(c.acc[I >> 3][I & 7]); });
  SBAR();
  sfor<8>([&](auto J) AI { k1_piece<1, J>(c, c.sw_kv + 64u * WRS, c.sx_kv + 128u); });
  asm volatile("s_waitcnt vmcnt(8)\n\ts_barrier" ::: "memory");
  sfor<24>([&](auto I) AI { k1_read<0, 0, I>(c, c.f0); });
  SBAR();
  c.sx2 = mk_srd(P.wq, 0u);
  k1_step<WA, 0, 0>(c, P, 2);
#pragma nounroll
  for (int t = 1; t < 13; t += 2) {
    k1_step<WA, 1, 1>(c, P, t + 2);
    c.sx2 = mk_srd(t == 1 ? P.wq : t == 3 ? P.x : t == 5 ? P.wo : t == 7 ? P.ssmk : P.bq, t == 1 || t == 5 ? DM * WRS : t == 11 ? 0u : SEQ * DM * 2u);
    c.pf = t == 9 ? pf2 : c.pf;
    k1_step<WA, 1, 0>(c, P, t + 3);
  }
  k1_step<WA, 13, 1>(c, P, 15);
  {
    const unsigned lane = lane_id(), rho = lane & 15u, g = lane >> 4, q = rho >> 2, p = lane & 3u, f = q | ((g & 1u) << 2);
    sfor<2>([&](auto I) AI { c.vx_q[I] = (lane >> 3) * (DM * 2) + 16u * ((lane & 7u) ^ (4u * I + (lane >> 4)));
      c.xq[I] = (lds_cptr)(size_t)(c.ldsb + (32u * c.wid + rho) * 128u + 16u * ((4u * I + g) ^ (rho >> 1))); PINV(c.xq[I]); });
    sfor<8>([&](auto I) AI { c.wq[I] = (lds_cptr)(size_t)(c.ldsb + 65536u + (8u * g + q) * 512u + ((unsigned)I ^ f) * 32u + 8u * p); PINV(c.wq[I]); });
  }
  k1_step<WA, 14, 0>(c, P, 0);
  k1_step<WA, 15, 1>(c, P, 0);
  sfor<224>([&](auto X) AI { k1_qgap<WA, X>(c, P); });
  MFMA_DRAIN(); SBAR();
  sfor<16>([&](auto I) AI { constexpr int db = I >> 1, q = I & 1; PINV(c.qa[2 * db][q]); PINV(c.qa[2 * db + 1][q]); qo[I] = pack8(c.qa[2 * db][q], c.qa[2 * db + 1][q], QSCALE); PINV(qo[I]); SBAR(); });
  asm volatile("s_waitcnt vmcnt(0)" : "+v"(c.ss->hA[0]), "+v"(c.ss->hA[1]), "+v"(c.ss->hB[0]), "+v"(c.ss->hB[1]), "+v"(c.ss->f0) :: "memory");
}

DI unsigned long long phase_proj(const Params& P, char* lds, const unsigned wid, const unsigned qblk, u32x4 (&qo)[16], SsmS& ss, srd_t scode) {
  const unsigned lane = lane_id(), wr = wid >> 1, wc = wid & 1;
  const unsigned b = blockIdx.x, xcd = b & 7u, idx = b >> 3;
  const unsigned pm = idx & 7u, which = idx >> 3, h = 2u * xcd + (which >> 1), isV = which & 1u;
  K1C c; c.ss = &ss; c.scode = scode;
  c.ldsb = (unsigned)(size_t)lds; c.lds8 = c.ldsb + wid * 8192u; c.lds4 = c.ldsb + wid * 4096u; c.wid = wid; c.vlane = lane * 16u; c.b = b; c.hb = 16u * h + (idx & 15u);
  const unsigned short* wkv = isV ? P.wuv : P.wuk;
  c.sX = mk_srd(P.ckv, SEQ * DC * 2); c.sW = mk_srd(wkv, DC * WRS); c.sXq = mk_srd(P.x, SEQ * DM * 2); c.sWq = mk_srd(P.wq, DM * WRS);
  sfor<2>([&](auto I) AI { c.vx_kv[I] = (lane >> 3) * (DC * 2) + 16u * ((lane & 7u) ^ (4u * I + (lane >> 4))); });
  sfor<4>([&](auto I) AI { const unsigned f = (2u * (I & 1) + (lane >> 5)) | (4u * (I >> 1)); c.vw[I] = (lane >> 5) * WRS + 16u * ((lane & 31u) ^ (f << 1)); });
  c.sx_kv = (256u * pm + 64u * wid) * (DC * 2); c.sw_kv = 16u * wid * WRS + h * (DH * 2);
  c.sx_q = (128u * qblk + 32u * wid) * (DM * 2);  c.sw_q = c.sw_kv;
  k1_stage<0>(c, 0);
    const unsigned rho = lane & 15u, g = lane >> 4;
  sfor<2>([&](auto I) AI { c.xr_kv[I] = (lds_cptr)lds + (wr * 128u + 16u * (rho >> 3) + (rho & 7u)) * 128u + 16u * ((4u * I + g) ^ (rho >> 1)); PINV(c.xr_kv[I]); });
  { const unsigned q = (lane & 15u) >> 2, p = lane & 3u, f = q | ((g & 1u) << 2);
    sfor<8>([&](auto I) AI { c.wr[I] = (lds_cptr)lds + 65536u + (8u * g + q) * 512u + (8u * wc + ((unsigned)I ^ f)) * 32u + 8u * p; PINV(c.wr[I]); }); }
  c.sO = mk_srd(P.ws + WS_K + (isV ? MB16 : 0u) + (size_t)h * (SEQ * DH * 2), SEQ * DH * 2);
  c.so = (8u * pm + 4u * wr) * 16384u + wc * 8192u;
  const bool pw = lane >= 32u;
  const unsigned k = lane & 15u;
  unsigned pfa = (192u + 8u * pm + 2u * wid + ((lane >> 2) & 1u)) * WRS + h * (DH * 2) + (lane & 3u) * 128u, pfb = (256u * pm + 64u * which + 16u * wid + k) * (DC * 2) + 384u;
  PINV(pfa); PINV(pfb);
  c.pf = (const char*)(pw ? wkv : P.ckv) + (pw ? pfa : pfb);
  c.pfstep = pw ? 64u * WRS : 128u;
  c.xoff = ((b * 4u + wid) * 28u + min(lane, 27u)) * 128u;
  const unsigned st0 = (lane & 16u) ? 0u : 3u;
  pfa = (64u * st0 + 4u * (idx & 15u) + wid) * WRS + h * (DH * 2) + (lane & 3u) * 128u; pfb = (128u * qblk + 64u * (idx >> 4) + 16u * wid + k) * (DM * 2) + 128u * st0;
  PINV(pfa); PINV(pfb);
  const char* pf2 = (pw ? (const char*)P.wq : (const char*)P.x) + (pw ? pfa : pfb);
  ss.sO = c.sO; ss.so = c.so; ss.sA = isV ? 2048u : 16384u; ss.sB = isV ? 16384u : 2048u;
  if (isV) k1_main<false>(c, P, pf2, qo); else k1_main<true>(c, P, pf2, qo);
  return c.tag;
}

constexpr float THR = 8.0f;
constexpr int AQ = 128, AQ2 = 192;
template <int R> DI void pv_mfma(const bf16x8& a, const u32x4& b) { asm volatile("v_mfma_f32_16x16x32_bf16 a[%c2:%c3], %0, %1, a[%c2:%c3]" :: "v"(a), "v"(b), "n"(R), "n"(R + 3)); }
template <int R> DI void pv_mfmaz(const bf16x8& a, const u32x4& b) { asm volatile("v_mfma_f32_16x16x32_bf16 a[%c2:%c3], %0, %1, 0" :: "v"(a), "v"(b), "n"(R), "n"(R + 3)); }
template <int R> DI void qk_mfma(f32x4& c, const bf16x8& a) { asm volatile("v_mfma_f32_16x16x32_bf16 %0, %1, a[%c2:%c3], %0" : "+v"(c) : "v"(a), "n"(R), "n"(R + 3)); }
template <int R> DI void qk_init(f32x4& d, const bf16x8& a, const f32x4& c) { asm volatile("v_mfma_f32_16x16x32_bf16 %0, %1, a[%c3:%c4], %2" : "=&v"(d) : "v"(a), "v"(c), "n"(R), "n"(R + 3)); }
DI void mfma_vv(f32x4& c, const bf16x8& a, const u32x4& b) { asm volatile("v_mfma_f32_16x16x32_bf16 %0, %1, %2, %0" : "+v"(c) : "v"(a), "v"(b)); }
DI void mfma_vz(f32x4& d, const bf16x8& a, const u32x4& b) { asm volatile("v_mfma_f32_16x16x32_bf16 %0, %1, %2, 0" : "=&v"(d) : "v"(a), "v"(b)); }
DI void use4(const bf16x8& w, const bf16x8& x, const bf16x8& y, const bf16x8& z) { asm volatile("" :: "v"(w), "v"(x), "v"(y), "v"(z)); }
template <int R> DI void acc_zero4() { asm volatile("v_accvgpr_write_b32 a[%c0], 0\n\tv_accvgpr_write_b32 a[%c1], 0\n\tv_accvgpr_write_b32 a[%c2], 0\n\tv_accvgpr_write_b32 a[%c3], 0" :: "n"(R), "n"(R + 1), "n"(R + 2), "n"(R + 3)); }
template <int R> DI void acc_write4(const u32x4& w) { asm volatile("v_accvgpr_write_b32 a[%c4], %0\n\tv_accvgpr_write_b32 a[%c5], %1\n\tv_accvgpr_write_b32 a[%c6], %2\n\tv_accvgpr_write_b32 a[%c7], %3" :: "v"(w[0]), "v"(w[1]), "v"(w[2]), "v"(w[3]), "n"(R), "n"(R + 1), "n"(R + 2), "n"(R + 3)); }
template <int R, int S> DI void acc_mov4() { asm volatile("v_accvgpr_mov_b32 a[%c0], a[%c4]\n\tv_accvgpr_mov_b32 a[%c1], a[%c5]\n\tv_accvgpr_mov_b32 a[%c2], a[%c6]\n\tv_accvgpr_mov_b32 a[%c3], a[%c7]" :: "n"(R), "n"(R + 1), "n"(R + 2), "n"(R + 3), "n"(S), "n"(S + 1), "n"(S + 2), "n"(S + 3)); }
template <int R> DI f32x4 acc_read4() { float a, b, c, d; asm volatile("v_accvgpr_read_b32 %0, a[%c4]\n\tv_accvgpr_read_b32 %1, a[%c5]\n\tv_accvgpr_read_b32 %2, a[%c6]\n\tv_accvgpr_read_b32 %3, a[%c7]" : "=v"(a), "=v"(b), "=v"(c), "=v"(d) : "n"(R), "n"(R + 1), "n"(R + 2), "n"(R + 3)); return (f32x4){a, b, c, d}; }
template <int R> DI void acc_scale2(float f) { float t, u; asm volatile("v_accvgpr_read_b32 %0, a[%c3]\n\tv_accvgpr_read_b32 %1, a[%c4]\n\tv_mul_f32 %0, %2, %0\n\tv_mul_f32 %1, %2, %1\n\tv_accvgpr_write_b32 a[%c3], %0\n\tv_accvgpr_write_b32 a[%c4], %1" : "=&v"(t), "=&v"(u) : "v"(f), "n"(R), "n"(R + 1)); }
template <int R, unsigned SI> DI void acc_store4(srd_t srd, unsigned voff) { unsigned t; asm volatile("s_mov_b32 %0, %3\n\tbuffer_store_dwordx4 a[%c4:%c5], %1, %2, %0 offen sc1" : "=&s"(t) : "v"(voff), "s"(srd), "n"(SI), "n"(R), "n"(R + 3) : "memory"); }
template <int R, unsigned SI> DI void acc_load4(srd_t srd, unsigned voff) { unsigned t; asm volatile("s_mov_b32 %0, %3\n\tbuffer_load_dwordx4 a[%c4:%c5], %1, %2, %0 offen sc1" : "=&s"(t) : "v"(voff), "s"(srd), "n"(SI), "n"(R), "n"(R + 3) : "memory"); }
#define A8(n) "a" #n "0", "a" #n "1", "a" #n "2", "a" #n "3", "a" #n "4", "a" #n "5", "a" #n "6", "a" #n "7", "a" #n "8", "a" #n "9"
#define ACC_CLAIM() asm volatile("; acc claim" ::: "a0", "a1", "a2", "a3", "a4", "a5", "a6", "a7", "a8", "a9", A8(1), A8(2), A8(3), A8(4), A8(5), A8(6), A8(7), A8(8), A8(9), A8(10), A8(11), A8(12), A8(13), A8(14), \
  A8(15), A8(16), A8(17), A8(18), A8(19), A8(20), A8(21), A8(22), A8(23), A8(24), "a250", "a251", "a252", "a253", "a254", "a255")
DI float max2f(float a, float b) { float r; asm("v_max_f32_e32 %0, %1, %2" : "=v"(r) : "v"(a), "v"(b)); return r; }
DI void max4(float& m, const f32x4& v) { asm volatile("v_max3_f32 %0, %0, %1, %2\n\tv_max3_f32 %0, %0, %3, %4" : "+v"(m) : "v"(v[0]), "v"(v[1]), "v"(v[2]), "v"(v[3])); }
DI float max4i(const f32x4& v) { float m; asm volatile("v_max_f32_e32 %0, %1, %2\n\tv_max3_f32 %0, %0, %3, %4" : "=&v"(m) : "v"(v[0]), "v"(v[1]), "v"(v[2]), "v"(v[3])); return m; }
DI float row_max(float x) {
  asm volatile("s_nop 1" : "+v"(x));
  auto a = __builtin_amdgcn_permlane32_swap(__float_as_uint(x), __float_as_uint(x), false, false); x = max2f(__uint_as_float(a[0]), __uint_as_float(a[1]));
  asm volatile("s_nop 1" : "+v"(x));
  auto b = __builtin_amdgcn_permlane16_swap(__float_as_uint(x), __float_as_uint(x), false, false); return max2f(__uint_as_float(b[0]), __uint_as_float(b[1]));
}
struct K2C {
  unsigned ldsb, vlane;
  srd_t sK;
  lds_cptr kr, vr;
  unsigned kslot_w, vslot_w, vslot_r;
  unsigned ksoff, vsoff, dt;
  f32x4 s[2][4], ci[2][4]; u32x4 p[4]; bf16x8 fr[16];
  f32x4 L[2]; bf16x8 ones;
  float m[2], fct[2], mS[2], mx, thr; bool pend, first;
  unsigned spec, t0off, wbase; int pos, kn1, kend; lds_cptr vbase;
  f32x4 ml, ob[32];
  srd_t sP;
  f32x4 tri[4];
};
template <int J> DI void k2_dma(unsigned& t, unsigned m0v, unsigned voff, srd_t srd, unsigned soff) {
  if constexpr (J == 0) asm volatile("s_mov_b32 m0, %1\n\ts_mov_b32 %0, %4\n\tbuffer_load_dwordx4 %2, %3, %0 offen sc1 lds" : "=&s"(t) : "s"(m0v), "v"(voff), "s"(srd), "s"(soff) : "memory");
  else if constexpr (J == 4) asm volatile("s_add_u32 m0, m0, 4096\n\ts_add_u32 %0, %0, 4096\n\tbuffer_load_dwordx4 %1, %2, %0 offen sc1 lds" : "+s"(t) : "v"(voff), "s"(srd) : "memory", "scc");
  else asm volatile("buffer_load_dwordx4 %0, %1, %2 offen offset:%3 sc1 lds" :: "v"(voff), "s"(srd), "s"(t), "n"((J & 3) * 1024) : "memory");
}
DI void k2_dma8(unsigned m0v, unsigned voff, srd_t srd, unsigned soff) { unsigned t; sfor<8>([&](auto J) AI { k2_dma<J>(t, m0v, voff, srd, soff); }); }
DI void k2_cinit(K2C& c, float r0, float r1, int rel) {
  sfor<2>([&](auto KB) AI {
    const int rb = rel + 32 * KB;
    const float cap = rb > 0 ? -__builtin_inff() : __builtin_inff(), lift = rb < 0 ? __builtin_inff() : -__builtin_inff();
    const float a0 = __builtin_fminf(r0, cap), a1 = __builtin_fminf(r1, cap), b0 = __builtin_fminf(a0, lift), b1 = __builtin_fminf(a1, lift);
    sfor<16>([&](auto E) AI { constexpr int t = E >> 2, i = E & 3; c.ci[KB][t][i] = __builtin_amdgcn_fmed3f(c.tri[t][i], t >> 1 ? a1 : a0, t >> 1 ? b1 : b0); });
    sfor<4>([&](auto T) AI { PINV(c.ci[KB][T]); });
  });
}
DI void s_seleq(unsigned& x, int a, int b, unsigned y) { asm volatile("s_cmp_eq_u32 %1, %2\n\ts_cselect_b32 %0, %3, %0" : "+s"(x) : "s"(a), "s"(b), "s"(y) : "scc"); }
DI void s_rot3(unsigned& x) { asm volatile("s_add_u32 %0, %0, 0x8000\n\ts_cmp_lg_u32 %0, 0x18000\n\ts_cselect_b32 %0, %0, 0" : "+s"(x) :: "scc"); }
template <int kb, int e> DI void k2_exp(K2C& c) { c.s[kb][e >> 2][e & 3] = __builtin_amdgcn_exp2f(c.s[kb][e >> 2][e & 3]); PINV(c.s[kb][e >> 2]); }
template <int kb, int w> DI void k2_pack(K2C& c) { c.p[2 * kb + (w >> 2)][w & 3] = cvtpk(c.s[kb][w >> 1][2 * (w & 1)], c.s[kb][w >> 1][2 * (w & 1) + 1]); PINV(c.p[2 * kb + (w >> 2)]); }
template <bool FIN, bool QK, bool FL, int g> DI void k2_gapA(K2C& c) {
  constexpr int kb = g >> 4, db = (g >> 1) & 7, m = g & 1;
  if constexpr (QK) {
    if constexpr ((g & 3) == 0) use4(c.fr[g & 15], c.fr[(g + 1) & 15], c.fr[(g + 2) & 15], c.fr[(g + 3) & 15]);
    if constexpr (db == 0) qk_init<AQ>(c.s[kb][m], c.fr[g & 15], c.ci[kb][m]); else qk_mfma<AQ + 8 * db>(c.s[kb][m], c.fr[g & 15]);
    SBAR();
    if constexpr (g < 24) c.fr[(g + 8) & 15] = lds_rd128(c.kr + (g + 8) * 1024);
  }
  if constexpr (FIN && (QK ? g >= 24 : g < 8)) c.fr[g & 7] = lds_rd128(c.vr + (g & 7) * 1024);
  if constexpr (FIN && g < 10 && g != 1 && g != 5) k2_pack<1, (g < 1 ? 0 : g < 5 ? g - 1 : g - 2)>(c);
  if constexpr (!FIN) acc_zero4<4 * g>();
  if constexpr (FIN && (QK ? g < 24 && (g & 3) == 1 : g < 6)) k2_dma<(QK ? g / 4 : g) + 2>(c.dt, 0u, c.vlane, c.sK, 0u);
  if constexpr (QK && g >= 24 && (g & 3) == 1) k2_dma<(g - 24) / 4>(c.dt, c.ldsb + 65536u + c.vslot_w, c.vlane, c.sK, c.vsoff);
  SBAR();
  if constexpr (QK) {
    if constexpr (db == 0) qk_init<AQ + 4>(c.s[kb][2 + m], c.fr[g & 15], c.ci[kb][2 + m]); else qk_mfma<AQ + 8 * db + 4>(c.s[kb][2 + m], c.fr[g & 15]);
    SBAR();
  }
  if constexpr (FIN && g < 8) k2_exp<1, 8 + g>(c);
  if constexpr (!QK && g < 8) asm volatile("s_nop 0");
  if constexpr (FL) {
    acc_store4<4 * g, 1024 * g>(c.sP, c.vlane);
    if constexpr (g == 12) { float a = c.mS[0], b = c.mS[1], d = c.L[0][0], e = c.L[1][0]; PINV(a); PINV(b); PINV(d); PINV(e);
      st16w<32 * 1024>((u32x4){__float_as_uint(a), __float_as_uint(b), __float_as_uint(d), __float_as_uint(e)}, c.sP, c.vlane, 0u); }
  }
  if constexpr (QK) {
    if constexpr (g == 12) asm volatile("s_add_u32 %0, %1, %2\n\ts_add_u32 %1, %1, 0x8000" : "=&s"(c.vsoff), "+s"(c.ksoff) : "n"(MB16) : "scc");
    if constexpr (g == 13) s_seleq(c.vsoff, c.pos, c.kend - 1, c.wbase + 65536u);
    if constexpr (g == 14) s_seleq(c.ksoff, c.pos, c.kn1 - 2, c.t0off);
    if constexpr (g == 15) s_seleq(c.ksoff, c.pos, c.kend - 2, c.wbase);
    if constexpr (g == 26) s_rot3(c.vslot_w);
    if constexpr (g == 20) c.mx = max4i(c.s[0][0]);
    if constexpr (g >= 21 && g < 24) max4(c.mx, c.s[0][g - 20]);
  }
  SBAR();
}
template <bool FIN, bool QK, bool FL = false> DI void k2_phaseA(K2C& c) {
  if constexpr (QK && !FIN) sfor<8>([&](auto I) AI { c.fr[I] = lds_rd128(c.kr + I * 1024); });
  sfor<(QK ? 32 : 10)>([&](auto G) AI { k2_gapA<FIN, QK, FL, G>(c); });
}
DI void k2_rebase(K2C& c, bool first) {
  sfor<2>([&](auto Q) AI {
    float x = max4i(c.s[0][2 * Q]); max4(x, c.s[0][2 * Q + 1]); max4(x, c.s[1][2 * Q]); max4(x, c.s[1][2 * Q + 1]);
    const float rm = row_max(x), dl = max2f(rm, first ? -1e30f : 0.f);
    float om = c.m[Q], oms = c.mS[Q]; PINV(om); PINV(oms);
    c.mS[Q] = first ? om : oms; c.fct[Q] = __builtin_amdgcn_exp2f(-dl); c.m[Q] = (first ? 0.f : om) + dl;
    sfor<4>([&](auto E) AI { c.s[E >> 1][2 * Q + (E & 1)] -= dl; });
  });
  c.pend = !first; c.thr = THR; c.first = false; c.spec |= 2u << c.pos;
}
template <bool PV, bool SM, bool FL, int g> DI void k2_gapB(K2C& c) {
  constexpr int kb = g >> 4, nb = g & 15;
  if constexpr (SM && g == 24) {
    asm volatile("s_waitcnt vmcnt(%0) lgkmcnt(0)\n\ts_barrier" :: "n"(FL ? 6 : 8) : "memory");
    c.kr = (lds_cptr)(size_t)((unsigned)(size_t)c.kr ^ 32768u);
    SBAR();
  }
  if constexpr (PV) {
    if constexpr ((g & 3) == 0) use4(c.fr[g & 15], c.fr[(g + 1) & 15], c.fr[(g + 2) & 15], c.fr[(g + 3) & 15]);
    if constexpr (SM && FL && kb == 0) pv_mfmaz<8 * nb>(c.fr[g & 15], c.p[0]); else pv_mfma<8 * nb>(c.fr[g & 15], c.p[2 * kb]);
    SBAR();
    if constexpr (g < 2) { c.fr[(8 + 2 * g) & 15] = lds_rd128(c.vr + (8 + 2 * g) * 1024); c.fr[(9 + 2 * g) & 15] = lds_rd128(c.vr + (9 + 2 * g) * 1024); }
    else if constexpr (g < 8) c.fr[(g + 10) & 15] = lds_rd128(c.vr + (g + 10) * 1024);
    else if constexpr (g < 10) { c.fr[(2 * g + 2) & 15] = lds_rd128(c.vr + (2 * g + 2) * 1024); c.fr[(2 * g + 3) & 15] = lds_rd128(c.vr + (2 * g + 3) * 1024); }
    else if constexpr (g < 20) c.fr[(g + 12) & 15] = lds_rd128(c.vr + (g + 12) * 1024);
  }
  if constexpr (SM) {
    if constexpr (g >= 24) c.fr[(g + 8) & 15] = lds_rd128(c.kr + (g - 24) * 1024);
    if constexpr (g >= 16 && g < 24) k2_pack<0, g - 16>(c);
    if constexpr ((g & 3) == 1) { if constexpr (g < 24) k2_dma<g / 4 + 2>(c.dt, 0u, c.vlane, c.sK, 0u); else k2_dma<(g - 24) / 4>(c.dt, c.ldsb + c.kslot_w, c.vlane, c.sK, c.ksoff); }
  }
  SBAR();
  if constexpr (PV) {
    if constexpr (SM && FL && kb == 0) pv_mfmaz<8 * nb + 4>(c.fr[g & 15], c.p[1]); else pv_mfma<8 * nb + 4>(c.fr[g & 15], c.p[2 * kb + 1]);
    SBAR();
  }
  if constexpr (PV && (nb == 13 || nb == 14)) { if constexpr (SM && FL && kb == 0) mfma_vz(c.L[nb - 13], c.ones, c.p[nb - 13]); else mfma_vv(c.L[nb - 13], c.ones, c.p[2 * kb + nb - 13]); SBAR(); }
  if constexpr (!SM && FL) c.ob[g] = __builtin_bit_cast(f32x4, __builtin_amdgcn_raw_buffer_load_b128(c.sP, c.vlane, g * 1024, 16));
  if constexpr (SM) {
    if constexpr (PV && g >= 2 && g < 6) max4(c.mx, c.s[1][g - 2]);
    if constexpr (g == 6) {
      if constexpr (!PV) k2_rebase(c, true);
      else if (__builtin_expect(__any(c.mx > c.thr), 0)) k2_rebase(c, c.first);
    }
    if constexpr (g == 23) { c.vr = c.vbase + c.vslot_r; PINV(c.vr); s_rot3(c.vslot_r); }
    if constexpr (g == 26) asm volatile("s_xor_b32 %0, %0, 0x8000" : "+s"(c.kslot_w) :: "scc");
    if constexpr (g >= 7 && g < 23) k2_exp<0, g - 7>(c);
    if constexpr (g >= 24) k2_exp<1, g - 24>(c);
    if constexpr (!PV && g >= 7) asm volatile("s_nop 0");
  }
  SBAR();
}
template <bool PV, bool SM, bool FL = false> DI void k2_phaseB(K2C& c) {
  if constexpr (!PV) { MFMA_DRAIN(); SBAR(); }
  if constexpr (!SM) { PINV(c.L[0]); PINV(c.L[1]); }
  sfor<32>([&](auto G) AI { k2_gapB<PV, SM, FL, G>(c); });
}
DI void k2_scaleO(K2C& c) { MFMA_DRAIN(); SBAR(); sfor<64>([&](auto I) AI { acc_scale2<2 * I>(c.fct[(I >> 1) & 1]); });
  PINV(c.L[0]); PINV(c.L[1]); c.L[0] *= c.fct[0]; c.L[1] *= c.fct[1]; PINV(c.L[0]); PINV(c.L[1]); c.pend = false; }

struct K2E { u32x4 cx[16]; lds_cptr rc, rn; unsigned slotw, wsoff; srd_t sP; u32x4 pk; };
template <int CH, int g> DI void k2_gapC(K2C& c, K2E& e) {
  constexpr int kb = g >> 4, j = (g >> 1) & 7, m = g & 1;
  if constexpr (CH == 3 && g == 24) { asm volatile("s_waitcnt vmcnt(6)\n\ts_barrier" ::: "memory"); SBAR(); }
  if constexpr ((g & 3) == 0) use4(c.fr[g & 15], c.fr[(g + 1) & 15], c.fr[(g + 2) & 15], c.fr[(g + 3) & 15]);
  if constexpr (j == 0) mfma_vz(c.s[kb][m], c.fr[g & 15], e.cx[0]); else mfma_vv(c.s[kb][m], c.fr[g & 15], e.cx[2 * j]);
  SBAR();
  if constexpr (CH < 2 && g == 28) { asm volatile("s_barrier" ::: "memory"); SBAR(); }
  if constexpr (g < 24) c.fr[(g + 8) & 15] = lds_rd128(e.rc + (g + 8) * 1024);
  else if constexpr (CH < 6) c.fr[(g + 8) & 15] = lds_rd128(e.rn + (g - 24) * 1024);
  if constexpr (CH < 3 && g >= 8 && g < 16) k2_dma<g - 8>(c.dt, c.ldsb + e.slotw, c.vlane, c.sK, e.wsoff);
  SBAR();
  if constexpr (j == 0) mfma_vz(c.s[kb][2 + m], c.fr[g & 15], e.cx[1]); else mfma_vv(c.s[kb][2 + m], c.fr[g & 15], e.cx[2 * j + 1]);
  SBAR();
  constexpr int u = (g & 15) - 2;
  if constexpr (u >= 0 && u < 6 && (g >= 16 || CH > 0)) {
    constexpr int n = 2 * CH + (g < 16 ? -1 : 0), a = u / 3, i = u % 3;
    if constexpr (i < 2) { const f32x4& t = c.s[kb ^ 1][2 * a + i]; e.pk[2 * i] = cvtpk(t[0], t[1]); e.pk[2 * i + 1] = cvtpk(t[2], t[3]); PINV(e.pk); }
    else st16w<(2 * n + a) * 1024>(e.pk, e.sP, c.vlane, 0u);
  }
  SBAR();
}
template <int CH> DI void k2_phaseC(K2C& c, K2E& e) { sfor<32>([&](auto G) AI { k2_gapC<CH, G>(c, e); }); }

template <class F> DI void phase_attn(const Params& P, char* lds, unsigned long long tag, const unsigned wid, u32x4 (&qo)[16], srd_t scode, F&& filler) {
  const char *KVW = P.ws + WS_K; char *Pf = P.ws + WS_PF, *part = P.ws + WS_PART; gu64* flags = (gu64*)(P.ws + WS_FL);
  const unsigned b = blockIdx.x, xcd = b & 7u, idx = b >> 3, h = 2u * xcd + (idx >> 4), pi = (idx >> 1) & 7u, roleB = idx & 1u;
  const unsigned pair = h * 8u + pi;
  const int NA = pi == 0 ? 18 : 17;
  const int n1 = roleB ? 32 - 2 * (int)pi - NA : NA, tb1 = roleB ? NA : 0, ntot = roleB ? 34 - NA : NA;
  const unsigned wbase = 2u * MB16 + h * (DH * DM * 2) + wid * 8192u;
  const unsigned t0off = h * (SEQ * DH * 2) + wid * 8192u, k0off = t0off + (unsigned)tb1 * 32768u, k1off = n1 > 1 ? k0off + 32768u : t0off;
  K2C c;
  c.ldsb = (unsigned)(size_t)lds + wid * 8192u; c.vlane = lane_id() * 16u;
  c.sK = mk_srd(KVW, 2u * MB16 + NH * DH * DM * 2);
  unsigned qb = 15u - pi;
  ACC_CLAIM();
  if (roleB) sfor<16>([&](auto I) AI { acc_write4<AQ2 + 4 * I>(qo[I]); }); else sfor<16>([&](auto I) AI { acc_write4<AQ + 4 * I>(qo[I]); });
  SBAR();
  filler();
  wait_flags(flags + FL_S1 + (xcd + 8u * ((idx & 16u) + (lane_id() & 15u))) * 16u, tag);
  k2_dma8(c.ldsb, c.vlane, c.sK, k0off);
  if (roleB) {
    const srd_t sq = mk_srd(P.ws + ((size_t)pair * 4u + wid) * 16384u, 16384u);
    sfor<16>([&](auto I) AI { acc_load4<AQ + 4 * I, I * 1024>(sq, c.vlane); });
  }
  k2_dma8(c.ldsb + 32768u, c.vlane, c.sK, k1off); k2_dma8(c.ldsb + 65536u, c.vlane, c.sK, k0off + MB16);
  SBAR();
  const unsigned lane = lane_id();
  { const int dq = (int)(lane & 15u) - 16 * (int)(lane >> 5) - 4 * (int)((lane >> 4) & 1u);
    sfor<16>([&](auto E) AI { constexpr int t = E >> 2, i = E & 3; c.tri[t][i] = (8 * (t & 1) + i - 16 * (t >> 1) > dq) ? -__builtin_inff() : __builtin_inff(); });
    sfor<4>([&](auto T) AI { PINV(c.tri[T]); }); }
  c.kr = (lds_cptr)lds + c.vlane; c.kslot_w = 0u; c.vslot_w = 32768u; c.vslot_r = 0u;
  c.sP = mk_srd(part + ((size_t)pair * 4u + wid) * 33792u, 33792u);
  c.pos = 0; c.kn1 = roleB ? n1 : 99; c.kend = ntot; c.t0off = t0off; c.wbase = wbase; c.ksoff = k1off; c.vbase = (lds_cptr)lds + 65536u + c.vlane;
  c.spec = (7u << (ntot - 3)) | (roleB ? (n1 > 1 ? 7u << (n1 - 2) : 3u) : 0u);
  int relb = 64 * tb1 - (int)(qb * 128u + wid * 32u);
  u32x2 f2 = {0u, 0u}; unsigned sink = 0u;
  auto prelude = [&]() AI {
    if (__builtin_expect((c.spec >> c.pos) & 1u, 0)) {
      if (c.pend) k2_scaleO(c);
      float r0 = -c.m[0], r1 = -c.m[1];
      if (c.pos == c.kn1) {
        sfor<16>([&](auto I) AI { acc_mov4<AQ + 4 * I, AQ2 + 4 * I>(); });
        r0 = 0.f; r1 = 0.f; c.thr = -__builtin_inff(); c.first = true; relb = -64 * n1 - (int)(pi * 128u + wid * 32u);
      }
      k2_cinit(c, r0, r1, relb + 64 * c.pos);
      if (!roleB && c.pos == ntot - 1) asm volatile("buffer_load_dwordx2 %0, off, %1, 0 sc1" : "=v"(f2) : "s"(mk_srd(P.ws + WS_FL + (FL_S2 + pair * 16u) * 8u, 8u)) : "memory");
      if (c.pos == ntot - 3 && wid == 3) ld4(sink, scode, code_off());
      asm volatile("s_nop 1");
    }
    SBAR();
  };
  sfor<2>([&](auto Q) AI { c.L[Q] = f32x4{}; PINV(c.L[Q]); c.m[Q] = 0.f; c.fct[Q] = 1.f; c.mS[Q] = 0.f; });
  c.ones = (bf16x8){0x3f80, 0x3f80, 0x3f80, 0x3f80, 0x3f80, 0x3f80, 0x3f80, 0x3f80}; PINV(c.ones);
  c.pend = false; c.thr = THR; c.first = false;
  k2_cinit(c, 0.f, 0.f, relb);
  SBAR();
  asm volatile("s_waitcnt vmcnt(16) lgkmcnt(0)\n\ts_barrier" ::: "memory");
  k2_phaseA<false, true>(c); k2_phaseB<false, true>(c);
  if (wid == 0) flag_store(flags + FL_S4 + b * 16u, tag);
  ++c.pos;
  auto run = [&](int stop) AI {
#pragma nounroll
    while (c.pos < stop) {
      prelude();
      k2_phaseA<true, true>(c); k2_phaseB<true, true>(c);
      PINV(f2); PINV(sink);
      ++c.pos;
    }
  };
  run(roleB ? n1 + 1 : ntot);
  if (roleB) {
    prelude();
    k2_phaseA<true, true, true>(c); k2_phaseB<true, true, true>(c);
    PINV(sink);
    if (wid == 0) flag_store(flags + FL_S2 + pair * 16u, tag);
    ++c.pos;
    run(ntot);
  }
  if (c.pend) k2_scaleO(c);
  qb = roleB ? pi : qb;
  const bool early = !roleB && rfl(f2[0]) == (unsigned)tag && rfl(f2[1]) == (unsigned)(tag >> 32);
  SBAR();
  k2_phaseA<true, false>(c);
  k2_dma8(c.ldsb + 65536u + c.vslot_w, c.vlane, c.sK, wbase + 3u * 32768u);
  if (early) { c.ml = __builtin_bit_cast(f32x4, __builtin_amdgcn_raw_buffer_load_b128(c.sP, c.vlane, 32 * 1024, 16)); SBAR(); k2_phaseB<true, false, true>(c); } else k2_phaseB<true, false>(c);
  MFMA_DRAIN(); SBAR();
  PINV(c.L[0]); PINV(c.L[1]);
  const float l[2] = {c.L[0][0], c.L[1][0]};
  K2E e;
  if (roleB) {
    const float rl[2] = {1.0f / l[0], 1.0f / l[1]};
    sfor<16>([&](auto I) AI { constexpr int r = 16 * (I >> 1) + 4 * (I & 1); e.cx[I] = pack8(acc_read4<r>(), acc_read4<r + 8>(), rl[I & 1]); PINV(e.cx[I]); });
  } else {
    if (!early) {
      wait_flags(flags + FL_S2 + pair * 16u, tag);
      c.ml = __builtin_bit_cast(f32x4, __builtin_amdgcn_raw_buffer_load_b128(c.sP, c.vlane, 32 * 1024, 16));
      sfor<32>([&](auto K) AI { c.ob[K] = __builtin_bit_cast(f32x4, __builtin_amdgcn_raw_buffer_load_b128(c.sP, c.vlane, K * 1024, 16)); });
    }
    const f32x4 ml = c.ml;
    SBAR();
    float fa[2], fb[2];
    sfor<2>([&](auto Q) AI { const float mm = fmaxf(c.m[Q], ml[0 + Q]), wa = __builtin_amdgcn_exp2f(c.m[Q] - mm), wb = __builtin_amdgcn_exp2f(ml[0 + Q] - mm), rl = 1.0f / (wa * l[Q] + wb * ml[2 + Q]); fa[Q] = wa * rl; fb[Q] = wb * rl; });
    sfor<16>([&](auto I) AI { constexpr int r = 16 * (I >> 1) + 4 * (I & 1), q = I & 1;
      e.cx[I] = pack8(acc_read4<r>() * fa[q] + c.ob[r / 4] * fb[q], acc_read4<r + 8>() * fa[q] + c.ob[r / 4 + 2] * fb[q], 1.0f); PINV(e.cx[I]); });
  }
  e.sP = mk_srd(Pf + ((size_t)h * 64u + qb * 4u + wid) * 28672u, 28672u);
  const unsigned kA = c.kslot_w, kB = c.kslot_w ^ 32768u;
  const unsigned v2 = 65536u + c.vslot_r, v3 = 65536u + c.vslot_w, v4 = 65536u + (98304u - c.vslot_r - c.vslot_w);
  const lds_cptr lb = (lds_cptr)lds + c.vlane;
  asm volatile("s_waitcnt vmcnt(0)\n\ts_barrier" ::: "memory");
  e.rc = lb + kA;
#pragma unroll
  for (int i = 0; i < 8; ++i) c.fr[i] = lds_rd128(e.rc + i * 1024);
  e.rn = lb + kB; e.slotw = v4; e.wsoff = wbase + 4u * 32768u; SBAR(); k2_phaseC<0>(c, e);
  e.rc = e.rn; e.rn = lb + v2; e.slotw = kA; e.wsoff = wbase + 5u * 32768u; SBAR(); k2_phaseC<1>(c, e);
  e.rc = e.rn; e.rn = lb + v3; e.slotw = kB; e.wsoff = wbase + 6u * 32768u; SBAR(); k2_phaseC<2>(c, e);
  e.rc = e.rn; e.rn = lb + v4; SBAR(); k2_phaseC<3>(c, e);
  e.rc = e.rn; e.rn = lb + kA; SBAR(); k2_phaseC<4>(c, e);
  e.rc = e.rn; e.rn = lb + kB; SBAR(); k2_phaseC<5>(c, e);
  e.rc = e.rn; SBAR(); k2_phaseC<6>(c, e);
  MFMA_DRAIN(); sfor<4>([&](auto I) AI { PINV(c.s[1][I]); }); SBAR();
  st16w<26 * 1024>(pack8(c.s[1][0], c.s[1][1], 1.0f), e.sP, c.vlane, 0u); st16w<27 * 1024>(pack8(c.s[1][2], c.s[1][3], 1.0f), e.sP, c.vlane, 0u);
}
constexpr int OUTW = 2 * DM;
template <int R> DI void reduce_sum(unsigned task, unsigned lane, const bf16x8& sel0, const bf16x8& sel1, unsigned short* out) {
  const unsigned rb = task / 28u, f = task % 28u;
  f32x4 lo = {}, hi = {}; PINV(lo); PINV(hi); asm volatile("s_nop 1");
  sfor<16>([&](auto H) AI { qk_mfma<R + 4 * H>(lo, sel0); qk_mfma<R + 4 * H>(hi, sel1); });
  MFMA_DRAIN(); PINV(lo); PINV(hi);
  *(u32x4*)(out + (size_t)(32u * rb + 16u * (f & 1u) + (lane & 15u)) * OUTW + 32u * (f >> 1) + 8u * (lane >> 4)) = pack8(lo, hi, 1.0f);
}

constexpr int HCOPY = 4160, HCH = 4 * HCOPY, XROW = 80, XCH = 96 * XROW;
constexpr int SSM_LDS = 98304, XOFF = SSM_LDS + 2 * HCH;
struct K3S { lds_cptr ha, xa; f32x16 y0, y1; s16x4 al[4][2], ah[4][2]; bf16x8 bx[4][2]; };
template <int e> DI void k3_ssm_rd(K3S& c) {
  constexpr int s = e & 3;
  c.al[s][0] = *(const volatile LAS s16x4*)(c.ha + e * 64); c.ah[s][0] = *(const volatile LAS s16x4*)(c.ha + e * 64 + 8);
  c.bx[s][0] = lds_rd128(c.xa + (63 - e) * XROW); c.bx[s][1] = lds_rd128(c.xa + (31 - e) * XROW);
  c.al[s][1] = *(const volatile LAS s16x4*)(c.ha + (e + 32) * 64); c.ah[s][1] = *(const volatile LAS s16x4*)(c.ha + (e + 32) * 64 + 8);
}
template <int e, class F> DI void k3_ssm_step(K3S& c, F&& f) {
  constexpr int s = e & 3;
  if constexpr (e + 2 < 32) k3_ssm_rd<e + 2>(c);
  SBAR();
  const bf16x8 a0 = cat(c.al[s][0], c.ah[s][0]), a1 = cat(c.al[s][1], c.ah[s][1]);
  c.y1 = MFMA32(a0, c.bx[s][0], c.y1); c.y0 = MFMA32(a0, c.bx[s][1], c.y0); c.y1 = MFMA32(a1, c.bx[s][1], c.y1);
  SBAR();
  f(std::integral_constant<int, e>{});
  SBAR();
}
DI void ssm_load(SsmS& s, const Params& P, unsigned blk, unsigned long long tag, const unsigned wid) {
  const unsigned lane = lane_id(), tid = wid * 64u + lane;
  s.c0 = 2u * (28u * (blk & 7u) + (blk >> 3));
  if (!__all((((unsigned long long)s.f0[1] << 32) | s.f0[0]) == tag))
    wait_flags((gu64*)(P.ws + WS_FL) + FL_S0 + (7u * (lane & 31u) + (s.c0 >> 6)) * 16u, tag);
  const srd_t sx = mk_srd(P.ws + WS_XT, SEQ * DM * 2);
#pragma unroll
  for (int ch = 0; ch < 2; ++ch) s.xw[ch] = __builtin_amdgcn_raw_buffer_load_b128(sx, (((tid >> 3) * DM + s.c0 + ch) * 64u + 8u * (tid & 7u)) * 2u, 0, 16);
  s.dw = *(const unsigned*)(P.ssmd + s.c0);
}
DI void ssm_build(SsmS& s, char* lds, const unsigned wid) {
  const unsigned tid = wid * 64u + lane_id();
#pragma unroll
  for (int ch = 0; ch < 2; ++ch) {
    const unsigned w[6] = {s.hA[ch][0], s.hA[ch][1], s.hA[ch][2], s.hA[ch][3], s.hB[ch][0], s.hB[ch][1]};
    unsigned o[5];
#pragma unroll
    for (int k = 0; k < 5; ++k) o[k] = __builtin_amdgcn_alignbyte(w[k + 1], w[k], 2);
    LAS char* dst = (LAS char*)lds + SSM_LDS + ch * HCH + tid * 16u + 64u;
    *(LAS u32x4*)(dst) = (u32x4){o[0], o[1], o[2], o[3]};
    *(LAS u32x4*)(dst + HCOPY) = (u32x4){w[1], w[2], w[3], w[4]};
    *(LAS u32x4*)(dst + 2 * HCOPY) = (u32x4){o[1], o[2], o[3], o[4]};
    *(LAS u32x4*)(dst + 3 * HCOPY) = (u32x4){w[2], w[3], w[4], w[5]};
    if (tid < 4u) {
      const unsigned z0 = tid ? 0u : w[0], z1 = tid ? 0u : w[1], zo = tid ? 0u : o[0];
      LAS char* d2 = (LAS char*)lds + SSM_LDS + ch * HCH + (3u - tid) * 16u;
      *(LAS u32x4*)(d2) = (u32x4){0u, 0u, 0u, z0 << 16};
      *(LAS u32x4*)(d2 + HCOPY) = (u32x4){0u, 0u, 0u, z0};
      *(LAS u32x4*)(d2 + 2 * HCOPY) = (u32x4){0u, 0u, z0 << 16, zo};
      *(LAS u32x4*)(d2 + 3 * HCOPY) = (u32x4){0u, 0u, z0, z1};
    }
    LAS char* xb = (LAS char*)lds + XOFF + ch * XCH;
    if (tid < 160u) *(LAS u32x4*)(xb + tid * 16u) = (u32x4){0u, 0u, 0u, 0u};
  }
#pragma unroll
  for (int ch = 0; ch < 2; ++ch) *(LAS u32x4*)((LAS char*)lds + XOFF + ch * XCH + (32u + (tid >> 2)) * XROW + (tid & 3u) * 16u) = s.xw[ch];
}
template <class F> DI void ssm_conv(SsmS& s, char* lds, const unsigned wid, F&& f) {
  const unsigned lane = lane_id(), hi = lane >> 5, r = lane & 31u;
  const unsigned ch = wid >> 1, dh = wid & 1u;
  K3S c;
  c.ha = (lds_cptr)lds + SSM_LDS + ch * HCH + (r & 3u) * HCOPY + (24u - 16u * dh - 8u * hi + 4u * (r >> 2)) * 2u; PINV(c.ha);
  c.xa = (lds_cptr)lds + XOFF + ch * XCH + (r + 1u) * XROW + (2u * dh + hi) * 16u; PINV(c.xa);
  c.y0 = f32x16{}; c.y1 = f32x16{};
  k3_ssm_rd<0>(c); k3_ssm_rd<1>(c);
  sfor<32>([&](auto E) AI { k3_ssm_step<E>(c, f); });
  s.y0 = c.y0; s.y1 = c.y1;
}
DI void ssm_finish(SsmS& s, const Params& P, char* lds, const unsigned wid) {
  const unsigned lane = lane_id(), tid = wid * 64u + lane, hi = lane >> 5, r = lane & 31u;
  LAS char* ys = (LAS char*)lds + SSM_LDS + wid * 8192u;
#pragma unroll
  for (int k = 0; k < 4; ++k) {
    const unsigned o = r * 128u + (((2u * k + hi) ^ (r & 7u)) << 4);
    *(LAS f32x4*)(ys + o) = (f32x4){s.y0[4 * k], s.y0[4 * k + 1], s.y0[4 * k + 2], s.y0[4 * k + 3]};
    *(LAS f32x4*)(ys + 4096u + o) = (f32x4){s.y1[4 * k], s.y1[4 * k + 1], s.y1[4 * k + 2], s.y1[4 * k + 3]};
  }
  asm volatile("s_waitcnt lgkmcnt(0)\n\ts_barrier" ::: "memory");
  const unsigned row = tid >> 2, yo0 = row * 128u + (((2u * (tid & 3u)) ^ (row & 7u)) << 4), yo1 = row * 128u + (((2u * (tid & 3u) + 1u) ^ (row & 7u)) << 4);
  const srd_t sz = mk_srd(P.ws + WS_ZT, SEQ * DM * 2);
  sfor<2>([&](auto C) AI {
    const LAS char* yp = (const LAS char*)lds + SSM_LDS + C * 16384;
    const f32x4 a = *(const LAS f32x4*)(yp + yo0) + *(const LAS f32x4*)(yp + 8192 + yo0), bb = *(const LAS f32x4*)(yp + yo1) + *(const LAS f32x4*)(yp + 8192 + yo1);
    const float dd = bf2f(C ? s.dw >> 16 : s.dw & 0xffffu);
    u32x4 z;
#pragma unroll
    for (int i = 0; i < 4; ++i) {
      const f32x2 kv = up2(s.xw[C][3 - i]), y = rb2(i < 2 ? (f32x2){a[2 * i], a[2 * i + 1]} : (f32x2){bb[2 * i - 4], bb[2 * i - 3]}), kd = rb2((f32x2){kv[1] * dd, kv[0] * dd});
      z[i] = cvtpk(y[0] + kd[0], y[1] + kd[1]);
    }
    st16w<C * 128>(z, sz, (((tid >> 3) * DM + s.c0) * 64u + 8u * (tid & 7u)) * 2u, 0u);
  });
}
struct GateS { unsigned qw[2][8]; u32x4 z[2][2]; unsigned long long f; };
DI gu64* gate_flag(const Params& P, unsigned g0) {
  const unsigned lane = lane_id(), pr = 32u * ((g0 + (lane >> 5)) % 7u) + (lane & 31u);
  return (gu64*)(P.ws + WS_FL) + FL_S4 + (pr / 28u + 8u * (pr % 28u)) * 16u;
}
DI void gate_load1(GateS& s, const Params& P, unsigned g0, const unsigned wid) {
  const unsigned tid = wid * 64u + lane_id();
  s.f = __hip_atomic_load(gate_flag(P, g0), RLX_AGENT);
  sfor<16>([&](auto I) AI { const unsigned blk = g0 + (I >> 3); s.qw[I >> 3][I & 7] = *(const unsigned*)(P.bq + (size_t)(64u * (blk / 7u) + 8u * (tid >> 5) + (I & 7)) * DM + 64u * (blk % 7u) + 2u * (tid & 31u)); });
}
DI void gate_load2(GateS& s, const Params& P, unsigned g0, unsigned long long tag, const unsigned wid) {
  const unsigned tid = wid * 64u + lane_id();
  if (!__all(s.f == tag)) wait_flags(gate_flag(P, g0), tag);
  const srd_t sz = mk_srd(P.ws + WS_ZT, SEQ * DM * 2);
  sfor<4>([&](auto I) AI { const unsigned blk = g0 + (I >> 1);
    s.z[I >> 1][I & 1] = __builtin_amdgcn_raw_buffer_load_b128(sz, (((blk / 7u) * DM + 64u * (blk % 7u) + 2u * (tid & 31u) + (I & 1)) * 64u + 8u * (tid >> 5)) * 2u, 0, 16); });
}
DI void gate_store(GateS& s, const Params& P, unsigned g0, const unsigned wid) {
  const unsigned tid = wid * 64u + lane_id();
  sfor<16>([&](auto I) AI { constexpr int u = I >> 3, i = I & 7; const unsigned blk = g0 + u;
    const f32x2 q = up2(s.qw[u][i]), a = up2(s.z[u][0][i >> 1]), bb = up2(s.z[u][1][i >> 1]);
    *(unsigned*)(P.out + (size_t)(64u * (blk / 7u) + 8u * (tid >> 5) + i) * OUTW + DM + 64u * (blk % 7u) + 2u * (tid & 31u)) = cvtpk(q[0] * a[i & 1], q[1] * bb[i & 1]); });
}

__global__ __launch_bounds__(256, 1) void k1_fused(Params P) {
  extern __shared__ __attribute__((aligned(16))) char lds[];
  const unsigned wid = rfl(threadIdx.x >> 6);
  const unsigned b = blockIdx.x, xcd = b & 7u, idx = b >> 3, h = 2u * xcd + (idx >> 4), j = idx & 15u;
  srd_t scode;
  { unsigned long long pc; unsigned len;
    asm volatile("s_getpc_b64 %0\n.Lcode_begin:\n\ts_mov_b32 %1, .Lcode_end-.Lcode_begin" : "=s"(pc), "=s"(len));
    scode = mk_srd((const void*)pc, len); }
  if (wid == 3) touch_lds((unsigned)(size_t)lds, scode, code_off());
  gu64* flags = (gu64*)(P.ws + WS_FL);
  const unsigned qb = (j & 1u) ? (j >> 1) : 15u - (j >> 1);
  const bool ssm = b < 224u;
  u32x4 qo[16]; SsmS s;
  const unsigned long long tag = phase_proj(P, lds, wid, qb, qo, s, scode);
  if (ssm) ssm_load(s, P, b, tag, wid);
  SBAR();
  {
    const unsigned lane = lane_id(), vl = lane * 16u;
    const lds_cptr rp = (lds_cptr)lds + WO_LDS + wid * 4096u + (lane >> 2) * 64u + 16u * (lane & 3u);
    bf16x8 wf[4];
    sfor<4>([&](auto J) AI { wf[J] = cat(lds_tr(rp + (J >> 1) * 2048 + (J & 1) * 8), lds_tr(rp + (J >> 1) * 2048 + (J & 1) * 8 + 1024)); });
    const srd_t sw_ = mk_srd(P.ws + WS_W + (size_t)h * (DH * DM * 2), DH * DM * 2);
    const srd_t sq = mk_srd(P.ws + ((size_t)(h * 8u + (j >> 1)) * 4u + wid) * 16384u, (j & 1u) ? 0u : 16384u);
    auto deferred = [&](auto E) AI {
      constexpr int k = E & 15;
      if constexpr (E < 16) st16w<k * 1024>(qo[k], sq, vl, 0u);
      else if constexpr (k < 12) { if constexpr (k < 4 * LPG) { constexpr int u = q_lunit(k), i = u >> 2, jj = u & 3; st16wa<(i & 1) * 1024>(s.late[k], s.sO, vl, s.so + (i >> 1) * s.sA + jj * s.sB); } }
      else st16w<((k - 12) >> 1) * 65536u + ((k - 12) & 1) * 1024u>(__builtin_bit_cast(u32x4, wf[k - 12]), sw_, vl, ((7u * (j & 1u) + wid) * 16u + (j & 14u)) * 1024u);
    };
    if (ssm) {
      ssm_build(s, lds, wid);
      asm volatile("s_waitcnt lgkmcnt(0)\n\ts_barrier" ::: "memory");
      ssm_conv(s, lds, wid, deferred);
    } else sfor<32>(deferred);
  }
  publish(flags + FL_S1 + b * 16u, tag, wid);
  PINV(s.y0); PINV(s.y1);
  phase_attn(P, lds, tag, wid, qo, scode, [&]() AI { if (ssm) ssm_finish(s, P, lds, wid); });
  if (!(j & 1u) && j >= 2u) {
    const unsigned g0 = 2u * (7u * h + (j >> 1) - 1u);
    GateS gs;
    gate_load1(gs, P, g0, wid);
    publish(flags + FL_S3 + b * 16u, tag, wid);
    gate_load2(gs, P, g0, tag, wid);
    gate_store(gs, P, g0, wid);
  } else publish(flags + FL_S3 + b * 16u, tag, wid);
  const unsigned lane = lane_id();
  const unsigned task = qb * 112u + h * 7u + wid;
  const srd_t sPa = mk_srd(P.ws + WS_PF + (size_t)task * 1024u, NH * 64u * 28672u - task * 1024u), sPb = mk_srd(P.ws + WS_PF + (size_t)(task + 4u) * 1024u, wid < 3u ? NH * 64u * 28672u - (task + 4u) * 1024u : 0u);
  gu64* fl = flags + FL_S3 + (((lane & 15u) >> 1) + 8u * (16u * (lane & 1u) + j)) * 16u;
  const unsigned one = (lane >> 4) == ((lane & 15u) >> 2) ? 0x3f80u << (16u * (lane & 1u)) : 0u, w0 = (lane & 2u) ? 0u : one, w1 = (lane & 2u) ? one : 0u;
  bf16x8 sel0 = __builtin_bit_cast(bf16x8, (u32x4){w0, w1, 0u, 0u}), sel1 = __builtin_bit_cast(bf16x8, (u32x4){0u, 0u, w0, w1}); PINV(sel0); PINV(sel1);
  ACC_CLAIM();
  unsigned done = 0u;
  for (int spin = 0; spin < (1 << 20) && done != 0xffffu; ++spin) {
    const unsigned go = (unsigned)__ballot(__hip_atomic_load(fl, RLX_AGENT) == tag) & 0xffffu & ~done;
    sfor<16>([&](auto H) AI { if ((go >> H) & 1u) { acc_load4<4 * H, H * (64u * 28672u)>(sPa, lane * 16u); acc_load4<64 + 4 * H, H * (64u * 28672u)>(sPb, lane * 16u); } });
    done |= go;
    if (!go) __builtin_amdgcn_s_sleep(1);
  }
  if (b == 0 && wid == 0) flag_store(flags + FL_EPOCH, tag);
  asm volatile("s_waitcnt vmcnt(0)" ::: "memory");
  reduce_sum<0>(task, lane, sel0, sel1, P.out);
  if (wid < 3u) reduce_sum<64>(task + 4u, lane, sel0, sel1, P.out);
  asm volatile(".Lcode_end:");
}

extern "C" void kernel_launch(void* const* d_in, const int* in_sizes, int n_in, void* d_out, int out_size, void* d_ws, size_t ws_size, hipStream_t stream) {
  typedef const unsigned short* cu16;
  Params P = {(cu16)d_in[0], (cu16)d_in[1], (cu16)d_in[2], (cu16)d_in[3], (cu16)d_in[4], (cu16)d_in[5], (cu16)d_in[6], (cu16)d_in[7], (cu16)d_in[8], (cu16)d_in[9], (cu16)d_in[10], (cu16)d_in[11],
              (unsigned short*)d_out, (char*)d_ws};
  (void)hipFuncSetAttribute((const void*)k1_fused, hipFuncAttributeMaxDynamicSharedMemorySize, 163840);
  k1_fused<<<256, 256, 163840, stream>>>(P);
}
```

```cpp
#include <hip/hip_runtime.h>
#include <hip/hip_bf16.h>
#include <utility>

typedef short    bf16x8 __attribute__((ext_vector_type(8)));
typedef short    s16x4  __attribute__((ext_vector_type(4)));
typedef float    f32x16 __attribute__((ext_vector_type(16)));
typedef float    f32x4  __attribute__((ext_vector_type(4)));
typedef float    f32x2  __attribute__((ext_vector_type(2)));
typedef __bf16   bf16x2 __attribute__((ext_vector_type(2)));
typedef unsigned u32x4  __attribute__((ext_vector_type(4)));
typedef unsigned u32x2  __attribute__((ext_vector_type(2)));
#define DI __device__ __forceinline__
#define AI __attribute__((always_inline))
#define LAS __attribute__((address_space(3)))
#define SBAR() __builtin_amdgcn_sched_barrier(0)
template <class T> DI void PINV(T& x) { asm volatile("" : "+v"(x)); }
template <class T> DI void PINA(T& x) { asm volatile("" : "+a"(x)); }
#define MFMA32(a, b, c) __builtin_amdgcn_mfma_f32_32x32x16_bf16((a), (b), (c), 0, 0, 0)
#define MFMA_DRAIN() asm volatile("s_nop 7\n\ts_nop 7" ::: "memory")
typedef LAS const char* lds_cptr;
typedef __amdgpu_buffer_rsrc_t srd_t;
template <int... Is> using iseq = std::integer_sequence<int, Is...>;
template <int N> using mkseq = std::make_integer_sequence<int, N>;

DI unsigned rfl(unsigned x) { return __builtin_amdgcn_readfirstlane(x); }
DI unsigned lane_id() { unsigned l; asm volatile("v_mbcnt_lo_u32_b32 %0, -1, 0\n\tv_mbcnt_hi_u32_b32 %0, -1, %0" : "=v"(l)); return l; }
DI unsigned cvtpk(float lo, float hi) { f32x2 v = {lo, hi}; bf16x2 b = __builtin_convertvector(v, bf16x2); return __builtin_bit_cast(unsigned, b); }
DI float bf2f(unsigned h) { return __uint_as_float(h << 16); }
DI float rbf(float x) { return __uint_as_float(cvtpk(x, x) << 16); }
DI bf16x8 lds_rd128(lds_cptr p) { return *(const LAS bf16x8*)p; }
DI s16x4 lds_tr(lds_cptr p) { return __builtin_amdgcn_ds_read_tr16_b64_v4i16((LAS s16x4*)p); }
DI bf16x8 cat(s16x4 l, s16x4 h) { return __builtin_shufflevector(l, h, 0, 1, 2, 3, 4, 5, 6, 7); }
DI srd_t mk_srd(const void* p, unsigned bytes) { return __builtin_amdgcn_make_buffer_rsrc((void*)p, (short)0, (int)bytes, 0x00020000); }
template <unsigned MI, unsigned SI> DI void dma16(unsigned mb, unsigned voff, srd_t srd, unsigned sb) {
  unsigned t;
  asm volatile("s_add_u32 m0, %1, %5\n\ts_add_u32 %0, %4, %6\n\tbuffer_load_dwordx4 %2, %3, %0 offen lds" : "=&s"(t) : "s"(mb), "v"(voff), "s"(srd), "s"(sb), "n"(MI), "n"(SI) : "memory", "scc");
}
template <unsigned MI, unsigned SI> DI void dma_prep(unsigned& t, unsigned mb, unsigned sb) { asm volatile("s_add_u32 m0, %1, %3\n\ts_add_u32 %0, %2, %4" : "=&s"(t) : "s"(mb), "s"(sb), "n"(MI), "n"(SI) : "scc"); }
DI void dma_go(unsigned t, unsigned voff, srd_t srd) { asm volatile("buffer_load_dwordx4 %0, %1, %2 offen lds" :: "v"(voff), "s"(srd), "s"(t) : "memory"); }
template <unsigned SI> DI void st16w(u32x4 v, srd_t srd, unsigned voff, unsigned sb) {
  unsigned t; asm volatile("s_add_u32 %0, %4, %5\n\tbuffer_store_dwordx4 %1, %2, %3, %0 offen sc1\n\ts_nop 1" : "=&s"(t) : "v"(v), "v"(voff), "s"(srd), "s"(sb), "n"(SI) : "memory", "scc"); }
template <unsigned SI> DI void st16wa(f32x4 v, srd_t srd, unsigned voff, unsigned sb) {
  unsigned t; asm volatile("s_add_u32 %0, %4, %5\n\tbuffer_store_dwordx4 %1, %2, %3, %0 offen sc1\n\ts_nop 1" : "=&s"(t) : "a"(v), "v"(voff), "s"(srd), "s"(sb), "n"(SI) : "memory", "scc"); }
DI void ld4(unsigned& d, srd_t srd, unsigned voff) { asm volatile("buffer_load_dword %0, %1, %2, 0 offen" : "=v"(d) : "v"(voff), "s"(srd) : "memory"); }
template <unsigned SI> DI void ld16a(bf16x8& d, srd_t srd, unsigned voff) { unsigned t; asm volatile("s_mov_b32 %1, %4\n\tbuffer_load_dwordx4 %0, %2, %3, %1 offen sc1" : "=a"(d), "=&s"(t) : "v"(voff), "s"(srd), "n"(SI) : "memory"); }
template <class F, int... Is> DI void sfor_(F&& f, iseq<Is...>) { (f(std::integral_constant<int, Is>{}), ...); }
template <int N, class F> DI void sfor(F&& f) { sfor_(f, mkseq<N>{}); }
DI u32x4 pack8(const f32x4& a, const f32x4& b, float sc) { u32x4 w = {cvtpk(a[0] * sc, a[1] * sc), cvtpk(a[2] * sc, a[3] * sc), cvtpk(b[0] * sc, b[1] * sc), cvtpk(b[2] * sc, b[3] * sc)}; return w; }
DI u32x4 pack8w(const f32x16& a, int s, float sc) {
  u32x4 w = {cvtpk(a[8 * s] * sc, a[8 * s + 1] * sc), cvtpk(a[8 * s + 2] * sc, a[8 * s + 3] * sc), cvtpk(a[8 * s + 4] * sc, a[8 * s + 5] * sc), cvtpk(a[8 * s + 6] * sc, a[8 * s + 7] * sc)};
  return w;
}
constexpr int SEQ = 2048, NH = 16, DH = 256, DM = 448, DC = 1024;
constexpr unsigned WRS = NH * DH * 2;
constexpr unsigned MB16 = SEQ * NH * DH * 2;
constexpr float QSCALE = 1.4426950408889634f / 16.0f;

DI unsigned code_off() { return ((blockIdx.x >> 3) * 64u + lane_id()) * 64u; }
typedef __attribute__((address_space(1))) unsigned long long gu64;
#define RLX_AGENT __ATOMIC_RELAXED, __HIP_MEMORY_SCOPE_AGENT
struct Params {
  const unsigned short *x, *ckv, *wq, *wuk, *wuv, *wo, *bq, *bk, *bv, *shk, *ssmk, *ssmd;
  unsigned short* out; char* ws;
};
struct Ctx { srd_t code; };
constexpr size_t WS_K = MB16, WS_W = 3 * (size_t)MB16, WS_XT = WS_W + (4u << 20), WS_ZT = WS_XT + (2u << 20), WS_FL = WS_ZT + (2u << 20), WS_PART = WS_FL + (1u << 20), WS_PF = WS_PART + (17u << 20);
constexpr int FL_EPOCH = 0, FL_S0 = 16, FL_S1 = FL_S0 + 256 * 16, FL_S2 = FL_S1 + 256 * 16, FL_S3 = FL_S2 + 128 * 16, FL_S4 = FL_S3 + 256 * 16;
DI void flag_store(gu64* flag, unsigned long long tag) {
  const u32x2 v = {(unsigned)tag, (unsigned)(tag >> 32)};
  asm volatile("s_nop 4\n\tbuffer_store_dwordx2 %0, %1, %2, 0 offen sc1" :: "v"(v), "v"(lane_id() * 16u), "s"(mk_srd((const void*)flag, 8u)) : "memory");
}
DI void publish(gu64* flag, unsigned long long tag, unsigned wid) {
  asm volatile("s_waitcnt vmcnt(0)\n\ts_barrier" ::: "memory");
  if (wid == 0) flag_store(flag, tag);
}
DI void wait_flags(gu64* lane_flag, unsigned long long tag) {
  for (int spin = 0; spin < (1 << 20); ++spin) {
    if (__all(__hip_atomic_load(lane_flag, RLX_AGENT) == tag)) break;
    __builtin_amdgcn_s_sleep(1);
  }
}

DI void mfma16a(f32x4& c, const bf16x8& a, const bf16x8& b) { asm volatile("v_mfma_f32_16x16x32_bf16 %0, %1, %2, %0" : "+a"(c) : "v"(a), "v"(b)); }
DI void mfma16az(f32x4& c, const bf16x8& a, const bf16x8& b) { asm volatile("v_mfma_f32_16x16x32_bf16 %0, %1, %2, 0" : "=a"(c) : "v"(a), "v"(b)); }
DI void mfma16v(f32x4& c, const bf16x8& a, const bf16x8& b) { asm volatile("v_mfma_f32_16x16x32_bf16 %0, %1, %2, %0" : "+v"(c) : "v"(a), "v"(b)); }
DI void mfma16z(f32x4& c, const bf16x8& a, const bf16x8& b) { asm volatile("v_mfma_f32_16x16x32_bf16 %0, %1, %2, 0" : "=&v"(c) : "v"(a), "v"(b)); }
DI f32x2 up2(unsigned w) { f32x2 r = {__uint_as_float(w << 16), __uint_as_float(w & 0xffff0000u)}; return r; }
DI f32x2 rb2(f32x2 v) { return up2(cvtpk(v[0], v[1])); }
constexpr unsigned qxs(int s) { return 16384u * s; }
constexpr unsigned qws(int s) { return s == 0 ? 65536u : s == 1 ? 131072u : 98304u; }
constexpr unsigned WO_LDS = 49152u;
struct SsmS { u32x4 xw[2], hA[2]; u32x2 hB[2], f0; unsigned dw, c0; f32x16 y0, y1; f32x4 late[12]; srd_t sO; unsigned so, sA, sB; };
struct Frags { bf16x8 x[8]; s16x4 wl[8], wh[8]; };
struct K1C {
  unsigned ldsb, lds8, lds4, wid, vlane, b, hb;
  unsigned vx_kv[2], vx_q[2], vw[4];
  srd_t sX, sW, sXq, sWq, sO;
  unsigned sx_kv, sw_kv, sx_q, sw_q, so;
  lds_cptr xr_kv[2], wr[8], xq[2], wq[8];
  const char* pf; unsigned pfstep, xoff, dt, tabl, tabh, tabn; int it; srd_t sx2;
  f32x4 acc[8][8]; Frags f0, f1; f32x4 ta, tb; u32x4 pk;
  f32x4 qa[16][2]; s16x4 ql[16], qh[16]; bf16x8 qx[2][2];
  unsigned kw[11], vv[8], kv[8]; u32x4 sk; u32x2 ep; f32x2 kf[11], sc[4], pa, pt;
  unsigned long long tag; SsmS* ss; srd_t scode;
};
template <int L> DI void wlane(unsigned& v, unsigned s) { asm volatile("v_writelane_b32 %0, %1, %2" : "+v"(v) : "s"(s), "n"(L)); }
DI void touch_lds(unsigned ldsb, srd_t srd, unsigned voff) {
  asm volatile("s_add_u32 m0, %0, 147456\n\ts_nop 0\n\tbuffer_load_dword %1, %2, 0 offen lds" :: "s"(ldsb), "v"(voff), "s"(srd) : "memory", "scc"); }
DI void k1_touch(const K1C& c) {
  asm volatile("s_add_u32 m0, %0, 147456\n\ts_nop 0\n\tglobal_load_lds_dword %1, off" :: "s"(c.ldsb), "v"(c.pf) : "memory", "scc"); }
template <int buf, int j> DI void k1_piece(const K1C& c, unsigned sw, unsigned sx) {
  constexpr int p = j >> 1;
  if constexpr (!(j & 1)) dma16<buf * 32768u + 65536u + p * 1024u, 2u * p * WRS>(c.lds8, c.vw[(p & 1) + 2 * (p >> 2)], c.sW, sw);
  else dma16<buf * 32768u + p * 1024u, 8u * p * (DC * 2)>(c.lds8, c.vx_kv[(p >> 1) & 1], c.sX, sx);
}
template <int buf, int j> DI void k1_prep(K1C& c, unsigned sw, unsigned sx) {
  constexpr int p = j >> 1;
  if constexpr (!(j & 1)) dma_prep<buf * 32768u + 65536u + p * 1024u, 2u * p * WRS>(c.dt, c.lds8, sw); else dma_prep<buf * 32768u + p * 1024u, 8u * p * (DC * 2)>(c.dt, c.lds8, sx);
}
template <int j> DI void k1_go(const K1C& c) { constexpr int p = j >> 1; if constexpr (!(j & 1)) dma_go(c.dt, c.vw[(p & 1) + 2 * (p >> 2)], c.sW); else dma_go(c.dt, c.vx_kv[(p >> 1) & 1], c.sX); }
template <int S, int j> DI void k1_qpiece(const K1C& c) {
  if constexpr (j % 3 != 1) { constexpr int p = 2 * (j / 3) + (j % 3 == 2); dma16<qws(S % 3) + p * 1024u, (64u * S + 2u * p) * WRS>(c.lds8, c.vw[(p & 1) + 2 * (p >> 2)], c.sWq, c.sw_q); }
  else { constexpr int p = j / 3; dma16<qxs(S % 3) + p * 1024u, 8u * p * (DM * 2) + 128u * S>(c.lds4, c.vx_q[p & 1], c.sXq, c.sx_q); }
}
template <int buf> DI void k1_stage(const K1C& c, int st) { sfor<16>([&](auto J) AI { k1_piece<buf, J>(c, c.sw_kv + (unsigned)st * (64u * WRS), c.sx_kv + (unsigned)st * 128u); }); }
template <int buf, int ks, int i> DI void k1_read(const K1C& c, Frags& f) {
  constexpr int so = buf * 32768;
  if constexpr (i < 2 || i >= 10) { constexpr int k = i < 2 ? i : i - 8, nb = k >> 1, t = k & 1;
    const s16x4 v = lds_tr(c.wr[nb] + so + ks * 16384 + t * 2048); if constexpr (t) f.wh[nb] = v; else f.wl[nb] = v; }
  else { constexpr int mb = i - 2; f.x[mb] = lds_rd128(c.xr_kv[ks] + so + (mb >> 1) * 4096 + (mb & 1) * 1024); }
}
template <int F> DI void k1_qw(K1C& c) {
  constexpr int T = F >> 5, ks = (F >> 4) & 1, nb = F & 15, sl = T % 3, o = (sl == 2 ? 32768 : 0) + ks * 16384 + (nb >> 3) * 256;
  if constexpr ((F & 31) < 8 && sl != 0) { c.wq[nb] += sl == 1 ? 65536 : -65536; PINV(c.wq[nb]); }
  c.ql[F & 15] = lds_tr(c.wq[nb & 7] + o); c.qh[F & 15] = lds_tr(c.wq[nb & 7] + o + 2048);
}
template <int T, int ks> DI void k1_qx(K1C& c) { c.qx[ks][0] = lds_rd128(c.xq[ks] + qxs(T % 3)); c.qx[ks][1] = lds_rd128(c.xq[ks] + qxs(T % 3) + 2048); }
template <int m> DI void k1_pre(K1C& c, const Params& P) {
  if constexpr (m == 0) {
    asm volatile("" : "+v"(c.kw[0]), "+v"(c.kw[1]), "+v"(c.kw[2]), "+v"(c.kw[3]), "+v"(c.kw[4]), "+v"(c.kw[5]), "+v"(c.kw[6]), "+v"(c.kw[7]), "+v"(c.kw[8]), "+v"(c.kw[9]), "+v"(c.kw[10]),
                 "+v"(c.vv[0]), "+v"(c.vv[1]), "+v"(c.vv[2]), "+v"(c.vv[3]), "+v"(c.vv[4]), "+v"(c.vv[5]), "+v"(c.vv[6]), "+v"(c.vv[7]), "+v"(c.sk), "+v"(c.ep));
    const unsigned long long e0 = ((unsigned long long)rfl(c.ep[1]) << 32) | rfl(c.ep[0]);
    c.tag = (e0 ^ 0x9E3779B97F4A7C15ull) * 0xD1342543DE82EF95ull + 1ull;
  } else if constexpr (m < 12) { c.kf[m - 1] = up2(c.kw[m - 1]); PINV(c.kf[m - 1]); }
  else if constexpr (m < 14) { constexpr int i = m - 12;
    c.sc[2 * i] = (f32x2){bf2f(c.sk[i] & 0xffffu), bf2f(c.sk[2 + i] & 0xffffu)}; c.sc[2 * i + 1] = (f32x2){bf2f(c.sk[i] >> 16), bf2f(c.sk[2 + i] >> 16)}; PINV(c.sc[2 * i]); PINV(c.sc[2 * i + 1]); }
  else if constexpr (m < 78) { constexpr int i = (m - 14) >> 3, s = (m - 14) & 7;
    if constexpr (s == 0) { c.pa = rb2(c.kf[i] * c.sc[3]); PINV(c.pa); }
    else if constexpr (s == 7) { const f32x2 a = c.pa * up2(c.vv[i]); c.kv[i] = cvtpk(a[0], a[1]); PINV(c.kv[i]); }
    else if constexpr (s & 1) { c.pt = rb2(c.kf[i + (s + 1) / 2] * c.sc[3 - (s + 1) / 2]); PINV(c.pt); }
    else { c.pa = rb2(c.pa + c.pt); PINV(c.pa); }
  } else {
    constexpr unsigned sel = m == 78 ? 0x05040100u : 0x07060302u;
    const unsigned tid = c.wid * 64u + (c.vlane >> 4), xo = (((c.b / 7u) * DM + 64u * (c.b % 7u) + 2u * (tid & 31u)) * 64u + 8u * (tid >> 5)) * 2u;
    st16w<(m - 78) * 128>((u32x4){__builtin_amdgcn_perm(c.kv[6], c.kv[7], sel), __builtin_amdgcn_perm(c.kv[4], c.kv[5], sel), __builtin_amdgcn_perm(c.kv[2], c.kv[3], sel), __builtin_amdgcn_perm(c.kv[0], c.kv[1], sel)},
                          mk_srd(P.ws + WS_XT, SEQ * DM * 2), xo, 0u);
  }
}
template <int i> DI void k1_preload(K1C& c, const Params& P) {
  const unsigned tid = c.wid * 64u + (c.vlane >> 4), bc0 = 64u * (c.b % 7u) + 2u * (tid & 31u), vo = ((64u * (c.b / 7u) + 8u * (tid >> 5)) * DM + bc0) * 2u;
  if constexpr (i < 11) ld4(c.kw[i], mk_srd(P.bk, SEQ * DM * 2), vo + (unsigned)((i - 3) * DM * 2));
  else if constexpr (i < 19) ld4(c.vv[i - 11], mk_srd(P.bv, SEQ * DM * 2), vo + (unsigned)((i - 11) * DM * 2));
  else if constexpr (i == 19) asm volatile("buffer_load_dwordx4 %0, %1, %2, 0 offen" : "=v"(c.sk) : "v"(bc0 * 8u), "s"(mk_srd(P.shk, DM * 8)) : "memory");
  else asm volatile("buffer_load_dwordx2 %0, off, %1, 0 sc1" : "=v"(c.ep) : "s"(mk_srd(P.ws + WS_FL, 64)) : "memory");
}
template <bool WA, int STEP, int buf, int G> DI void k1_gap(K1C& c, const Params& P, unsigned sw, unsigned sx) {
  constexpr int ks = G >> 6, g = G & 63, nb = g >> 3, mb = g & 7;
  Frags& fc = ks ? c.f1 : c.f0; Frags& fn = ks ? c.f0 : c.f1;
  if constexpr (G == 64) {
    asm volatile("s_waitcnt vmcnt(%0) lgkmcnt(0)\n\ts_barrier" :: "n"(STEP == 0 ? 3 : STEP == 14 ? 0 : STEP == 15 ? 12 : buf == 1 ? 2 : 1) : "memory");
    SBAR();
  }
  const bf16x8 w = cat(fc.wl[nb], fc.wh[nb]);
  if constexpr (G == 0) asm volatile("s_nop 1" :: "v"(w), "v"(fc.x[0]));
  if constexpr (STEP == 0 && ks == 0) { if constexpr (WA) mfma16az(c.acc[mb][nb], w, fc.x[mb]); else mfma16az(c.acc[nb][mb], fc.x[mb], w); }
  else if constexpr (WA) mfma16a(c.acc[mb][nb], w, fc.x[mb]); else mfma16a(c.acc[nb][mb], fc.x[mb], w);
  SBAR();
  if constexpr (!(g & 1) && g < 48) {
    if constexpr (ks == 0) k1_read<buf, 1, g / 2>(c, fn);
    else if constexpr (STEP != 15) k1_read<buf ^ 1, 0, g / 2>(c, fn);
  }
  if constexpr (STEP == 15 && ks == 1 && g >= 36 && !(g & 3)) { if constexpr (g == 36) k1_qx<0, 0>(c); else k1_qw<(g - 40) / 4>(c); }
  if constexpr (STEP == 0 && ks == 0 && (g & 1) && g < 16) k1_piece<1, 8 + (g >> 1)>(c, c.sw_kv + 64u * WRS, c.sx_kv + 128u);
  if constexpr (STEP == 0 && (G == 17 || G == 19 || G == 21)) { k1_touch(c); c.pf += c.pfstep; }
  if constexpr (STEP == 1 && G >= 61 && G < 124 && (G == 61 || (G > 64 && (G & 3) == 2 + ((G >> 2) & 1) * 0 + 1))) k1_prep<buf, (G == 61 ? 0 : (G - 63) >> 2)>(c, sw, sx);
  if constexpr (STEP == 1 && buf == 0 && G == 51) c.sx2 = mk_srd((const void*)(((unsigned long long)__builtin_amdgcn_readlane(c.tabh, c.it) << 32) | (unsigned)__builtin_amdgcn_readlane(c.tabl, c.it)), __builtin_amdgcn_readlane(c.tabn, c.it));
  if constexpr (STEP == 13 && G == 64) { if (c.wid == 3) touch_lds(c.ldsb, c.scode, code_off()); }
  if constexpr (ks == 1) {
    if constexpr (STEP != 1 && STEP <= 13 && (g & 3) == 1) k1_piece<buf, (g >> 2)>(c, sw, sx);
    if constexpr (STEP == 1 && (g & 3) == 1) k1_go<(g >> 2)>(c);
    if constexpr (STEP == 0 && (g & 3) == 3) k1_preload<(g >> 2)>(c, P);
    if constexpr (STEP == 0 && (g & 3) == 2 && g >= 44) k1_preload<16 + ((g - 44) >> 2)>(c, P);
    if constexpr (STEP == 14 && (g & 1) && g < 60 && (g >> 1) % 5 != 4) { constexpr int j = (g >> 1) - (g >> 1) / 5; k1_qpiece<j / 12, j % 12>(c); }
    if constexpr (STEP == 15 && (g & 3) == 1 && g < 48) k1_qpiece<2, (g >> 2)>(c);
  }
  if constexpr (STEP <= 13 && G == 127) { k1_touch(c); c.pf += c.pfstep; if constexpr (buf == 0) touch_lds(c.ldsb, c.sx2, c.xoff); }
  if constexpr (STEP >= 13) { constexpr int X = (STEP - 13) * 128 + G; if constexpr (X >= 56 && X % 4 == 3 && (X - 56) / 4 < 80) k1_pre<(X - 56) / 4>(c, P); }
  SBAR();
}
template <bool WA, int STEP, int buf> DI void k1_step(K1C& c, const Params& P, int sb) {
  const unsigned sw = c.sw_kv + (unsigned)sb * (64u * WRS), sx = c.sx_kv + (unsigned)sb * 128u;
  sfor<128>([&](auto G) AI { k1_gap<WA, STEP, buf, G>(c, P, sw, sx); });
}
#define LPG 3
constexpr int q_lpos(int k) { return k == 0 ? 7 : k == 1 ? 3 : 5; }
constexpr bool q_late(int u) { for (int k = 0; k < LPG; ++k) if (u % 8 == q_lpos(k)) return true; return false; }
constexpr int q_lidx(int u) { for (int k = 0; k < LPG; ++k) if (u % 8 == q_lpos(k)) return LPG * (u / 8) + k; return 0; }
constexpr int q_lunit(int k) { return 8 * (k / (LPG ? LPG : 1)) + q_lpos(k % (LPG ? LPG : 1)); }
constexpr int q_stores(int lo, int hi) { int n = 0; for (int u = 0; u < 32; ++u) n += (!q_late(u) && 6 * u + 5 > lo && 6 * u + 5 < hi); return n; }
constexpr int q_wait(int T) {
  return T == 0 ? 14 + q_stores(-1, 26) : T == 1 ? 12 + q_stores(-1, 58) : (T + 2 <= 6 ? 12 : 0) + q_stores(32 * (T - 2) + 48, 32 * T + 26); }
template <bool WA, int X> DI void k1_qgap(K1C& c, const Params& P) {
  constexpr int T = X >> 5, g = X & 31, ks = g >> 4, nb = g & 15;
  if constexpr (g == 26 && T < 6) {
    asm volatile("s_waitcnt vmcnt(%0) lgkmcnt(0)\n\ts_barrier" :: "n"(q_wait(T)) : "memory"); SBAR();
    if constexpr (T == 1) { if (c.wid == 0) flag_store((gu64*)(P.ws + WS_FL) + FL_S0 + c.b * 16u, c.tag); SBAR(); }
  }
  const bf16x8 w = cat(c.ql[X & 15], c.qh[X & 15]);
  if constexpr (X < 16) mfma16z(c.qa[nb][0], w, c.qx[ks][0]); else mfma16v(c.qa[nb][0], w, c.qx[ks][0]);
  SBAR();
  if constexpr (g < 22) { constexpr int f = 6 + g + (g + 4) / 5 - (g > 20 ? 1 : 0); k1_qw<32 * T + f>(c); if constexpr (g % 5 == 0 && g < 20) k1_qw<32 * T + f + 1>(c); }
  if constexpr (g >= 26 && T < 6) k1_qw<32 * (T + 1) + g - 26>(c);
  SBAR();
  if constexpr (X < 16) mfma16z(c.qa[nb][1], w, c.qx[ks][1]); else mfma16v(c.qa[nb][1], w, c.qx[ks][1]);
  SBAR();
  if constexpr (g == 7) k1_qx<T, 1>(c);
  if constexpr (g == 27 && T < 6) k1_qx<T + 1, 0>(c);
  if constexpr (X >= 26 && !(X & 1) && (X - 26) % 32 < 24 && (X - 26) / 32 + 3 <= 6) k1_qpiece<(X - 26) / 32 + 3, ((X - 26) % 32) / 2>(c);
  constexpr int u = X / 6, ph = X % 6;
  if constexpr (u < 32) {
    constexpr int i = u >> 2, j = u & 3;
    if constexpr (ph == 1) { c.ta = c.acc[i][2 * j]; PINV(c.ta); }
    if constexpr (ph == 2) { c.tb = c.acc[i][2 * j + 1]; PINV(c.tb); }
    if constexpr (ph == 3) { c.pk[0] = cvtpk(c.ta[0], c.ta[1]); c.pk[1] = cvtpk(c.ta[2], c.ta[3]); PINV(c.pk); }
    if constexpr (ph == 4) { c.pk[2] = cvtpk(c.tb[0], c.tb[1]); c.pk[3] = cvtpk(c.tb[2], c.tb[3]); PINV(c.pk); }
    if constexpr (ph == 5 && q_late(u)) { c.ss->late[q_lidx(u)] = __builtin_bit_cast(f32x4, c.pk); PINA(c.ss->late[q_lidx(u)]); }
    if constexpr (ph == 5 && !q_late(u)) st16w<(WA ? (i >> 1) * 16384u + j * 2048u : (i >> 1) * 2048u + j * 16384u) + (i & 1) * 1024u>(c.pk, c.sO, c.vlane, c.so);
  }
  if constexpr (X >= 200 && X < 205) {
    const unsigned tid = c.wid * 64u + (c.vlane >> 4), c0 = 2u * (28u * (c.b & 7u) + (c.b >> 3)); constexpr int ch = (X - 200) >> 1;
    if constexpr (X == 204) asm volatile("buffer_load_dwordx2 %0, %1, %2, 0 offen sc1" : "=v"(c.ss->f0) : "v"((FL_S0 + (7u * (tid & 31u) + (c0 >> 6)) * 16u) * 8u), "s"(mk_srd(P.ws + WS_FL, 1u << 20)) : "memory");
    else if constexpr (X & 1) asm volatile("buffer_load_dwordx2 %0, %1, %2, 0 offen offset:16" : "=v"(c.ss->hB[ch]) : "v"(tid * 16u), "s"(mk_srd(P.ssmk + (size_t)(c0 + ch) * SEQ, c.b < 224u ? SEQ * 2 : 0)) : "memory");
    else asm volatile("buffer_load_dwordx4 %0, %1, %2, 0 offen" : "=v"(c.ss->hA[ch]) : "v"(tid * 16u), "s"(mk_srd(P.ssmk + (size_t)(c0 + ch) * SEQ, c.b < 224u ? SEQ * 2 : 0)) : "memory");
  }
  if constexpr (X >= 196 && X < 200) {
    constexpr int j = X - 196; const unsigned ln = c.vlane >> 4;
    dma16<WO_LDS + j * 1024u, (j >> 1) * 256u + (j & 1) * (16u * DM * 2)>(c.lds4, (ln >> 2) * (DM * 2) + (ln & 3u) * 16u, mk_srd(P.wo, NH * DH * DM * 2), (c.hb >> 1) * (32u * DM * 2) + (7u * (c.hb & 1u) + c.wid) * 64u);
  }
  SBAR();
}

template <bool WA> DI void k1_main(K1C& c, const Params& P, const char* pf2, u32x4 (&qo)[16]) {
  asm volatile("s_waitcnt vmcnt(8)\n\ts_barrier" ::: "memory");
  sfor<24>([&](auto I) AI { k1_read<0, 0, I>(c, c.f0); });
  SBAR();
  c.sx2 = mk_srd(P.wq, 0u);
  k1_step<WA, 0, 0>(c, P, 2);
#pragma nounroll
  for (int t = 1; t < 13; t += 2) {
    k1_step<WA, 1, 1>(c, P, t + 2);
    c.it = t;
    c.pf = t == 9 ? pf2 : c.pf;
    k1_step<WA, 1, 0>(c, P, t + 3);
  }
  k1_step<WA, 13, 1>(c, P, 15);
  {
    const unsigned lane = lane_id(), rho = lane & 15u, g = lane >> 4, q = rho >> 2, p = lane & 3u, f = q | ((g & 1u) << 2);
    sfor<2>([&](auto I) AI { c.vx_q[I] = (lane >> 3) * (DM * 2) + 16u * ((lane & 7u) ^ (4u * I + (lane >> 4)));
      c.xq[I] = (lds_cptr)(size_t)(c.ldsb + (32u * c.wid + rho) * 128u + 16u * ((4u * I + g) ^ (rho >> 1))); PINV(c.xq[I]); });
    sfor<8>([&](auto I) AI { c.wq[I] = (lds_cptr)(size_t)(c.ldsb + 65536u + (8u * g + q) * 512u + ((unsigned)I ^ f) * 32u + 8u * p); PINV(c.wq[I]); });
  }
  k1_step<WA, 14, 0>(c, P, 0);
  k1_step<WA, 15, 1>(c, P, 0);
  sfor<224>([&](auto X) AI { k1_qgap<WA, X>(c, P); });
  MFMA_DRAIN(); SBAR();
  sfor<16>([&](auto I) AI { constexpr int db = I >> 1, q = I & 1; PINV(c.qa[2 * db][q]); PINV(c.qa[2 * db + 1][q]); qo[I] = pack8(c.qa[2 * db][q], c.qa[2 * db + 1][q], QSCALE); PINV(qo[I]); SBAR(); });
  asm volatile("s_waitcnt vmcnt(0)" : "+v"(c.ss->hA[0]), "+v"(c.ss->hA[1]), "+v"(c.ss->hB[0]), "+v"(c.ss->hB[1]), "+v"(c.ss->f0) :: "memory");
}

DI unsigned long long phase_proj(const Params& P, char* lds, const unsigned wid, const unsigned qblk, u32x4 (&qo)[16], SsmS& ss, srd_t scode) {
  const unsigned lane = lane_id(), wr = wid >> 1, wc = wid & 1;
  const unsigned b = blockIdx.x, xcd = b & 7u, idx = b >> 3;
  const unsigned pm = idx & 7u, which = idx >> 3, h = 2u * xcd + (which >> 1), isV = which & 1u;
  K1C c; c.ss = &ss; c.scode = scode;
  c.ldsb = (unsigned)(size_t)lds; c.lds8 = c.ldsb + wid * 8192u; c.lds4 = c.ldsb + wid * 4096u; c.wid = wid; c.vlane = lane * 16u; c.b = b; c.hb = 16u * h + (idx & 15u);
  const unsigned short* wkv = isV ? P.wuv : P.wuk;
  c.sX = mk_srd(P.ckv, SEQ * DC * 2); c.sW = mk_srd(wkv, DC * WRS); c.sXq = mk_srd(P.x, SEQ * DM * 2); c.sWq = mk_srd(P.wq, DM * WRS);
  sfor<2>([&](auto I) AI { c.vx_kv[I] = (lane >> 3) * (DC * 2) + 16u * ((lane & 7u) ^ (4u * I + (lane >> 4))); });
  sfor<4>([&](auto I) AI { const unsigned f = (2u * (I & 1) + (lane >> 5)) | (4u * (I >> 1)); c.vw[I] = (lane >> 5) * WRS + 16u * ((lane & 31u) ^ (f << 1)); });
  c.sx_kv = (256u * pm + 64u * wid) * (DC * 2); c.sw_kv = 16u * wid * WRS + h * (DH * 2);
  c.sx_q = (128u * qblk + 32u * wid) * (DM * 2);  c.sw_q = c.sw_kv;
  k1_stage<0>(c, 0);
  sfor<8>([&](auto J) AI { k1_piece<1, J>(c, c.sw_kv + 64u * WRS, c.sx_kv + 128u); });
    const unsigned rho = lane & 15u, g = lane >> 4;
  sfor<2>([&](auto I) AI { c.xr_kv[I] = (lds_cptr)lds + (wr * 128u + 16u * (rho >> 3) + (rho & 7u)) * 128u + 16u * ((4u * I + g) ^ (rho >> 1)); PINV(c.xr_kv[I]); });
  { const unsigned q = (lane & 15u) >> 2, p = lane & 3u, f = q | ((g & 1u) << 2);
    sfor<8>([&](auto I) AI { c.wr[I] = (lds_cptr)lds + 65536u + (8u * g + q) * 512u + (8u * wc + ((unsigned)I ^ f)) * 32u + 8u * p; PINV(c.wr[I]); }); }
  c.sO = mk_srd(P.ws + WS_K + (isV ? MB16 : 0u) + (size_t)h * (SEQ * DH * 2), SEQ * DH * 2);
  c.so = (8u * pm + 4u * wr) * 16384u + wc * 8192u;
  const bool pw = lane >= 32u;
  const unsigned k = lane & 15u;
  unsigned pfa = (192u + 8u * pm + 2u * wid + ((lane >> 2) & 1u)) * WRS + h * (DH * 2) + (lane & 3u) * 128u, pfb = (256u * pm + 64u * which + 16u * wid + k) * (DC * 2) + 384u;
  PINV(pfa); PINV(pfb);
  c.pf = (const char*)(pw ? wkv : P.ckv) + (pw ? pfa : pfb);
  c.pfstep = pw ? 64u * WRS : 128u;
  c.tabl = 0u; c.tabh = 0u; c.tabn = 0u;
  sfor<5>([&](auto I) AI { const unsigned long long tp = (unsigned long long)(I == 0 ? P.wq : I == 1 ? P.x : I == 2 ? P.wo : I == 3 ? P.ssmk : P.bq);
    wlane<2 * I + 1>(c.tabl, (unsigned)tp); wlane<2 * I + 1>(c.tabh, (unsigned)(tp >> 32)); wlane<2 * I + 1>(c.tabn, I == 0 || I == 2 ? DM * WRS : SEQ * DM * 2u); });
  c.xoff = ((b * 4u + wid) * 28u + min(lane, 27u)) * 128u;
  const unsigned st0 = (lane & 16u) ? 0u : 3u;
  pfa = (64u * st0 + 4u * (idx & 15u) + wid) * WRS + h * (DH * 2) + (lane & 3u) * 128u; pfb = (128u * qblk + 64u * (idx >> 4) + 16u * wid + k) * (DM * 2) + 128u * st0;
  PINV(pfa); PINV(pfb);
  const char* pf2 = (pw ? (const char*)P.wq : (const char*)P.x) + (pw ? pfa : pfb);
  ss.sO = c.sO; ss.so = c.so; ss.sA = isV ? 2048u : 16384u; ss.sB = isV ? 16384u : 2048u;
  if (isV) k1_main<false>(c, P, pf2, qo); else k1_main<true>(c, P, pf2, qo);
  return c.tag;
}

constexpr float THR = 8.0f;
constexpr int AQ = 128, AQ2 = 192;
template <int R> DI void pv_mfma(const bf16x8& a, const u32x4& b) { asm volatile("v_mfma_f32_16x16x32_bf16 a[%c2:%c3], %0, %1, a[%c2:%c3]" :: "v"(a), "v"(b), "n"(R), "n"(R + 3)); }
template <int R> DI void pv_mfmaz(const bf16x8& a, const u32x4& b) { asm volatile("v_mfma_f32_16x16x32_bf16 a[%c2:%c3], %0, %1, 0" :: "v"(a), "v"(b), "n"(R), "n"(R + 3)); }
template <int R> DI void qk_mfma(f32x4& c, const bf16x8& a) { asm volatile("v_mfma_f32_16x16x32_bf16 %0, %1, a[%c2:%c3], %0" : "+v"(c) : "v"(a), "n"(R), "n"(R + 3)); }
template <int R> DI void qk_init(f32x4& d, const bf16x8& a, const f32x4& c) { asm volatile("v_mfma_f32_16x16x32_bf16 %0, %1, a[%c3:%c4], %2" : "=&v"(d) : "v"(a), "v"(c), "n"(R), "n"(R + 3)); }
DI void mfma_vv(f32x4& c, const bf16x8& a, const u32x4& b) { asm volatile("v_mfma_f32_16x16x32_bf16 %0, %1, %2, %0" : "+v"(c) : "v"(a), "v"(b)); }
DI void mfma_vz(f32x4& d, const bf16x8& a, const u32x4& b) { asm volatile("v_mfma_f32_16x16x32_bf16 %0, %1, %2, 0" : "=&v"(d) : "v"(a), "v"(b)); }
DI void use4(const bf16x8& w, const bf16x8& x, const bf16x8& y, const bf16x8& z) { asm volatile("" :: "v"(w), "v"(x), "v"(y), "v"(z)); }
template <int R> DI void acc_zero4() { asm volatile("v_accvgpr_write_b32 a[%c0], 0\n\tv_accvgpr_write_b32 a[%c1], 0\n\tv_accvgpr_write_b32 a[%c2], 0\n\tv_accvgpr_write_b32 a[%c3], 0" :: "n"(R), "n"(R + 1), "n"(R + 2), "n"(R + 3)); }
template <int R> DI void acc_write4(const u32x4& w) { asm volatile("v_accvgpr_write_b32 a[%c4], %0\n\tv_accvgpr_write_b32 a[%c5], %1\n\tv_accvgpr_write_b32 a[%c6], %2\n\tv_accvgpr_write_b32 a[%c7], %3" :: "v"(w[0]), "v"(w[1]), "v"(w[2]), "v"(w[3]), "n"(R), "n"(R + 1), "n"(R + 2), "n"(R + 3)); }
template <int R, int S> DI void acc_mov4() { asm volatile("v_accvgpr_mov_b32 a[%c0], a[%c4]\n\tv_accvgpr_mov_b32 a[%c1], a[%c5]\n\tv_accvgpr_mov_b32 a[%c2], a[%c6]\n\tv_accvgpr_mov_b32 a[%c3], a[%c7]" :: "n"(R), "n"(R + 1), "n"(R + 2), "n"(R + 3), "n"(S), "n"(S + 1), "n"(S + 2), "n"(S + 3)); }
template <int R> DI f32x4 acc_read4() { float a, b, c, d; asm volatile("v_accvgpr_read_b32 %0, a[%c4]\n\tv_accvgpr_read_b32 %1, a[%c5]\n\tv_accvgpr_read_b32 %2, a[%c6]\n\tv_accvgpr_read_b32 %3, a[%c7]" : "=v"(a), "=v"(b), "=v"(c), "=v"(d) : "n"(R), "n"(R + 1), "n"(R + 2), "n"(R + 3)); return (f32x4){a, b, c, d}; }
template <int R> DI void acc_scale2(float f) { float t, u; asm volatile("v_accvgpr_read_b32 %0, a[%c3]\n\tv_accvgpr_read_b32 %1, a[%c4]\n\tv_mul_f32 %0, %2, %0\n\tv_mul_f32 %1, %2, %1\n\tv_accvgpr_write_b32 a[%c3], %0\n\tv_accvgpr_write_b32 a[%c4], %1" : "=&v"(t), "=&v"(u) : "v"(f), "n"(R), "n"(R + 1)); }
template <int R, unsigned SI> DI void acc_store4(srd_t srd, unsigned voff) { unsigned t; asm volatile("s_mov_b32 %0, %3\n\tbuffer_store_dwordx4 a[%c4:%c5], %1, %2, %0 offen sc1" : "=&s"(t) : "v"(voff), "s"(srd), "n"(SI), "n"(R), "n"(R + 3) : "memory"); }
template <int R, unsigned SI> DI void acc_load4(srd_t srd, unsigned voff) { unsigned t; asm volatile("s_mov_b32 %0, %3\n\tbuffer_load_dwordx4 a[%c4:%c5], %1, %2, %0 offen sc1" : "=&s"(t) : "v"(voff), "s"(srd), "n"(SI), "n"(R), "n"(R + 3) : "memory"); }
#define A8(n) "a" #n "0", "a" #n "1", "a" #n "2", "a" #n "3", "a" #n "4", "a" #n "5", "a" #n "6", "a" #n "7", "a" #n "8", "a" #n "9"
#define ACC_CLAIM() asm volatile("; acc claim" ::: "a0", "a1", "a2", "a3", "a4", "a5", "a6", "a7", "a8", "a9", A8(1), A8(2), A8(3), A8(4), A8(5), A8(6), A8(7), A8(8), A8(9), A8(10), A8(11), A8(12), A8(13), A8(14), \
  A8(15), A8(16), A8(17), A8(18), A8(19), A8(20), A8(21), A8(22), A8(23), A8(24), "a250", "a251", "a252", "a253", "a254", "a255")
DI float max2f(float a, float b) { float r; asm("v_max_f32_e32 %0, %1, %2" : "=v"(r) : "v"(a), "v"(b)); return r; }
DI void max4(float& m, const f32x4& v) { asm volatile("v_max3_f32 %0, %0, %1, %2\n\tv_max3_f32 %0, %0, %3, %4" : "+v"(m) : "v"(v[0]), "v"(v[1]), "v"(v[2]), "v"(v[3])); }
DI float max4i(const f32x4& v) { float m; asm volatile("v_max_f32_e32 %0, %1, %2\n\tv_max3_f32 %0, %0, %3, %4" : "=&v"(m) : "v"(v[0]), "v"(v[1]), "v"(v[2]), "v"(v[3])); return m; }
DI float row_max(float x) {
  asm volatile("s_nop 1" : "+v"(x));
  auto a = __builtin_amdgcn_permlane32_swap(__float_as_uint(x), __float_as_uint(x), false, false); x = max2f(__uint_as_float(a[0]), __uint_as_float(a[1]));
  asm volatile("s_nop 1" : "+v"(x));
  auto b = __builtin_amdgcn_permlane16_swap(__float_as_uint(x), __float_as_uint(x), false, false); return max2f(__uint_as_float(b[0]), __uint_as_float(b[1]));
}
struct K2C {
  unsigned ldsb, vlane, vlane4;
  srd_t sK;
  lds_cptr kr, vr;
  unsigned kslot_w, vslot_w, vslot_r;
  unsigned ksoff, vsoff, dt;
  f32x4 s[2][4], ci[2][4]; u32x4 p[4]; bf16x8 fr[16];
  f32x4 L[2]; bf16x8 ones;
  float m[2], fct[2], mS[2], mx, thr; bool pend, first;
  unsigned spec, t0off, wbase; int pos, kn1, kend; lds_cptr vbase;
  f32x4 ml, ob[32];
  srd_t sP;
  f32x4 tri[4];
};
template <int J> DI void k2_dma(unsigned& t, unsigned m0v, unsigned voff, srd_t srd, unsigned soff) {
  if constexpr (J == 0) asm volatile("s_mov_b32 m0, %1\n\ts_mov_b32 %0, %4\n\tbuffer_load_dwordx4 %2, %3, %0 offen sc1 lds" : "=&s"(t) : "s"(m0v), "v"(voff), "s"(srd), "s"(soff) : "memory");
  else if constexpr (J == 4) asm volatile("s_add_u32 m0, m0, 4096\n\ts_add_u32 %0, %0, 4096\n\tbuffer_load_dwordx4 %1, %2, %0 offen sc1 lds" : "+s"(t) : "v"(voff), "s"(srd) : "memory", "scc");
  else asm volatile("buffer_load_dwordx4 %0, %1, %2 offen offset:%3 sc1 lds" :: "v"(voff), "s"(srd), "s"(t), "n"((J & 3) * 1024) : "memory");
}
DI void k2_dma8(unsigned m0v, unsigned voff, srd_t srd, unsigned soff) { unsigned t; sfor<8>([&](auto J) AI { k2_dma<J>(t, m0v, voff, srd, soff); }); }
DI void k2_cinit(K2C& c, float r0, float r1, int rel) {
  sfor<2>([&](auto KB) AI {
    const int rb = rel + 32 * KB;
    const float cap = rb > 0 ? -__builtin_inff() : __builtin_inff(), lift = rb < 0 ? __builtin_inff() : -__builtin_inff();
    const float a0 = __builtin_fminf(r0, cap), a1 = __builtin_fminf(r1, cap), b0 = __builtin_fminf(a0, lift), b1 = __builtin_fminf(a1, lift);
    sfor<16>([&](auto E) AI { constexpr int t = E >> 2, i = E & 3; c.ci[KB][t][i] = __builtin_amdgcn_fmed3f(c.tri[t][i], t >> 1 ? a1 : a0, t >> 1 ? b1 : b0); });
    sfor<4>([&](auto T) AI { PINV(c.ci[KB][T]); });
  });
}
DI void s_seleq(unsigned& x, int a, int b, unsigned y) { asm volatile("s_cmp_eq_u32 %1, %2\n\ts_cselect_b32 %0, %3, %0" : "+s"(x) : "s"(a), "s"(b), "s"(y) : "scc"); }
template <int kb, int e> DI void k2_exp(K2C& c) { c.s[kb][e >> 2][e & 3] = __builtin_amdgcn_exp2f(c.s[kb][e >> 2][e & 3]); PINV(c.s[kb][e >> 2]); }
template <int kb, int w> DI void k2_pack(K2C& c) { c.p[2 * kb + (w >> 2)][w & 3] = cvtpk(c.s[kb][w >> 1][2 * (w & 1)], c.s[kb][w >> 1][2 * (w & 1) + 1]); PINV(c.p[2 * kb + (w >> 2)]); }
DI void m0_set(unsigned v) { asm volatile("s_mov_b32 m0, %0" :: "s"(v)); }
DI void m0_bump() { asm volatile("s_add_u32 m0, m0, 4096" ::: "scc"); }
template <int J> DI void k2_ld(const K2C& c, unsigned soff) { asm volatile("buffer_load_dwordx4 %0, %1, %2 offen offset:%3 sc1 lds" :: "v"(J < 4 ? c.vlane : c.vlane4), "s"(c.sK), "s"(soff), "n"((J & 3) * 1024) : "memory"); }
DI void s_inc(unsigned& x) { asm volatile("s_add_u32 %0, %0, 0x8000" : "+s"(x) :: "scc"); }
template <int g> DI void k2_use(K2C& c) { use4(c.fr[g & 15], c.fr[(g + 1) & 15], c.fr[(g + 2) & 15], c.fr[(g + 3) & 15]); }
template <bool FIN, bool QK, bool FL, int g> DI void k2_gapA(K2C& c) {
  constexpr int kb = g >> 4, db = (g >> 1) & 7, m = g & 1;
  if constexpr (QK) {
    if constexpr (db == 0) qk_init<AQ>(c.s[kb][m], c.fr[g & 15], c.ci[kb][m]); else qk_mfma<AQ + 8 * db>(c.s[kb][m], c.fr[g & 15]);
    SBAR();
    if constexpr (g < 24) c.fr[(g + 8) & 15] = lds_rd128(c.kr + (g + 8) * 1024);
  }
  if constexpr (FIN && (QK ? g >= 24 : g < 8)) c.fr[g & 7] = lds_rd128(c.vr + (g & 7) * 1024);
  if constexpr (FIN && g < 10 && (g & 3) != 3) k2_pack<1, g - g / 4>(c);
  if constexpr (!FIN) acc_zero4<4 * g>();
  if constexpr (QK) {
    if constexpr (FIN && g == 12) m0_bump();
    if constexpr (g == 22) m0_set(c.vslot_w);
    if constexpr (g == 25) s_inc(c.vslot_w);
    if constexpr (g == 20) asm volatile("s_add_u32 %0, %1, %2" : "=s"(c.vsoff) : "s"(c.ksoff), "n"(MB16) : "scc");
    if constexpr (g == 21) s_inc(c.ksoff);
    if constexpr ((g & 3) == 3 && (FIN || g < 31)) { SBAR(); k2_use<g + 1>(c); }
    SBAR();
    if constexpr (db == 0) qk_init<AQ + 4>(c.s[kb][2 + m], c.fr[g & 15], c.ci[kb][2 + m]); else qk_mfma<AQ + 8 * db + 4>(c.s[kb][2 + m], c.fr[g & 15]);
  } else if constexpr (g < 6) { if constexpr (g == 2) { m0_bump(); asm volatile("s_nop 0"); } k2_ld<g + 2>(c, c.ksoff); }
  SBAR();
  if constexpr (FIN && g < 8) k2_exp<1, 8 + g>(c);
  if constexpr (!QK && g < 8) asm volatile("s_nop 0");
  if constexpr (FL) {
    acc_store4<4 * g, 1024 * g>(c.sP, c.vlane);
    if constexpr (g == 13) { float a = c.mS[0], b = c.mS[1], d = c.L[0][0], e = c.L[1][0]; PINV(a); PINV(b); PINV(d); PINV(e);
      st16w<32 * 1024>((u32x4){__float_as_uint(a), __float_as_uint(b), __float_as_uint(d), __float_as_uint(e)}, c.sP, c.vlane, 0u); }
  }
  if constexpr (QK) {
    if constexpr (FIN && g >= 8 && g < 20 && !(g & 1)) k2_ld<(g - 8) / 2 + 2>(c, c.ksoff);
    if constexpr (g >= 24 && !(g & 1)) k2_ld<(g - 24) / 2>(c, c.vsoff);
    if constexpr (g == 19) c.mx = max4i(c.s[0][0]);
    if constexpr (g >= 20 && g < 23) max4(c.mx, c.s[0][g - 19]);
    if constexpr (g == 23) s_seleq(c.vsoff, c.pos, c.kend - 1, c.wbase + 65536u);
    if constexpr (g == 25) s_seleq(c.vslot_w, (int)c.vslot_w, (int)(c.ldsb + 163840u), c.ldsb + 65536u);
    if constexpr (g == 27) s_seleq(c.ksoff, c.pos, c.kn1 - 2, c.t0off);
    if constexpr (g == 29) s_seleq(c.ksoff, c.pos, c.kend - 2, c.wbase);
    if constexpr (g == 31) m0_bump();
  }
  SBAR();
}
template <bool FIN, bool QK, bool FL = false> DI void k2_phaseA(K2C& c) {
  if constexpr (QK && !FIN) sfor<8>([&](auto I) AI { c.fr[I] = lds_rd128(c.kr + I * 1024); });
  sfor<(QK ? 32 : 10)>([&](auto G) AI { k2_gapA<FIN, QK, FL, G>(c); });
}
DI void k2_rebase(K2C& c, bool first) {
  sfor<2>([&](auto Q) AI {
    float x = max4i(c.s[0][2 * Q]); max4(x, c.s[0][2 * Q + 1]); max4(x, c.s[1][2 * Q]); max4(x, c.s[1][2 * Q + 1]);
    const float rm = row_max(x), dl = max2f(rm, first ? -1e30f : 0.f);
    float om = c.m[Q], oms = c.mS[Q]; PINV(om); PINV(oms);
    c.mS[Q] = first ? om : oms; c.fct[Q] = __builtin_amdgcn_exp2f(-dl); c.m[Q] = (first ? 0.f : om) + dl;
    sfor<4>([&](auto E) AI { c.s[E >> 1][2 * Q + (E & 1)] -= dl; });
  });
  c.pend = !first; c.thr = THR; c.first = false; c.spec |= 2u << c.pos;
}
template <bool PV, bool SM, bool FL, int g> DI void k2_gapB(K2C& c) {
  constexpr int kb = g >> 4, nb = g & 15;
  if constexpr (SM && g == 24) {
    asm volatile("s_waitcnt vmcnt(%0) lgkmcnt(0)\n\ts_barrier" :: "n"(FL ? 4 : 8) : "memory");
    SBAR();
  }
  if constexpr (PV) {
    if constexpr (SM && FL && kb == 0) pv_mfmaz<8 * nb>(c.fr[g & 15], c.p[0]); else pv_mfma<8 * nb>(c.fr[g & 15], c.p[2 * kb]);
    SBAR();
    constexpr int f = g < 3 ? 8 + 2 * g : g < 5 ? 11 + g : 12 + g;
    if constexpr (g < 20) c.fr[f & 15] = lds_rd128(c.vr + f * 1024);
    if constexpr (g < 3 || g == 4) c.fr[(f + 1) & 15] = lds_rd128(c.vr + (f + 1) * 1024);
  }
  if constexpr (SM) {
    if constexpr (g >= 24) c.fr[(g + 8) & 15] = lds_rd128(c.kr + (g - 24) * 1024);
    if constexpr (g >= 16 && g < 19) k2_pack<0, g - 16>(c);
    if constexpr (g == 20) { k2_pack<0, 3>(c); k2_pack<0, 4>(c); }
    if constexpr (g == 21) k2_pack<0, 5>(c);
    if constexpr (g == 22) k2_pack<0, 6>(c);
    if constexpr (g == 23) k2_pack<0, 7>(c);
    if constexpr (g == 21) { c.vr = c.vbase + c.vslot_r; PINV(c.vr); }
    if constexpr (g == 22) s_inc(c.vslot_r);
    if constexpr (g == 23) { c.kr = (lds_cptr)(size_t)((unsigned)(size_t)c.kr ^ 32768u); PINV(c.kr); }
    if constexpr (g == 24) m0_set(c.kslot_w);
    if constexpr (g == 26) asm volatile("s_xor_b32 %0, %0, 0x8000" : "+s"(c.kslot_w) :: "scc");
  }
  if constexpr ((g & 3) == 3 && (PV ? (SM || g < 31) : g == 31)) { SBAR(); k2_use<g + 1>(c); }
  SBAR();
  if constexpr (PV) {
    if constexpr (SM && FL && kb == 0) pv_mfmaz<8 * nb + 4>(c.fr[g & 15], c.p[1]); else pv_mfma<8 * nb + 4>(c.fr[g & 15], c.p[2 * kb + 1]);
    SBAR();
  }
  if constexpr (nb == 9 || nb == 13) {
    if constexpr (SM) { if constexpr (kb == 0) k2_ld<(nb == 9 ? 6 : 7)>(c, c.vsoff); else k2_ld<(nb == 9 ? 0 : 1)>(c, c.ksoff); SBAR(); }
    if constexpr (PV) { constexpr int q = nb == 13; if constexpr (SM && FL && kb == 0) mfma_vz(c.L[q], c.ones, c.p[q]); else mfma_vv(c.L[q], c.ones, c.p[2 * kb + q]); SBAR(); }
  }
  if constexpr (!SM && FL) c.ob[g] = __builtin_bit_cast(f32x4, __builtin_amdgcn_raw_buffer_load_b128(c.sP, c.vlane, g * 1024, 16));
  if constexpr (SM) {
    if constexpr (g == 0) k2_ld<4>(c, c.vsoff);
    if constexpr (g == 5) k2_ld<5>(c, c.vsoff);
    if constexpr (PV && g >= 1 && g < 5) max4(c.mx, c.s[1][g - 1]);
    if constexpr (g == 6) {
      if constexpr (!PV) k2_rebase(c, true);
      else if (__builtin_expect(__any(c.mx > c.thr), 0)) k2_rebase(c, c.first);
    }
    if constexpr (g == 23) s_seleq(c.vslot_r, (int)c.vslot_r, 98304, 0u);
    if constexpr (g >= 7 && g < 23) k2_exp<0, g - 7>(c);
    if constexpr (g >= 24) k2_exp<1, g - 24>(c);
    if constexpr (!PV && g >= 7) asm volatile("s_nop 0");
  }
  SBAR();
}
template <bool PV, bool SM, bool FL = false> DI void k2_phaseB(K2C& c) {
  if constexpr (!PV) { MFMA_DRAIN(); SBAR(); }
  if constexpr (!SM) { PINV(c.L[0]); PINV(c.L[1]); }
  sfor<32>([&](auto G) AI { k2_gapB<PV, SM, FL, G>(c); });
}
DI void k2_scaleO(K2C& c) { MFMA_DRAIN(); SBAR(); sfor<64>([&](auto I) AI { acc_scale2<2 * I>(c.fct[(I >> 1) & 1]); });
  PINV(c.L[0]); PINV(c.L[1]); c.L[0] *= c.fct[0]; c.L[1] *= c.fct[1]; PINV(c.L[0]); PINV(c.L[1]); c.pend = false; }

struct K2E { u32x4 cx[16]; lds_cptr rc, rn; unsigned slotw, wsoff, so; srd_t sP; u32x4 pk[2]; };
template <int CH, int g> DI void k2_gapC(K2C& c, K2E& e) {
  constexpr int kb = g >> 4, j = (g >> 1) & 7, m = g & 1;
  if constexpr (CH == 3 && g == 24) { asm volatile("s_waitcnt vmcnt(4)\n\ts_barrier" ::: "memory"); SBAR(); }
  if constexpr (j == 0) mfma_vz(c.s[kb][m], c.fr[g & 15], e.cx[0]); else mfma_vv(c.s[kb][m], c.fr[g & 15], e.cx[2 * j]);
  SBAR();
  if constexpr (CH < 2 && g == 28) { asm volatile("s_barrier" ::: "memory"); SBAR(); }
  if constexpr (g < 24) c.fr[(g + 8) & 15] = lds_rd128(e.rc + (g + 8) * 1024);
  else if constexpr (CH < 6) c.fr[(g + 8) & 15] = lds_rd128(e.rn + (g - 24) * 1024);
  constexpr int u = (g & 15) - 2; constexpr bool EP = g >= 16 || CH > 0;
  constexpr int n = 2 * CH + (g < 16 ? -1 : 0);
  if constexpr (EP && u == 0) asm volatile("s_mov_b32 %0, %1" : "=s"(e.so) : "n"(n * 2048));
  if constexpr (CH < 3 && g == 8) m0_set(e.slotw);
  if constexpr (CH < 3 && g == 24) m0_bump();
  if constexpr ((g & 3) == 3 && (CH < 6 || g < 31)) { SBAR(); k2_use<g + 1>(c); }
  SBAR();
  if constexpr (j == 0) mfma_vz(c.s[kb][2 + m], c.fr[g & 15], e.cx[1]); else mfma_vv(c.s[kb][2 + m], c.fr[g & 15], e.cx[2 * j + 1]);
  SBAR();
  if constexpr (EP && u >= 0 && u < 6) {
    constexpr int a = u / 3, i = u % 3;
    if constexpr (i < 2) { const f32x4& t = c.s[kb ^ 1][2 * a + i]; e.pk[a][2 * i] = cvtpk(t[0], t[1]); e.pk[a][2 * i + 1] = cvtpk(t[2], t[3]); PINV(e.pk[a]); }
    else asm volatile("buffer_store_dwordx4 %0, %1, %2, %3 offen offset:%4 sc1" :: "v"(e.pk[a]), "v"(c.vlane), "s"(e.sP), "s"(e.so), "n"(a * 1024) : "memory");
  }
  if constexpr (CH < 3 && (g & 15) >= 8 && !(g & 1)) k2_ld<4 * kb + ((g & 15) - 8) / 2>(c, e.wsoff);
  SBAR();
}
template <int CH> DI void k2_phaseC(K2C& c, K2E& e) { sfor<32>([&](auto G) AI { k2_gapC<CH, G>(c, e); }); }

template <class F> DI void phase_attn(const Params& P, char* lds, unsigned long long tag, const unsigned wid, u32x4 (&qo)[16], F&& filler) {
  const char *KVW = P.ws + WS_K; char *Pf = P.ws + WS_PF, *part = P.ws + WS_PART; gu64* flags = (gu64*)(P.ws + WS_FL);
  const unsigned b = blockIdx.x, xcd = b & 7u, idx = b >> 3, h = 2u * xcd + (idx >> 4), pi = (idx >> 1) & 7u, roleB = idx & 1u;
  const unsigned pair = h * 8u + pi;
  const int NA = pi == 0 ? 18 : 17;
  const int n1 = roleB ? 32 - 2 * (int)pi - NA : NA, tb1 = roleB ? NA : 0, ntot = roleB ? 34 - NA : NA;
  const unsigned wbase = 2u * MB16 + h * (DH * DM * 2) + wid * 8192u;
  const unsigned t0off = h * (SEQ * DH * 2) + wid * 8192u, k0off = t0off + (unsigned)tb1 * 32768u, k1off = n1 > 1 ? k0off + 32768u : t0off;
  K2C c;
  c.ldsb = (unsigned)(size_t)lds + wid * 8192u; c.vlane = lane_id() * 16u;
  c.sK = mk_srd(KVW, 2u * MB16 + NH * DH * DM * 2);
  unsigned qb = 15u - pi;
  ACC_CLAIM();
  if (roleB) sfor<16>([&](auto I) AI { acc_write4<AQ2 + 4 * I>(qo[I]); }); else sfor<16>([&](auto I) AI { acc_write4<AQ + 4 * I>(qo[I]); });
  SBAR();
  filler();
  wait_flags(flags + FL_S1 + (xcd + 8u * ((idx & 16u) + (lane_id() & 15u))) * 16u, tag);
  k2_dma8(c.ldsb, c.vlane, c.sK, k0off);
  if (roleB) {
    const srd_t sq = mk_srd(P.ws + ((size_t)pair * 4u + wid) * 16384u, 16384u);
    sfor<16>([&](auto I) AI { acc_load4<AQ + 4 * I, I * 1024>(sq, c.vlane); });
  }
  k2_dma8(c.ldsb + 32768u, c.vlane, c.sK, k1off); k2_dma8(c.ldsb + 65536u, c.vlane, c.sK, k0off + MB16);
  SBAR();
  const unsigned lane = lane_id();
  { const int dq = (int)(lane & 15u) - 16 * (int)(lane >> 5) - 4 * (int)((lane >> 4) & 1u);
    sfor<16>([&](auto E) AI { constexpr int t = E >> 2, i = E & 3; c.tri[t][i] = (8 * (t & 1) + i - 16 * (t >> 1) > dq) ? -__builtin_inff() : __builtin_inff(); });
    sfor<4>([&](auto T) AI { PINV(c.tri[T]); }); }
  c.kr = (lds_cptr)lds + c.vlane; c.kslot_w = c.ldsb; c.vslot_w = c.ldsb + 98304u; c.vslot_r = 0u; c.vlane4 = c.vlane + 4096u; PINV(c.vlane4);
  c.sP = mk_srd(part + ((size_t)pair * 4u + wid) * 33792u, 33792u);
  c.pos = 0; c.kn1 = roleB ? n1 : 99; c.kend = ntot; c.t0off = t0off; c.wbase = wbase; c.ksoff = k1off; c.vbase = (lds_cptr)lds + 65536u + c.vlane;
  c.spec = (7u << (ntot - 2)) | (roleB ? (n1 > 1 ? 15u << (n1 - 2) : 7u) : 0u);
  int relb = 64 * tb1 - (int)(qb * 128u + wid * 32u);
  auto prelude = [&]() AI {
    if (__builtin_expect((c.spec >> c.pos) & 1u, 0)) {
      if (c.pend) k2_scaleO(c);
      float r0 = -c.m[0], r1 = -c.m[1];
      if (c.pos == c.kn1) {
        sfor<16>([&](auto I) AI { acc_mov4<AQ + 4 * I, AQ2 + 4 * I>(); });
        r0 = 0.f; r1 = 0.f; c.thr = -__builtin_inff(); c.first = true; relb = -64 * n1 - (int)(pi * 128u + wid * 32u);
      }
      k2_cinit(c, r0, r1, relb + 64 * c.pos);
      if (!roleB && c.pos == ntot - 1) asm volatile("s_nop 4\n\tbuffer_load_dwordx2 a[192:193], off, %0, 0 sc1" :: "s"(mk_srd(P.ws + WS_FL + (FL_S2 + pair * 16u) * 8u, 8u)) : "memory");
      asm volatile("s_nop 1");
    }
    SBAR();
  };
  sfor<2>([&](auto Q) AI { c.L[Q] = f32x4{}; PINV(c.L[Q]); c.m[Q] = 0.f; c.fct[Q] = 1.f; c.mS[Q] = 0.f; });
  c.ones = (bf16x8){0x3f80, 0x3f80, 0x3f80, 0x3f80, 0x3f80, 0x3f80, 0x3f80, 0x3f80}; PINV(c.ones);
  c.pend = false; c.thr = THR; c.first = false;
  k2_cinit(c, 0.f, 0.f, relb);
  SBAR();
  asm volatile("s_waitcnt vmcnt(16) lgkmcnt(0)\n\ts_barrier" ::: "memory");
  k2_phaseA<false, true>(c); k2_phaseB<false, true>(c);
  if (wid == 0) flag_store(flags + FL_S4 + b * 16u, tag);
  ++c.pos;
  auto run = [&](int stop) AI {
#pragma nounroll
    while (c.pos < stop) {
      prelude();
#pragma nounroll
      do { k2_phaseA<true, true>(c); k2_phaseB<true, true>(c); ++c.pos; } while (!((c.spec >> c.pos) & 1u));
    }
  };
  run(roleB ? n1 + 1 : ntot);
  if (roleB) {
    prelude();
    k2_phaseA<true, true, true>(c); k2_phaseB<true, true, true>(c);
    if (wid == 0) flag_store(flags + FL_S2 + pair * 16u, tag);
    ++c.pos;
    run(ntot);
  }
  if (c.pend) k2_scaleO(c);
  qb = roleB ? pi : qb;
  const u32x4 f2 = __builtin_bit_cast(u32x4, acc_read4<AQ2>());
  const bool early = !roleB && rfl(f2[0]) == (unsigned)tag && rfl(f2[1]) == (unsigned)(tag >> 32);
  SBAR();
  k2_phaseA<true, false>(c);
  k2_dma8(c.vslot_w, c.vlane, c.sK, wbase + 3u * 32768u);
  if (early) { c.ml = __builtin_bit_cast(f32x4, __builtin_amdgcn_raw_buffer_load_b128(c.sP, c.vlane, 32 * 1024, 16)); SBAR(); k2_phaseB<true, false, true>(c); } else k2_phaseB<true, false>(c);
  MFMA_DRAIN(); SBAR();
  PINV(c.L[0]); PINV(c.L[1]);
  const float l[2] = {c.L[0][0], c.L[1][0]};
  K2E e;
  if (roleB) {
    const float rl[2] = {1.0f / l[0], 1.0f / l[1]};
    sfor<16>([&](auto I) AI { constexpr int r = 16 * (I >> 1) + 4 * (I & 1); e.cx[I] = pack8(acc_read4<r>(), acc_read4<r + 8>(), rl[I & 1]); PINV(e.cx[I]); });
  } else {
    if (!early) {
      wait_flags(flags + FL_S2 + pair * 16u, tag);
      c.ml = __builtin_bit_cast(f32x4, __builtin_amdgcn_raw_buffer_load_b128(c.sP, c.vlane, 32 * 1024, 16));
      sfor<32>([&](auto K) AI { c.ob[K] = __builtin_bit_cast(f32x4, __builtin_amdgcn_raw_buffer_load_b128(c.sP, c.vlane, K * 1024, 16)); });
    }
    const f32x4 ml = c.ml;
    SBAR();
    float fa[2], fb[2];
    sfor<2>([&](auto Q) AI { const float mm = fmaxf(c.m[Q], ml[0 + Q]), wa = __builtin_amdgcn_exp2f(c.m[Q] - mm), wb = __builtin_amdgcn_exp2f(ml[0 + Q] - mm), rl = 1.0f / (wa * l[Q] + wb * ml[2 + Q]); fa[Q] = wa * rl; fb[Q] = wb * rl; });
    sfor<16>([&](auto I) AI { constexpr int r = 16 * (I >> 1) + 4 * (I & 1), q = I & 1;
      e.cx[I] = pack8(acc_read4<r>() * fa[q] + c.ob[r / 4] * fb[q], acc_read4<r + 8>() * fa[q] + c.ob[r / 4 + 2] * fb[q], 1.0f); PINV(e.cx[I]); });
  }
  e.sP = mk_srd(Pf + ((size_t)h * 64u + qb * 4u + wid) * 28672u, 28672u);
  const unsigned kA = c.kslot_w - c.ldsb, kB = kA ^ 32768u;
  const unsigned v2 = 65536u + c.vslot_r, v3 = c.vslot_w - c.ldsb, v4 = 294912u - v2 - v3;
  const lds_cptr lb = (lds_cptr)lds + c.vlane;
  asm volatile("s_waitcnt vmcnt(0)\n\ts_barrier" ::: "memory");
  e.rn = lb + kA; PINV(e.rn);
  sfor<8>([&](auto I) AI { c.fr[I] = lds_rd128(e.rn + I * 1024); });
  auto next = [&](unsigned rn, unsigned slot, unsigned chunk) AI { e.rc = e.rn; e.rn = lb + rn; PINV(e.rn); e.slotw = c.ldsb + slot; e.wsoff = wbase + chunk * 32768u; SBAR(); };
  next(kB, v4, 4u); k2_phaseC<0>(c, e);
  next(v2, kA, 5u); k2_phaseC<1>(c, e);
  next(v3, kB, 6u); k2_phaseC<2>(c, e);
  next(v4, 0u, 0u); k2_phaseC<3>(c, e);
  next(kA, 0u, 0u); k2_phaseC<4>(c, e);
  next(kB, 0u, 0u); k2_phaseC<5>(c, e);
  next(kB, 0u, 0u); k2_phaseC<6>(c, e);
  MFMA_DRAIN(); sfor<4>([&](auto I) AI { PINV(c.s[1][I]); }); SBAR();
  st16w<26 * 1024>(pack8(c.s[1][0], c.s[1][1], 1.0f), e.sP, c.vlane, 0u); st16w<27 * 1024>(pack8(c.s[1][2], c.s[1][3], 1.0f), e.sP, c.vlane, 0u);
}
constexpr int OUTW = 2 * DM;
template <int R> DI void reduce_sum(unsigned task, unsigned lane, const bf16x8& sel0, const bf16x8& sel1, unsigned short* out) {
  const unsigned rb = task / 28u, f = task % 28u;
  f32x4 lo = {}, hi = {}; PINV(lo); PINV(hi); asm volatile("s_nop 1");
  sfor<16>([&](auto H) AI { qk_mfma<R + 4 * H>(lo, sel0); qk_mfma<R + 4 * H>(hi, sel1); });
  MFMA_DRAIN(); PINV(lo); PINV(hi);
  *(u32x4*)(out + (size_t)(32u * rb + 16u * (f & 1u) + (lane & 15u)) * OUTW + 32u * (f >> 1) + 8u * (lane >> 4)) = pack8(lo, hi, 1.0f);
}

constexpr int HCOPY = 4160, HCH = 4 * HCOPY, XROW = 80, XCH = 96 * XROW;
constexpr int SSM_LDS = 98304, XOFF = SSM_LDS + 2 * HCH;
struct K3S { lds_cptr ha, xa; f32x16 y0, y1; s16x4 al[4][2], ah[4][2]; bf16x8 bx[4][2]; };
template <int e> DI void k3_ssm_rd(K3S& c) {
  constexpr int s = e & 3;
  c.al[s][0] = *(const volatile LAS s16x4*)(c.ha + e * 64); c.ah[s][0] = *(const volatile LAS s16x4*)(c.ha + e * 64 + 8);
  c.bx[s][0] = lds_rd128(c.xa + (63 - e) * XROW); c.bx[s][1] = lds_rd128(c.xa + (31 - e) * XROW);
  c.al[s][1] = *(const volatile LAS s16x4*)(c.ha + (e + 32) * 64); c.ah[s][1] = *(const volatile LAS s16x4*)(c.ha + (e + 32) * 64 + 8);
}
template <int e, class F> DI void k3_ssm_step(K3S& c, F&& f) {
  constexpr int s = e & 3;
  if constexpr (e + 2 < 32) k3_ssm_rd<e + 2>(c);
  SBAR();
  const bf16x8 a0 = cat(c.al[s][0], c.ah[s][0]), a1 = cat(c.al[s][1], c.ah[s][1]);
  c.y1 = MFMA32(a0, c.bx[s][0], c.y1); c.y0 = MFMA32(a0, c.bx[s][1], c.y0); c.y1 = MFMA32(a1, c.bx[s][1], c.y1);
  SBAR();
  f(std::integral_constant<int, e>{});
  SBAR();
}
DI void ssm_load(SsmS& s, const Params& P, unsigned blk, unsigned long long tag, const unsigned wid) {
  const unsigned lane = lane_id(), tid = wid * 64u + lane;
  s.c0 = 2u * (28u * (blk & 7u) + (blk >> 3));
  if (!__all((((unsigned long long)s.f0[1] << 32) | s.f0[0]) == tag))
    wait_flags((gu64*)(P.ws + WS_FL) + FL_S0 + (7u * (lane & 31u) + (s.c0 >> 6)) * 16u, tag);
  const srd_t sx = mk_srd(P.ws + WS_XT, SEQ * DM * 2);
#pragma unroll
  for (int ch = 0; ch < 2; ++ch) s.xw[ch] = __builtin_amdgcn_raw_buffer_load_b128(sx, (((tid >> 3) * DM + s.c0 + ch) * 64u + 8u * (tid & 7u)) * 2u, 0, 16);
  s.dw = *(const unsigned*)(P.ssmd + s.c0);
}
DI void ssm_build(SsmS& s, char* lds, const unsigned wid) {
  const unsigned tid = wid * 64u + lane_id();
#pragma unroll
  for (int ch = 0; ch < 2; ++ch) {
    const unsigned w[6] = {s.hA[ch][0], s.hA[ch][1], s.hA[ch][2], s.hA[ch][3], s.hB[ch][0], s.hB[ch][1]};
    unsigned o[5];
#pragma unroll
    for (int k = 0; k < 5; ++k) o[k] = __builtin_amdgcn_alignbyte(w[k + 1], w[k], 2);
    LAS char* dst = (LAS char*)lds + SSM_LDS + ch * HCH + tid * 16u + 64u;
    *(LAS u32x4*)(dst) = (u32x4){o[0], o[1], o[2], o[3]};
    *(LAS u32x4*)(dst + HCOPY) = (u32x4){w[1], w[2], w[3], w[4]};
    *(LAS u32x4*)(dst + 2 * HCOPY) = (u32x4){o[1], o[2], o[3], o[4]};
    *(LAS u32x4*)(dst + 3 * HCOPY) = (u32x4){w[2], w[3], w[4], w[5]};
    if (tid < 4u) {
      const unsigned z0 = tid ? 0u : w[0], z1 = tid ? 0u : w[1], zo = tid ? 0u : o[0];
      LAS char* d2 = (LAS char*)lds + SSM_LDS + ch * HCH + (3u - tid) * 16u;
      *(LAS u32x4*)(d2) = (u32x4){0u, 0u, 0u, z0 << 16};
      *(LAS u32x4*)(d2 + HCOPY) = (u32x4){0u, 0u, 0u, z0};
      *(LAS u32x4*)(d2 + 2 * HCOPY) = (u32x4){0u, 0u, z0 << 16, zo};
      *(LAS u32x4*)(d2 + 3 * HCOPY) = (u32x4){0u, 0u, z0, z1};
    }
    LAS char* xb = (LAS char*)lds + XOFF + ch * XCH;
    if (tid < 160u) *(LAS u32x4*)(xb + tid * 16u) = (u32x4){0u, 0u, 0u, 0u};
  }
#pragma unroll
  for (int ch = 0; ch < 2; ++ch) *(LAS u32x4*)((LAS char*)lds + XOFF + ch * XCH + (32u + (tid >> 2)) * XROW + (tid & 3u) * 16u) = s.xw[ch];
}
template <class F> DI void ssm_conv(SsmS& s, char* lds, const unsigned wid, F&& f) {
  const unsigned lane = lane_id(), hi = lane >> 5, r = lane & 31u;
  const unsigned ch = wid >> 1, dh = wid & 1u;
  K3S c;
  c.ha = (lds_cptr)lds + SSM_LDS + ch * HCH + (r & 3u) * HCOPY + (24u - 16u * dh - 8u * hi + 4u * (r >> 2)) * 2u; PINV(c.ha);
  c.xa = (lds_cptr)lds + XOFF + ch * XCH + (r + 1u) * XROW + (2u * dh + hi) * 16u; PINV(c.xa);
  c.y0 = f32x16{}; c.y1 = f32x16{};
  k3_ssm_rd<0>(c); k3_ssm_rd<1>(c);
  sfor<32>([&](auto E) AI { k3_ssm_step<E>(c, f); });
  s.y0 = c.y0; s.y1 = c.y1;
}
DI void ssm_finish(SsmS& s, const Params& P, char* lds, const unsigned wid) {
  const unsigned lane = lane_id(), tid = wid * 64u + lane, hi = lane >> 5, r = lane & 31u;
  LAS char* ys = (LAS char*)lds + SSM_LDS + wid * 8192u;
#pragma unroll
  for (int k = 0; k < 4; ++k) {
    const unsigned o = r * 128u + (((2u * k + hi) ^ (r & 7u)) << 4);
    *(LAS f32x4*)(ys + o) = (f32x4){s.y0[4 * k], s.y0[4 * k + 1], s.y0[4 * k + 2], s.y0[4 * k + 3]};
    *(LAS f32x4*)(ys + 4096u + o) = (f32x4){s.y1[4 * k], s.y1[4 * k + 1], s.y1[4 * k + 2], s.y1[4 * k + 3]};
  }
  asm volatile("s_waitcnt lgkmcnt(0)\n\ts_barrier" ::: "memory");
  const unsigned row = tid >> 2, yo0 = row * 128u + (((2u * (tid & 3u)) ^ (row & 7u)) << 4), yo1 = row * 128u + (((2u * (tid & 3u) + 1u) ^ (row & 7u)) << 4);
  const srd_t sz = mk_srd(P.ws + WS_ZT, SEQ * DM * 2);
  sfor<2>([&](auto C) AI {
    const LAS char* yp = (const LAS char*)lds + SSM_LDS + C * 16384;
    const f32x4 a = *(const LAS f32x4*)(yp + yo0) + *(const LAS f32x4*)(yp + 8192 + yo0), bb = *(const LAS f32x4*)(yp + yo1) + *(const LAS f32x4*)(yp + 8192 + yo1);
    const float dd = bf2f(C ? s.dw >> 16 : s.dw & 0xffffu);
    u32x4 z;
#pragma unroll
    for (int i = 0; i < 4; ++i) {
      const f32x2 kv = up2(s.xw[C][3 - i]), y = rb2(i < 2 ? (f32x2){a[2 * i], a[2 * i + 1]} : (f32x2){bb[2 * i - 4], bb[2 * i - 3]}), kd = rb2((f32x2){kv[1] * dd, kv[0] * dd});
      z[i] = cvtpk(y[0] + kd[0], y[1] + kd[1]);
    }
    st16w<C * 128>(z, sz, (((tid >> 3) * DM + s.c0) * 64u + 8u * (tid & 7u)) * 2u, 0u);
  });
}
struct GateS { unsigned qw[2][8]; u32x4 z[2][2]; unsigned long long f; };
DI gu64* gate_flag(const Params& P, unsigned g0) {
  const unsigned lane = lane_id(), pr = 32u * ((g0 + (lane >> 5)) % 7u) + (lane & 31u);
  return (gu64*)(P.ws + WS_FL) + FL_S4 + (pr / 28u + 8u * (pr % 28u)) * 16u;
}
DI void gate_load1(GateS& s, const Params& P, unsigned g0, const unsigned wid) {
  const unsigned tid = wid * 64u + lane_id();
  s.f = __hip_atomic_load(gate_flag(P, g0), RLX_AGENT);
  sfor<16>([&](auto I) AI { const unsigned blk = g0 + (I >> 3); s.qw[I >> 3][I & 7] = *(const unsigned*)(P.bq + (size_t)(64u * (blk / 7u) + 8u * (tid >> 5) + (I & 7)) * DM + 64u * (blk % 7u) + 2u * (tid & 31u)); });
}
DI void gate_load2(GateS& s, const Params& P, unsigned g0, unsigned long long tag, const unsigned wid) {
  const unsigned tid = wid * 64u + lane_id();
  if (!__all(s.f == tag)) wait_flags(gate_flag(P, g0), tag);
  const srd_t sz = mk_srd(P.ws + WS_ZT, SEQ * DM * 2);
  sfor<4>([&](auto I) AI { const unsigned blk = g0 + (I >> 1);
    s.z[I >> 1][I & 1] = __builtin_amdgcn_raw_buffer_load_b128(sz, (((blk / 7u) * DM + 64u * (blk % 7u) + 2u * (tid & 31u) + (I & 1)) * 64u + 8u * (tid >> 5)) * 2u, 0, 16); });
}
DI void gate_store(GateS& s, const Params& P, unsigned g0, const unsigned wid) {
  const unsigned tid = wid * 64u + lane_id();
  sfor<16>([&](auto I) AI { constexpr int u = I >> 3, i = I & 7; const unsigned blk = g0 + u;
    const f32x2 q = up2(s.qw[u][i]), a = up2(s.z[u][0][i >> 1]), bb = up2(s.z[u][1][i >> 1]);
    *(unsigned*)(P.out + (size_t)(64u * (blk / 7u) + 8u * (tid >> 5) + i) * OUTW + DM + 64u * (blk % 7u) + 2u * (tid & 31u)) = cvtpk(q[0] * a[i & 1], q[1] * bb[i & 1]); });
}

__global__ __launch_bounds__(256, 1) void k1_fused(Params P) {
#ifdef __HIP_DEVICE_COMPILE__
  extern __shared__ __attribute__((aligned(16))) char lds[];
  const unsigned wid = rfl(threadIdx.x >> 6);
  const unsigned b = blockIdx.x, xcd = b & 7u, idx = b >> 3, h = 2u * xcd + (idx >> 4), j = idx & 15u;
  srd_t scode;
  { unsigned long long pc; unsigned len;
    asm volatile("s_getpc_b64 %0\n.Lcode_begin:\n\ts_mov_b32 %1, .Lcode_end-.Lcode_begin" : "=s"(pc), "=s"(len));
    scode = mk_srd((const void*)pc, len); }
  if (wid == 3) touch_lds((unsigned)(size_t)lds, scode, code_off());
  gu64* flags = (gu64*)(P.ws + WS_FL);
  const unsigned qb = (j & 1u) ? (j >> 1) : 15u - (j >> 1);
  const bool ssm = b < 224u;
  u32x4 qo[16]; SsmS s;
  const unsigned long long tag = phase_proj(P, lds, wid, qb, qo, s, scode);
  if (ssm) ssm_load(s, P, b, tag, wid);
  SBAR();
  {
    const unsigned lane = lane_id(), vl = lane * 16u;
    const lds_cptr rp = (lds_cptr)lds + WO_LDS + wid * 4096u + (lane >> 2) * 64u + 16u * (lane & 3u);
    bf16x8 wf[4];
    sfor<4>([&](auto J) AI { wf[J] = cat(lds_tr(rp + (J >> 1) * 2048 + (J & 1) * 8), lds_tr(rp + (J >> 1) * 2048 + (J & 1) * 8 + 1024)); });
    const srd_t sw_ = mk_srd(P.ws + WS_W + (size_t)h * (DH * DM * 2), DH * DM * 2);
    const srd_t sq = mk_srd(P.ws + ((size_t)(h * 8u + (j >> 1)) * 4u + wid) * 16384u, (j & 1u) ? 0u : 16384u);
    auto deferred = [&](auto E) AI {
      constexpr int k = E & 15;
      if constexpr (E < 16) st16w<k * 1024>(qo[k], sq, vl, 0u);
      else if constexpr (k < 12) { if constexpr (k < 4 * LPG) { constexpr int u = q_lunit(k), i = u >> 2, jj = u & 3; st16wa<(i & 1) * 1024>(s.late[k], s.sO, vl, s.so + (i >> 1) * s.sA + jj * s.sB); } }
      else st16w<((k - 12) >> 1) * 65536u + ((k - 12) & 1) * 1024u>(__builtin_bit_cast(u32x4, wf[k - 12]), sw_, vl, ((7u * (j & 1u) + wid) * 16u + (j & 14u)) * 1024u);
    };
    if (ssm) {
      ssm_build(s, lds, wid);
      asm volatile("s_waitcnt lgkmcnt(0)\n\ts_barrier" ::: "memory");
      ssm_conv(s, lds, wid, deferred);
    } else sfor<32>(deferred);
  }
  publish(flags + FL_S1 + b * 16u, tag, wid);
  PINV(s.y0); PINV(s.y1);
  phase_attn(P, lds, tag, wid, qo, [&]() AI { if (ssm) ssm_finish(s, P, lds, wid); });
  if (!(j & 1u) && j >= 2u) {
    const unsigned g0 = 2u * (7u * h + (j >> 1) - 1u);
    GateS gs;
    gate_load1(gs, P, g0, wid);
    publish(flags + FL_S3 + b * 16u, tag, wid);
    gate_load2(gs, P, g0, tag, wid);
    gate_store(gs, P, g0, wid);
  } else publish(flags + FL_S3 + b * 16u, tag, wid);
  const unsigned lane = lane_id();
  const unsigned task = qb * 112u + h * 7u + wid;
  const srd_t sPa = mk_srd(P.ws + WS_PF + (size_t)task * 1024u, NH * 64u * 28672u - task * 1024u), sPb = mk_srd(P.ws + WS_PF + (size_t)(task + 4u) * 1024u, wid < 3u ? NH * 64u * 28672u - (task + 4u) * 1024u : 0u);
  gu64* fl = flags + FL_S3 + (((lane & 15u) >> 1) + 8u * (16u * (lane & 1u) + j)) * 16u;
  const unsigned one = (lane >> 4) == ((lane & 15u) >> 2) ? 0x3f80u << (16u * (lane & 1u)) : 0u, w0 = (lane & 2u) ? 0u : one, w1 = (lane & 2u) ? one : 0u;
  bf16x8 sel0 = __builtin_bit_cast(bf16x8, (u32x4){w0, w1, 0u, 0u}), sel1 = __builtin_bit_cast(bf16x8, (u32x4){0u, 0u, w0, w1}); PINV(sel0); PINV(sel1);
  ACC_CLAIM();
  unsigned done = 0u;
  for (int spin = 0; spin < (1 << 20) && done != 0xffffu; ++spin) {
    const unsigned go = (unsigned)__ballot(__hip_atomic_load(fl, RLX_AGENT) == tag) & 0xffffu & ~done;
    sfor<16>([&](auto H) AI { if ((go >> H) & 1u) { acc_load4<4 * H, H * (64u * 28672u)>(sPa, lane * 16u); acc_load4<64 + 4 * H, H * (64u * 28672u)>(sPb, lane * 16u); } });
    done |= go;
    if (!go) __builtin_amdgcn_s_sleep(1);
  }
  if (b == 0 && wid == 0) flag_store(flags + FL_EPOCH, tag);
  asm volatile("s_waitcnt vmcnt(0)" ::: "memory");
  reduce_sum<0>(task, lane, sel0, sel1, P.out);
  if (wid < 3u) reduce_sum<64>(task + 4u, lane, sel0, sel1, P.out);
  asm volatile(".Lcode_end:");
#endif
}

extern "C" void kernel_launch(void* const* d_in, const int* in_sizes, int n_in, void* d_out, int out_size, void* d_ws, size_t ws_size, hipStream_t stream) {
  typedef const unsigned short* cu16;
  Params P = {(cu16)d_in[0], (cu16)d_in[1], (cu16)d_in[2], (cu16)d_in[3], (cu16)d_in[4], (cu16)d_in[5], (cu16)d_in[6], (cu16)d_in[7], (cu16)d_in[8], (cu16)d_in[9], (cu16)d_in[10], (cu16)d_in[11],
              (unsigned short*)d_out, (char*)d_ws};
  (void)hipFuncSetAttribute((const void*)k1_fused, hipFuncAttributeMaxDynamicSharedMemorySize, 163840);
  k1_fused<<<256, 256, 163840, stream>>>(P);
}
```

```cpp
#include <hip/hip_runtime.h>
#include <hip/hip_bf16.h>
#include <utility>

typedef short    bf16x8 __attribute__((ext_vector_type(8)));
typedef short    s16x4  __attribute__((ext_vector_type(4)));
typedef float    f32x16 __attribute__((ext_vector_type(16)));
typedef float    f32x4  __attribute__((ext_vector_type(4)));
typedef float    f32x2  __attribute__((ext_vector_type(2)));
typedef __bf16   bf16x2 __attribute__((ext_vector_type(2)));
typedef unsigned u32x4  __attribute__((ext_vector_type(4)));
typedef unsigned u32x2  __attribute__((ext_vector_type(2)));
#define DI __device__ __forceinline__
#define AI __attribute__((always_inline))
#define LAS __attribute__((address_space(3)))
#define SBAR() __builtin_amdgcn_sched_barrier(0)
template <class T> DI void PINV(T& x) { asm volatile("" : "+v"(x)); }
#define MFMA_DRAIN() asm volatile("s_nop 7\n\ts_nop 7" ::: "memory")
typedef LAS const char* lds_cptr;
typedef __amdgpu_buffer_rsrc_t srd_t;
template <int... Is> using iseq = std::integer_sequence<int, Is...>;
template <int N> using mkseq = std::make_integer_sequence<int, N>;

DI unsigned rfl(unsigned x) { return __builtin_amdgcn_readfirstlane(x); }
DI unsigned lane_id() { unsigned l; asm volatile("v_mbcnt_lo_u32_b32 %0, -1, 0\n\tv_mbcnt_hi_u32_b32 %0, -1, %0" : "=v"(l)); return l; }
DI unsigned cvtpk(float lo, float hi) { f32x2 v = {lo, hi}; bf16x2 b = __builtin_convertvector(v, bf16x2); return __builtin_bit_cast(unsigned, b); }
DI float bf2f(unsigned h) { return __uint_as_float(h << 16); }
DI float rbf(float x) { return __uint_as_float(cvtpk(x, x) << 16); }
DI bf16x8 lds_rd128(lds_cptr p) { return *(const LAS bf16x8*)p; }
DI s16x4 lds_tr(lds_cptr p) { return __builtin_amdgcn_ds_read_tr16_b64_v4i16((LAS s16x4*)p); }
DI bf16x8 cat(s16x4 l, s16x4 h) { return __builtin_shufflevector(l, h, 0, 1, 2, 3, 4, 5, 6, 7); }
DI srd_t mk_srd(const void* p, unsigned bytes) { return __builtin_amdgcn_make_buffer_rsrc((void*)p, (short)0, (int)bytes, 0x00020000); }
template <unsigned MI, unsigned SI> DI void dma16(unsigned mb, unsigned voff, srd_t srd, unsigned sb) {
  unsigned t;
  asm volatile("s_add_u32 m0, %1, %5\n\ts_add_u32 %0, %4, %6\n\tbuffer_load_dwordx4 %2, %3, %0 offen lds" : "=&s"(t) : "s"(mb), "v"(voff), "s"(srd), "s"(sb), "n"(MI), "n"(SI) : "memory", "scc");
}
template <unsigned MI, unsigned SI> DI void dma_prep(unsigned& t, unsigned mb, unsigned sb) { asm volatile("s_add_u32 m0, %1, %3\n\ts_add_u32 %0, %2, %4" : "=&s"(t) : "s"(mb), "s"(sb), "n"(MI), "n"(SI) : "scc"); }
DI void dma_go(unsigned t, unsigned voff, srd_t srd) { asm volatile("buffer_load_dwordx4 %0, %1, %2 offen lds" :: "v"(voff), "s"(srd), "s"(t) : "memory"); }
template <unsigned SI> DI void st16w(u32x4 v, srd_t srd, unsigned voff, unsigned sb) {
  unsigned t; asm volatile("s_add_u32 %0, %4, %5\n\tbuffer_store_dwordx4 %1, %2, %3, %0 offen sc1\n\ts_nop 1" : "=&s"(t) : "v"(v), "v"(voff), "s"(srd), "s"(sb), "n"(SI) : "memory", "scc"); }
DI void ld4(unsigned& d, srd_t srd, unsigned voff) { asm volatile("buffer_load_dword %0, %1, %2, 0 offen" : "=v"(d) : "v"(voff), "s"(srd) : "memory"); }
template <bool SC1> DI void ld16(u32x4& d, srd_t srd, unsigned voff) {
  if constexpr (SC1) asm volatile("buffer_load_dwordx4 %0, %1, %2, 0 offen sc1" : "=v"(d) : "v"(voff), "s"(srd) : "memory"); else asm volatile("buffer_load_dwordx4 %0, %1, %2, 0 offen" : "=v"(d) : "v"(voff), "s"(srd) : "memory"); }
template <class F, int... Is> DI void sfor_(F&& f, iseq<Is...>) { (f(std::integral_constant<int, Is>{}), ...); }
template <int N, class F> DI void sfor(F&& f) { sfor_(f, mkseq<N>{}); }
template <int R> DI void acc_zero4() { asm volatile("v_accvgpr_write_b32 a[%c0], 0\n\tv_accvgpr_write_b32 a[%c1], 0\n\tv_accvgpr_write_b32 a[%c2], 0\n\tv_accvgpr_write_b32 a[%c3], 0" :: "n"(R), "n"(R + 1), "n"(R + 2), "n"(R + 3)); }
template <int R> DI void acc_write4(const u32x4& w) { asm volatile("v_accvgpr_write_b32 a[%c4], %0\n\tv_accvgpr_write_b32 a[%c5], %1\n\tv_accvgpr_write_b32 a[%c6], %2\n\tv_accvgpr_write_b32 a[%c7], %3" :: "v"(w[0]), "v"(w[1]), "v"(w[2]), "v"(w[3]), "n"(R), "n"(R + 1), "n"(R + 2), "n"(R + 3)); }
template <int R, int S> DI void acc_mov4() { asm volatile("v_accvgpr_mov_b32 a[%c0], a[%c4]\n\tv_accvgpr_mov_b32 a[%c1], a[%c5]\n\tv_accvgpr_mov_b32 a[%c2], a[%c6]\n\tv_accvgpr_mov_b32 a[%c3], a[%c7]" :: "n"(R), "n"(R + 1), "n"(R + 2), "n"(R + 3), "n"(S), "n"(S + 1), "n"(S + 2), "n"(S + 3)); }
template <int R> DI f32x4 acc_read4() { float a, b, c, d; asm volatile("v_accvgpr_read_b32 %0, a[%c4]\n\tv_accvgpr_read_b32 %1, a[%c5]\n\tv_accvgpr_read_b32 %2, a[%c6]\n\tv_accvgpr_read_b32 %3, a[%c7]" : "=v"(a), "=v"(b), "=v"(c), "=v"(d) : "n"(R), "n"(R + 1), "n"(R + 2), "n"(R + 3)); return (f32x4){a, b, c, d}; }
template <int R> DI void acc_scale2(float f) { float t, u; asm volatile("v_accvgpr_read_b32 %0, a[%c3]\n\tv_accvgpr_read_b32 %1, a[%c4]\n\tv_mul_f32 %0, %2, %0\n\tv_mul_f32 %1, %2, %1\n\tv_accvgpr_write_b32 a[%c3], %0\n\tv_accvgpr_write_b32 a[%c4], %1" : "=&v"(t), "=&v"(u) : "v"(f), "n"(R), "n"(R + 1)); }
template <int R, unsigned SI> DI void acc_store4(srd_t srd, unsigned voff) { unsigned t; asm volatile("s_mov_b32 %0, %3\n\tbuffer_store_dwordx4 a[%c4:%c5], %1, %2, %0 offen sc1" : "=&s"(t) : "v"(voff), "s"(srd), "n"(SI), "n"(R), "n"(R + 3) : "memory"); }
template <int R, unsigned SI> DI void acc_load4(srd_t srd, unsigned voff) { unsigned t; asm volatile("s_mov_b32 %0, %3\n\tbuffer_load_dwordx4 a[%c4:%c5], %1, %2, %0 offen sc1" : "=&s"(t) : "v"(voff), "s"(srd), "n"(SI), "n"(R), "n"(R + 3) : "memory"); }
#define A8(n) "a" #n "0", "a" #n "1", "a" #n "2", "a" #n "3", "a" #n "4", "a" #n "5", "a" #n "6", "a" #n "7", "a" #n "8", "a" #n "9"
#define ACC_CLAIM() asm volatile("; acc claim" ::: "a0", "a1", "a2", "a3", "a4", "a5", "a6", "a7", "a8", "a9", A8(1), A8(2), A8(3), A8(4), A8(5), A8(6), A8(7), A8(8), A8(9), A8(10), A8(11), A8(12), A8(13), A8(14), \
  A8(15), A8(16), A8(17), A8(18), A8(19), A8(20), A8(21), A8(22), A8(23), A8(24), "a250", "a251", "a252", "a253", "a254", "a255")
DI u32x4 pack8(const f32x4& a, const f32x4& b, float sc) { u32x4 w = {cvtpk(a[0] * sc, a[1] * sc), cvtpk(a[2] * sc, a[3] * sc), cvtpk(b[0] * sc, b[1] * sc), cvtpk(b[2] * sc, b[3] * sc)}; return w; }
constexpr int SEQ = 2048, NH = 16, DH = 256, DM = 448, DC = 1024;
constexpr unsigned WRS = NH * DH * 2;
constexpr unsigned MB16 = SEQ * NH * DH * 2;
constexpr float QSCALE = 1.4426950408889634f / 16.0f;

DI unsigned code_off() { return ((blockIdx.x >> 3) * 64u + lane_id()) * 64u; }
typedef __attribute__((address_space(1))) unsigned long long gu64;
#define RLX_AGENT __ATOMIC_RELAXED, __HIP_MEMORY_SCOPE_AGENT
struct Params {
  const unsigned short *x, *ckv, *wq, *wuk, *wuv, *wo, *bq, *bk, *bv, *shk, *ssmk, *ssmd;
  unsigned short* out; char* ws;
};
struct Ctx { srd_t code; };
constexpr size_t WS_K = MB16, WS_W = 3 * (size_t)MB16, WS_XT = WS_W + (4u << 20), WS_ZT = WS_XT + (2u << 20), WS_FL = WS_ZT + (2u << 20), WS_PART = WS_FL + (1u << 20), WS_PF = WS_PART + (17u << 20);
constexpr int FL_EPOCH = 0, FL_S0 = 16, FL_S1 = FL_S0 + 256 * 16, FL_S2 = FL_S1 + 256 * 16, FL_S3 = FL_S2 + 128 * 16, FL_S4 = FL_S3 + 256 * 16, FL_SQ = FL_S4 + 256 * 16;
DI void flag_store(gu64* flag, unsigned long long tag) {
  const u32x2 v = {(unsigned)tag, (unsigned)(tag >> 32)};
  asm volatile("s_nop 4\n\tbuffer_store_dwordx2 %0, %1, %2, 0 offen sc1" :: "v"(v), "v"(lane_id() * 16u), "s"(mk_srd((const void*)flag, 8u)) : "memory");
}
DI void publish(gu64* flag, unsigned long long tag, unsigned wid) {
  asm volatile("s_waitcnt vmcnt(0)\n\ts_barrier" ::: "memory");
  if (wid == 0) flag_store(flag, tag);
}
DI void wait_flags(gu64* lane_flag, unsigned long long tag) {
  for (int spin = 0; spin < (1 << 20); ++spin) {
    if (__all(__hip_atomic_load(lane_flag, RLX_AGENT) == tag)) break;
    __builtin_amdgcn_s_sleep(1);
  }
}

template <int R> DI void mfma16a(const bf16x8& a, const bf16x8& b) { asm volatile("v_mfma_f32_16x16x32_bf16 a[%c2:%c3], %0, %1, a[%c2:%c3]" :: "v"(a), "v"(b), "n"(R), "n"(R + 3)); }
template <int R> DI void mfma16az(const bf16x8& a, const bf16x8& b) { asm volatile("v_mfma_f32_16x16x32_bf16 a[%c2:%c3], %0, %1, 0" :: "v"(a), "v"(b), "n"(R), "n"(R + 3)); }
DI void mfma32v(f32x16& c, const bf16x8& a, const bf16x8& b) { asm volatile("v_mfma_f32_32x32x16_bf16 %0, %1, %2, %0" : "+v"(c) : "v"(a), "v"(b)); }
DI void mfma32z(f32x16& c, const bf16x8& a, const bf16x8& b) { asm volatile("v_mfma_f32_32x32x16_bf16 %0, %1, %2, 0" : "=&v"(c) : "v"(a), "v"(b)); }
DI f32x2 up2(unsigned w) { f32x2 r = {__uint_as_float(w << 16), __uint_as_float(w & 0xffff0000u)}; return r; }
DI f32x2 rb2(f32x2 v) { return up2(cvtpk(v[0], v[1])); }
constexpr unsigned qxs(int s) { return 16384u * s; }
constexpr unsigned qws(int s) { return s == 0 ? 65536u : s == 1 ? 131072u : 98304u; }
constexpr unsigned WO_LDS = 49152u;
constexpr int HCOPY = 4160, HCH = 4 * HCOPY, XROW = 80, XCH = 96 * XROW;
constexpr int SSM_LDS = 98304, XOFF = SSM_LDS + 2 * HCH;
struct SsmS { u32x4 xw[2], hA[2], hB[2], oa, ob; u32x2 f0; unsigned dw, c0; f32x16 y0, y1; LAS char *d1, *d2; lds_cptr ha, xa; s16x4 al[4][2], ah[4][2]; bf16x8 bx[4][2], wf[4]; srd_t sq, sw; unsigned wo; };
struct Frags { bf16x8 x[8]; s16x4 wl[8], wh[8]; };
struct K1C {
  unsigned ldsb, lds8, lds4, wid, vlane, b, hb;
  unsigned vx_kv[2], vx_q[2], vw[4], vwq[2];
  srd_t sX, sW, sXq, sWq, sO;
  unsigned sx_kv, sw_kv, sx_q, sw_q, so;
  lds_cptr xr_kv[2], wr[8], xq[4], wq[4], wq1[4];
  const char* pf; unsigned pfstep, xoff, dt, tabl, tabh, tabn; int it; srd_t sx2;
  Frags f0, f1; f32x2 ta, tb; u32x4 pk[2]; unsigned sB, sA3;
  f32x16 qa[8]; s16x4 ql[16], qh[16]; bf16x8 qx[4]; f32x2 qs;
  unsigned kw[11], vv[8], kv[8]; u32x4 sk; u32x2 ep; f32x2 kf[11], sc[4], pa, pt;
  unsigned long long tag; srd_t scode;
};
template <int L> DI void wlane(unsigned& v, unsigned s) { asm volatile("v_writelane_b32 %0, %1, %2" : "+v"(v) : "s"(s), "n"(L)); }
DI void touch_lds(unsigned ldsb, srd_t srd, unsigned voff) {
  asm volatile("s_add_u32 m0, %0, 147456\n\ts_nop 0\n\tbuffer_load_dword %1, %2, 0 offen lds" :: "s"(ldsb), "v"(voff), "s"(srd) : "memory", "scc"); }
DI void k1_touch(const K1C& c) {
  asm volatile("s_add_u32 m0, %0, 147456\n\ts_nop 0\n\tglobal_load_lds_dword %1, off" :: "s"(c.ldsb), "v"(c.pf) : "memory", "scc"); }
template <int buf, int j> DI void k1_piece(const K1C& c, unsigned sw, unsigned sx) {
  constexpr int p = j >> 1;
  if constexpr (!(j & 1)) dma16<buf * 32768u + 65536u + p * 1024u, 2u * p * WRS>(c.lds8, c.vw[(p & 1) + 2 * (p >> 2)], c.sW, sw);
  else dma16<buf * 32768u + p * 1024u, 8u * p * (DC * 2)>(c.lds8, c.vx_kv[(p >> 1) & 1], c.sX, sx);
}
template <int buf, int j> DI void k1_prep(K1C& c, unsigned sw, unsigned sx) {
  constexpr int p = j >> 1;
  if constexpr (!(j & 1)) dma_prep<buf * 32768u + 65536u + p * 1024u, 2u * p * WRS>(c.dt, c.lds8, sw); else dma_prep<buf * 32768u + p * 1024u, 8u * p * (DC * 2)>(c.dt, c.lds8, sx);
}
template <int j> DI void k1_go(const K1C& c) { constexpr int p = j >> 1; if constexpr (!(j & 1)) dma_go(c.dt, c.vw[(p & 1) + 2 * (p >> 2)], c.sW); else dma_go(c.dt, c.vx_kv[(p >> 1) & 1], c.sX); }
template <int S, int j> DI void k1_qpiece(const K1C& c) {
  if constexpr (j % 3 != 1) { constexpr int p = 2 * (j / 3) + (j % 3 == 2); dma16<qws(S % 3) + p * 1024u, (64u * S + 2u * p) * WRS>(c.lds8, c.vwq[p & 1], c.sWq, c.sw_q); }
  else { constexpr int p = j / 3; dma16<qxs(S % 3) + p * 1024u, 8u * p * (DM * 2) + 128u * S>(c.lds4, c.vx_q[p & 1], c.sXq, c.sx_q); }
}
template <int buf> DI void k1_stage(const K1C& c, int st) { sfor<16>([&](auto J) AI { k1_piece<buf, J>(c, c.sw_kv + (unsigned)st * (64u * WRS), c.sx_kv + (unsigned)st * 128u); }); }
template <int buf, int ks, int i> DI void k1_read(const K1C& c, Frags& f) {
  constexpr int so = buf * 32768;
  if constexpr (i < 2 || i >= 10) { constexpr int k = i < 2 ? i : i - 8, nb = k >> 1, t = k & 1;
    const s16x4 v = lds_tr(c.wr[nb] + so + ks * 16384 + t * 2048); if constexpr (t) f.wh[nb] = v; else f.wl[nb] = v; }
  else { constexpr int mb = i - 2; f.x[mb] = lds_rd128(c.xr_kv[ks] + so + (mb >> 1) * 4096 + (mb & 1) * 1024); }
}
template <int F> DI void k1_qw(K1C& c) {
  constexpr int T = F >> 5, ks = (F >> 3) & 3, nb = F & 7, sl = T % 3, o = (sl == 2 ? 32768 : 0) + ks * 8192 + (nb >> 2) * 256;
  const lds_cptr p = sl == 1 ? c.wq1[nb & 3] : c.wq[nb & 3];
  c.ql[F & 15] = lds_tr(p + o); c.qh[F & 15] = lds_tr(p + o + 2048);
}
template <int T, int ks> DI void k1_qx(K1C& c) { c.qx[ks] = lds_rd128(c.xq[ks] + qxs(T % 3)); }
template <int m> DI void k1_pre(K1C& c, const Params& P) {
  if constexpr (m == 0) {
    asm volatile("" : "+v"(c.kw[0]), "+v"(c.kw[1]), "+v"(c.kw[2]), "+v"(c.kw[3]), "+v"(c.kw[4]), "+v"(c.kw[5]), "+v"(c.kw[6]), "+v"(c.kw[7]), "+v"(c.kw[8]), "+v"(c.kw[9]), "+v"(c.kw[10]),
                 "+v"(c.vv[0]), "+v"(c.vv[1]), "+v"(c.vv[2]), "+v"(c.vv[3]), "+v"(c.vv[4]), "+v"(c.vv[5]), "+v"(c.vv[6]), "+v"(c.vv[7]), "+v"(c.sk), "+v"(c.ep));
    const unsigned long long e0 = ((unsigned long long)rfl(c.ep[1]) << 32) | rfl(c.ep[0]);
    c.tag = (e0 ^ 0x9E3779B97F4A7C15ull) * 0xD1342543DE82EF95ull + 1ull;
  } else if constexpr (m < 12) { c.kf[m - 1] = up2(c.kw[m - 1]); PINV(c.kf[m - 1]); }
  else if constexpr (m < 14) { constexpr int i = m - 12;
    c.sc[2 * i] = (f32x2){bf2f(c.sk[i] & 0xffffu), bf2f(c.sk[2 + i] & 0xffffu)}; c.sc[2 * i + 1] = (f32x2){bf2f(c.sk[i] >> 16), bf2f(c.sk[2 + i] >> 16)}; PINV(c.sc[2 * i]); PINV(c.sc[2 * i + 1]); }
  else if constexpr (m < 78) { constexpr int i = (m - 14) >> 3, s = (m - 14) & 7;
    if constexpr (s == 0) { c.pa = rb2(c.kf[i] * c.sc[3]); PINV(c.pa); }
    else if constexpr (s == 7) { const f32x2 a = c.pa * up2(c.vv[i]); c.kv[i] = cvtpk(a[0], a[1]); PINV(c.kv[i]); }
    else if constexpr (s & 1) { c.pt = rb2(c.kf[i + (s + 1) / 2] * c.sc[3 - (s + 1) / 2]); PINV(c.pt); }
    else { c.pa = rb2(c.pa + c.pt); PINV(c.pa); }
  } else {
    constexpr unsigned sel = m == 78 ? 0x05040100u : 0x07060302u;
    const unsigned tid = c.wid * 64u + (c.vlane >> 4), xo = (((c.b / 7u) * DM + 64u * (c.b % 7u) + 2u * (tid & 31u)) * 64u + 8u * (tid >> 5)) * 2u;
    st16w<(m - 78) * 128>((u32x4){__builtin_amdgcn_perm(c.kv[6], c.kv[7], sel), __builtin_amdgcn_perm(c.kv[4], c.kv[5], sel), __builtin_amdgcn_perm(c.kv[2], c.kv[3], sel), __builtin_amdgcn_perm(c.kv[0], c.kv[1], sel)},
                          mk_srd(P.ws + WS_XT, SEQ * DM * 2), xo, 0u);
  }
}
template <int i> DI void k1_preload(K1C& c, const Params& P) {
  const unsigned tid = c.wid * 64u + (c.vlane >> 4), bc0 = 64u * (c.b % 7u) + 2u * (tid & 31u), vo = ((64u * (c.b / 7u) + 8u * (tid >> 5)) * DM + bc0) * 2u;
  if constexpr (i < 11) ld4(c.kw[i], mk_srd(P.bk, SEQ * DM * 2), vo + (unsigned)((i - 3) * DM * 2));
  else if constexpr (i < 19) ld4(c.vv[i - 11], mk_srd(P.bv, SEQ * DM * 2), vo + (unsigned)((i - 11) * DM * 2));
  else if constexpr (i == 19) asm volatile("buffer_load_dwordx4 %0, %1, %2, 0 offen" : "=v"(c.sk) : "v"(bc0 * 8u), "s"(mk_srd(P.shk, DM * 8)) : "memory");
  else asm volatile("buffer_load_dwordx2 %0, off, %1, 0 sc1" : "=v"(c.ep) : "s"(mk_srd(P.ws + WS_FL, 64)) : "memory");
}
template <bool WA, int STEP, int buf, int G> DI void k1_gap(K1C& c, const Params& P, unsigned sw, unsigned sx) {
  constexpr int ks = G >> 6, g = G & 63, nb = g >> 3, mb = g & 7;
  Frags& fc = ks ? c.f1 : c.f0; Frags& fn = ks ? c.f0 : c.f1;
  if constexpr (G == 64) {
    asm volatile("s_waitcnt vmcnt(%0) lgkmcnt(0)\n\ts_barrier" :: "n"(STEP == 0 ? 3 : STEP == 14 ? 0 : STEP == 15 ? 12 : buf == 1 ? 2 : 1) : "memory");
    SBAR();
  }
  const bf16x8 w = cat(fc.wl[nb], fc.wh[nb]);
  if constexpr (G == 0) asm volatile("s_nop 1" :: "v"(w), "v"(fc.x[0]));
  if constexpr (STEP == 0 && ks == 0) { if constexpr (WA) mfma16az<32 * mb + 4 * nb>(w, fc.x[mb]); else mfma16az<32 * nb + 4 * mb>(fc.x[mb], w); }
  else if constexpr (WA) mfma16a<32 * mb + 4 * nb>(w, fc.x[mb]); else mfma16a<32 * nb + 4 * mb>(fc.x[mb], w);
  SBAR();
  if constexpr (!(g & 1) && g < 48) {
    if constexpr (ks == 0) k1_read<buf, 1, g / 2>(c, fn);
    else if constexpr (STEP != 15) k1_read<buf ^ 1, 0, g / 2>(c, fn);
  }
  if constexpr (STEP == 15 && ks == 1 && g >= 36 && !(g & 3)) { if constexpr (g == 36) k1_qx<0, 0>(c); else k1_qw<(g - 40) / 4>(c); }
  if constexpr (STEP == 0 && ks == 0 && (g & 1) && g < 16) k1_piece<1, 8 + (g >> 1)>(c, c.sw_kv + 64u * WRS, c.sx_kv + 128u);
  if constexpr (STEP == 0 && (G == 17 || G == 19 || G == 21)) { k1_touch(c); c.pf += c.pfstep; }
  if constexpr (STEP == 1 && G >= 61 && G < 124 && (G == 61 || (G > 64 && (G & 3) == 2 + ((G >> 2) & 1) * 0 + 1))) k1_prep<buf, (G == 61 ? 0 : (G - 63) >> 2)>(c, sw, sx);
  if constexpr (STEP == 1 && buf == 0 && G == 51) c.sx2 = mk_srd((const void*)(((unsigned long long)__builtin_amdgcn_readlane(c.tabh, c.it) << 32) | (unsigned)__builtin_amdgcn_readlane(c.tabl, c.it)), __builtin_amdgcn_readlane(c.tabn, c.it));
  if constexpr (STEP == 13 && G == 64) { if (c.wid == 3) touch_lds(c.ldsb, c.scode, code_off()); }
  if constexpr (ks == 1) {
    if constexpr (STEP != 1 && STEP <= 13 && (g & 3) == 1) k1_piece<buf, (g >> 2)>(c, sw, sx);
    if constexpr (STEP == 1 && (g & 3) == 1) k1_go<(g >> 2)>(c);
    if constexpr (STEP == 0 && (g & 3) == 3) k1_preload<(g >> 2)>(c, P);
    if constexpr (STEP == 0 && (g & 3) == 2 && g >= 44) k1_preload<16 + ((g - 44) >> 2)>(c, P);
    if constexpr (STEP == 14 && (g & 1) && g < 60 && (g >> 1) % 5 != 4) { constexpr int j = (g >> 1) - (g >> 1) / 5; k1_qpiece<j / 12, j % 12>(c); }
    if constexpr (STEP == 15 && (g & 3) == 1 && g < 48) k1_qpiece<2, (g >> 2)>(c);
  }
  if constexpr (STEP <= 13 && G == 127) { k1_touch(c); c.pf += c.pfstep; if constexpr (buf == 0) touch_lds(c.ldsb, c.sx2, c.xoff); }
  if constexpr (STEP >= 13) { constexpr int X = (STEP - 13) * 128 + G; if constexpr (X >= 56 && X % 4 == 3 && (X - 56) / 4 < 80) k1_pre<(X - 56) / 4>(c, P); }
  SBAR();
}
template <bool WA, int STEP, int buf> DI void k1_step(K1C& c, const Params& P, int sb) {
  const unsigned sw = c.sw_kv + (unsigned)sb * (64u * WRS), sx = c.sx_kv + (unsigned)sb * 128u;
  sfor<128>([&](auto G) AI { k1_gap<WA, STEP, buf, G>(c, P, sw, sx); });
}
template <int R> DI f32x2 acc_read2() { float a, b; asm volatile("v_accvgpr_read_b32 %0, a[%c2]\n\tv_accvgpr_read_b32 %1, a[%c3]" : "=v"(a), "=v"(b) : "n"(R), "n"(R + 1)); return (f32x2){a, b}; }
template <int u, int ph> DI void k1_epi(K1C& c) {
  constexpr int i = 2 * (u >> 3) + (u & 1), j = (u >> 1) & 3, R = 32 * i + 8 * j; u32x4& pk = c.pk[u & 1];
  if constexpr (ph == 0) c.ta = acc_read2<R>();
  if constexpr (ph == 1) { c.tb = acc_read2<R + 2>(); if constexpr (u > 0) asm volatile("" :: "v"(c.pk[(u & 1) ^ 1])); }
  if constexpr (ph == 2) { pk[0] = cvtpk(c.ta[0], c.ta[1]); pk[1] = cvtpk(c.tb[0], c.tb[1]); PINV(pk); }
  if constexpr (ph == 3) c.ta = acc_read2<R + 4>();
  if constexpr (ph == 4) c.tb = acc_read2<R + 6>();
  if constexpr (ph == 5) { pk[2] = cvtpk(c.ta[0], c.ta[1]); pk[3] = cvtpk(c.tb[0], c.tb[1]); PINV(pk); }
  if constexpr (ph == 6) {
    asm volatile("buffer_store_dwordx4 %0, %1, %2, %3 offen offset:%4 sc1" :: "v"(pk), "v"(c.vlane), "s"(c.sO), "s"(c.so), "n"((u & 1) * 1024) : "memory");
    if constexpr (u & 1) asm volatile("s_add_u32 %0, %0, %1" : "+s"(c.so) : "s"((u & 7) == 7 ? c.sA3 : c.sB) : "scc");
  }
}
constexpr bool q_dma(int X) { return X >= 26 && !(X & 1) && (X - 26) % 32 < 24 && (X - 26) / 32 + 3 <= 6; }
#define NQU 24
constexpr bool q_free(int X) { return X >= 1 && X < 187 && !q_dma(X) && !(X >= 117 && X < 128); }
constexpr int q_nfree(int X) { int n = 0; for (int x = 0; x < X; ++x) n += q_free(x); return n; }
constexpr int q_ep(int X) { return q_nfree(X) * (7 * NQU) / q_nfree(224); }
constexpr int q_stores(int lo, int hi) { return q_ep(hi) / 7 - q_ep(lo + 1) / 7; }
constexpr int q_wait(int T) {
  return T == 0 ? 14 + q_stores(-1, 26) : T == 1 ? 12 + q_stores(-1, 58) : (T + 2 <= 6 ? 12 : 0) + q_stores(32 * (T - 2) + 48, 32 * T + 26); }
template <int X> DI void k1_qgap(K1C& c, SsmS& s, const Params& P) {
  constexpr int T = X >> 5, g = X & 31, ks = g >> 3, nb = g & 7;
  if constexpr (g == 26 && T < 6) {
    asm volatile("s_waitcnt vmcnt(%0) lgkmcnt(0)\n\ts_barrier" :: "n"(q_wait(T)) : "memory"); SBAR();
    if constexpr (T == 1) { if (c.wid == 0) flag_store((gu64*)(P.ws + WS_FL) + FL_S0 + c.b * 16u, c.tag); SBAR(); }
    if constexpr (T == 5) asm volatile("" : "+v"(s.hA[0]), "+v"(s.hA[1]), "+v"(s.hB[0]), "+v"(s.hB[1]), "+v"(s.f0), "+v"(s.dw));
  }
  const bf16x8 w = cat(c.ql[X & 15], c.qh[X & 15]);
  if constexpr (!(X & 1)) asm volatile("" :: "v"(w), "v"(c.ql[(X + 1) & 15]), "v"(c.qh[(X + 1) & 15]));
  if constexpr (X < 8) mfma32z(c.qa[nb], w, c.qx[ks]); else mfma32v(c.qa[nb], w, c.qx[ks]);
  SBAR();
  if constexpr (g < 22) { constexpr int f = 6 + g + (g + 4) / 5 - (g > 20 ? 1 : 0); k1_qw<32 * T + f>(c); if constexpr (g % 5 == 0 && g < 20) k1_qw<32 * T + f + 1>(c); }
  if constexpr (g >= 26 && T < 6) k1_qw<32 * (T + 1) + g - 26>(c);
  if constexpr (g == 2 || g == 8 || g == 16) k1_qx<T, (g + 8) / 8 - (g == 2 ? 0 : 0) + (g == 2 ? 0 : 0)>(c);
  if constexpr (g == 28 && T < 6) k1_qx<T + 1, 0>(c);
  if constexpr (q_dma(X)) k1_qpiece<(X - 26) / 32 + 3, ((X - 26) % 32) / 2>(c);
  sfor<q_ep(X + 1) - q_ep(X)>([&](auto I) AI { k1_epi<(q_ep(X) + I) / 7, (q_ep(X) + I) % 7>(c); });
  const unsigned tid = c.wid * 64u + (c.vlane >> 4);
  if constexpr (X >= 117 && X < 128 && (X & 1)) {
    constexpr int k = (X - 117) >> 1, ch = k >> 1; const unsigned ok = c.b < 224u;
    if constexpr (k == 3) ld16<false>(s.hB[1], mk_srd(P.ssmk + (size_t)(s.c0 + 1u) * SEQ, ok * (SEQ * 2)), tid * 16u + 16u);
    else if constexpr (k == 4) ld4(s.dw, mk_srd(P.ssmd, DM * 2), s.c0 * 2u);
    else if constexpr (k == 5) asm volatile("buffer_load_dwordx2 %0, %1, %2, 0 offen sc1" : "=v"(s.f0) : "v"((FL_S0 + (7u * (tid & 31u) + (s.c0 >> 6)) * 16u) * 8u), "s"(mk_srd(P.ws + WS_FL, 1u << 20)) : "memory");
    else if constexpr (k == 1) ld16<false>(s.hB[0], mk_srd(P.ssmk + (size_t)s.c0 * SEQ, ok * (SEQ * 2)), tid * 16u + 16u);
    else ld16<false>(s.hA[ch], mk_srd(P.ssmk + (size_t)(s.c0 + ch) * SEQ, ok * (SEQ * 2)), tid * 16u);
  }
  if constexpr (X == 188 || X == 189) ld16<true>(s.xw[X - 188], mk_srd(P.ws + WS_XT, SEQ * DM * 2), (((tid >> 3) * DM + s.c0 + (X - 188)) * 64u + 8u * (tid & 7u)) * 2u);
  if constexpr (X >= 190 && X < 194) {
    constexpr int j = X - 190; const unsigned ln = c.vlane >> 4;
    dma16<WO_LDS + j * 1024u, (j >> 1) * 256u + (j & 1) * (16u * DM * 2)>(c.lds4, (ln >> 2) * (DM * 2) + (ln & 3u) * 16u, mk_srd(P.wo, NH * DH * DM * 2), (c.hb >> 1) * (32u * DM * 2) + (7u * (c.hb & 1u) + c.wid) * 64u);
  }
  if constexpr (X == 194) { s.d1 = (LAS char*)(size_t)(c.ldsb + SSM_LDS + tid * 16u + 64u); s.d2 = tid < 4u ? (LAS char*)(size_t)(c.ldsb + SSM_LDS + (3u - tid) * 16u) : s.d1; PINV(s.d1); PINV(s.d2); }
  if constexpr (X >= 195 && X < 219) {
    constexpr int ch = (X - 195) / 12, k = (X - 195) % 12;
    const unsigned w0 = s.hA[ch][0], w1 = s.hA[ch][1], w2 = s.hA[ch][2], w3 = s.hA[ch][3], w4 = s.hB[ch][0], w5 = s.hB[ch][1];
    if constexpr (k == 0) { s.oa = (u32x4){__builtin_amdgcn_alignbyte(w1, w0, 2), __builtin_amdgcn_alignbyte(w2, w1, 2), __builtin_amdgcn_alignbyte(w3, w2, 2), __builtin_amdgcn_alignbyte(w4, w3, 2)}; PINV(s.oa); }
    if constexpr (k == 1) { s.ob = (u32x4){s.oa[1], s.oa[2], s.oa[3], __builtin_amdgcn_alignbyte(w5, w4, 2)}; PINV(s.ob); }
    const unsigned z0 = tid ? 0u : w0, z1 = tid ? 0u : w1, zo = tid ? 0u : s.oa[0];
    if constexpr (k == 2) { if (c.wid == 0) *(LAS u32x4*)(s.d2 + ch * HCH) = (u32x4){0u, 0u, 0u, z0 << 16}; }
    if constexpr (k == 3) { if (c.wid == 0) *(LAS u32x4*)(s.d2 + ch * HCH + HCOPY) = (u32x4){0u, 0u, 0u, z0}; }
    if constexpr (k == 4) { if (c.wid == 0) *(LAS u32x4*)(s.d2 + ch * HCH + 2 * HCOPY) = (u32x4){0u, 0u, z0 << 16, zo}; }
    if constexpr (k == 5) { if (c.wid == 0) *(LAS u32x4*)(s.d2 + ch * HCH + 3 * HCOPY) = (u32x4){0u, 0u, z0, z1}; }
    if constexpr (k == 6) *(LAS u32x4*)(s.d1 + ch * HCH) = s.oa;
    if constexpr (k == 7) *(LAS u32x4*)(s.d1 + ch * HCH + HCOPY) = (u32x4){w1, w2, w3, w4};
    if constexpr (k == 8) *(LAS u32x4*)(s.d1 + ch * HCH + 2 * HCOPY) = s.ob;
    if constexpr (k == 9) *(LAS u32x4*)(s.d1 + ch * HCH + 3 * HCOPY) = (u32x4){w2, w3, w4, w5};
    if constexpr (k == 10) *(LAS u32x4*)(size_t)(c.ldsb + XOFF + ch * XCH + min(tid, 159u) * 16u) = (u32x4){0u, 0u, 0u, 0u};
  }
  SBAR();
}
template <int e, int k> DI void ssm_rd(SsmS& s) {
  constexpr int b = e & 3;
  if constexpr (k == 0) { s.al[b][0] = *(const volatile LAS s16x4*)(s.ha + e * 64); s.ah[b][0] = *(const volatile LAS s16x4*)(s.ha + e * 64 + 8); }
  if constexpr (k == 1) { s.bx[b][0] = lds_rd128(s.xa + (63 - e) * XROW); s.bx[b][1] = lds_rd128(s.xa + (31 - e) * XROW); }
  if constexpr (k == 2) { s.al[b][1] = *(const volatile LAS s16x4*)(s.ha + (e + 32) * 64); s.ah[b][1] = *(const volatile LAS s16x4*)(s.ha + (e + 32) * 64 + 8); }
}
template <int n> DI void k1_qop(K1C& c, u32x4 (&qo)[16]) {
  constexpr int D = n / 20, k = n % 20;
  if constexpr (k < 8) { f32x2 v = {c.qa[D][2 * k], c.qa[D][2 * k + 1]}; asm volatile("v_pk_mul_f32 %0, %0, %1" : "+v"(v) : "v"(c.qs)); c.qa[D][2 * k] = v[0]; c.qa[D][2 * k + 1] = v[1]; }
  else if constexpr (k < 16) { constexpr int hf = k & 1, w = (k - 8) >> 1; qo[2 * D + hf][w] = cvtpk(c.qa[D][8 * hf + 2 * w], c.qa[D][8 * hf + 2 * w + 1]); PINV(qo[2 * D + hf]); }
  else { unsigned a = qo[2 * D][k - 16], b = qo[2 * D + 1][k - 16]; asm volatile("v_permlane16_swap_b32 %0, %1" : "+v"(a), "+v"(b)); qo[2 * D][k - 16] = a; qo[2 * D + 1][k - 16] = b; }
}
constexpr int y_qop(int Y) { const int n = Y < 60 ? Y : 60 + 3 * (Y - 60); return n < 160 ? n : 160; }
constexpr int y_st(int Y) { const int a = Y <= 60 ? 0 : Y - 60, r = 4 + 2 * (y_qop(Y - 1) / 20); return a < r ? a : r; }
template <int k> DI void k1_late(K1C& c, SsmS& s, u32x4 (&qo)[16]) {
  if constexpr (k < 4) st16w<(k >> 1) * 65536u + (k & 1) * 1024u>(__builtin_bit_cast(u32x4, s.wf[k]), s.sw, c.vlane, s.wo);
  else { if constexpr ((k & 3) == 0) asm volatile("s_mov_b32 %0, %1" : "=s"(c.dt) : "n"((k - 4) * 1024));
    asm volatile("buffer_store_dwordx4 %0, %1, %2, %3 offen offset:%4 sc1" :: "v"(qo[k - 4]), "v"(c.vlane), "s"(s.sq), "s"(c.dt), "n"((k & 3) * 1024) : "memory"); }
}
template <int Y> DI void ssm_gap(K1C& c, SsmS& s, u32x4 (&qo)[16]) {
  constexpr int e = Y / 3, k = Y % 3, b = e & 3;
  const bf16x8 a0 = cat(s.al[b][0], s.ah[b][0]), a1 = cat(s.al[b][1], s.ah[b][1]);
  if constexpr (k == 0) asm volatile("" :: "v"(a0), "v"(a1), "v"(s.bx[b][0]), "v"(s.bx[b][1]));
  if constexpr (k == 0) { if constexpr (e == 0) mfma32z(s.y1, a0, s.bx[b][0]); else mfma32v(s.y1, a0, s.bx[b][0]); }
  if constexpr (k == 1) { if constexpr (e == 0) mfma32z(s.y0, a0, s.bx[b][1]); else mfma32v(s.y0, a0, s.bx[b][1]); }
  if constexpr (k == 2) mfma32v(s.y1, a1, s.bx[b][1]);
  SBAR();
  if constexpr (e + 2 < 32) ssm_rd<e + 2, k>(s);
  constexpr int n0 = 7 * NQU + Y * (224 - 7 * NQU) / 60, n1 = 7 * NQU + (Y + 1) * (224 - 7 * NQU) / 60;
  if constexpr (Y < 60) sfor<n1 - n0>([&](auto I) AI { k1_epi<(n0 + I) / 7, (n0 + I) % 7>(c); });
  sfor<y_st(Y + 1) - y_st(Y)>([&](auto I) AI { k1_late<y_st(Y) + I>(c, s, qo); });
  sfor<y_qop(Y + 1) - y_qop(Y)>([&](auto I) AI { k1_qop<y_qop(Y) + I>(c, qo); });
  SBAR();
}

template <bool WA> DI void k1_kv(K1C& c, const Params& P, const char* pf2) {
  asm volatile("s_waitcnt vmcnt(8)\n\ts_barrier" ::: "memory");
  sfor<24>([&](auto I) AI { k1_read<0, 0, I>(c, c.f0); });
  SBAR();
  c.sx2 = mk_srd(P.wq, 0u);
  k1_step<WA, 0, 0>(c, P, 2);
#pragma nounroll
  for (int t = 1; t < 13; t += 2) {
    k1_step<WA, 1, 1>(c, P, t + 2);
    c.it = t;
    c.pf = t == 9 ? pf2 : c.pf;
    k1_step<WA, 1, 0>(c, P, t + 3);
  }
  k1_step<WA, 13, 1>(c, P, 15);
  {
    const unsigned lane = lane_id(), r = lane & 31u, hh = lane >> 5, q = (lane & 15u) >> 2, p = lane & 3u, blk = (lane >> 4) & 1u;
    sfor<2>([&](auto I) AI { c.vx_q[I] = (lane >> 3) * (DM * 2) + 16u * ((lane & 7u) ^ (4u * I + (lane >> 4))); c.vwq[I] = hh * WRS + 16u * (r ^ ((2u * I + hh) << 2)); });
    sfor<4>([&](auto I) AI {
      c.xq[I] = (lds_cptr)(size_t)(c.ldsb + (32u * c.wid + r) * 128u + 16u * ((2u * I + hh) ^ ((r >> 1) & 7u))); PINV(c.xq[I]);
      c.wq[I] = (lds_cptr)(size_t)(c.ldsb + 65536u + (8u * hh + q) * 512u + ((unsigned)I ^ q) * 64u + 8u * (2u * p + blk)); PINV(c.wq[I]);
      c.wq1[I] = c.wq[I] + 65536; PINV(c.wq1[I]); });
  }
  k1_step<WA, 14, 0>(c, P, 0);
  k1_step<WA, 15, 1>(c, P, 0);
}

DI unsigned long long phase_proj(const Params& P, char* lds, const unsigned wid, const unsigned qblk, u32x4 (&qo)[16], SsmS& s, srd_t scode) {
  const unsigned lane = lane_id(), wr = wid >> 1, wc = wid & 1;
  const unsigned b = blockIdx.x, xcd = b & 7u, idx = b >> 3;
  const unsigned pm = idx & 7u, which = idx >> 3, h = 2u * xcd + (which >> 1), isV = which & 1u;
  K1C c; c.scode = scode;
  c.ldsb = (unsigned)(size_t)lds; c.lds8 = c.ldsb + wid * 8192u; c.lds4 = c.ldsb + wid * 4096u; c.wid = wid; c.vlane = lane * 16u; c.b = b; c.hb = 16u * h + (idx & 15u);
  const unsigned short* wkv = isV ? P.wuv : P.wuk;
  c.sX = mk_srd(P.ckv, SEQ * DC * 2); c.sW = mk_srd(wkv, DC * WRS); c.sXq = mk_srd(P.x, SEQ * DM * 2); c.sWq = mk_srd(P.wq, DM * WRS);
  sfor<2>([&](auto I) AI { c.vx_kv[I] = (lane >> 3) * (DC * 2) + 16u * ((lane & 7u) ^ (4u * I + (lane >> 4))); });
  sfor<4>([&](auto I) AI { const unsigned f = (2u * (I & 1) + (lane >> 5)) | (4u * (I >> 1)); c.vw[I] = (lane >> 5) * WRS + 16u * ((lane & 31u) ^ (f << 1)); });
  c.sx_kv = (256u * pm + 64u * wid) * (DC * 2); c.sw_kv = 16u * wid * WRS + h * (DH * 2);
  c.sx_q = (128u * qblk + 32u * wid) * (DM * 2);  c.sw_q = c.sw_kv;
  k1_stage<0>(c, 0);
  sfor<8>([&](auto J) AI { k1_piece<1, J>(c, c.sw_kv + 64u * WRS, c.sx_kv + 128u); });
    const unsigned rho = lane & 15u, g = lane >> 4;
  sfor<2>([&](auto I) AI { c.xr_kv[I] = (lds_cptr)lds + (wr * 128u + 16u * (rho >> 3) + (rho & 7u)) * 128u + 16u * ((4u * I + g) ^ (rho >> 1)); PINV(c.xr_kv[I]); });
  { const unsigned q = (lane & 15u) >> 2, p = lane & 3u, f = q | ((g & 1u) << 2);
    sfor<8>([&](auto I) AI { c.wr[I] = (lds_cptr)lds + 65536u + (8u * g + q) * 512u + (8u * wc + ((unsigned)I ^ f)) * 32u + 8u * p; PINV(c.wr[I]); }); }
  c.sO = mk_srd(P.ws + WS_K + (isV ? MB16 : 0u) + (size_t)h * (SEQ * DH * 2), SEQ * DH * 2);
  c.so = (8u * pm + 4u * wr) * 16384u + wc * 8192u;
  const bool pw = lane >= 32u;
  const unsigned k = lane & 15u;
  unsigned pfa = (192u + 8u * pm + 2u * wid + ((lane >> 2) & 1u)) * WRS + h * (DH * 2) + (lane & 3u) * 128u, pfb = (256u * pm + 64u * which + 16u * wid + k) * (DC * 2) + 384u;
  PINV(pfa); PINV(pfb);
  c.pf = (const char*)(pw ? wkv : P.ckv) + (pw ? pfa : pfb);
  c.pfstep = pw ? 64u * WRS : 128u;
  c.tabl = 0u; c.tabh = 0u; c.tabn = 0u;
  sfor<5>([&](auto I) AI { const unsigned long long tp = (unsigned long long)(I == 0 ? P.wq : I == 1 ? P.x : I == 2 ? P.wo : I == 3 ? P.ssmk : P.bq);
    wlane<2 * I + 1>(c.tabl, (unsigned)tp); wlane<2 * I + 1>(c.tabh, (unsigned)(tp >> 32)); wlane<2 * I + 1>(c.tabn, I == 0 || I == 2 ? DM * WRS : SEQ * DM * 2u); });
  c.xoff = ((b * 4u + wid) * 28u + min(lane, 27u)) * 128u;
  const unsigned st0 = (lane & 16u) ? 0u : 3u;
  pfa = (64u * st0 + 4u * (idx & 15u) + wid) * WRS + h * (DH * 2) + (lane & 3u) * 128u; pfb = (128u * qblk + 64u * (idx >> 4) + 16u * wid + k) * (DM * 2) + 128u * st0;
  PINV(pfa); PINV(pfb);
  const char* pf2 = (pw ? (const char*)P.wq : (const char*)P.x) + (pw ? pfa : pfb);
  if (isV) k1_kv<false>(c, P, pf2); else k1_kv<true>(c, P, pf2);
  c.sB = isV ? 16384u : 2048u; c.sA3 = (isV ? 2048u : 16384u) - 3u * c.sB;
  s.c0 = 2u * (28u * (b & 7u) + (b >> 3));
  asm volatile("s_nop 1"); SBAR();
  sfor<224>([&](auto X) AI { k1_qgap<X>(c, s, P); });
  {
    const unsigned ln = lane_id(), tid = wid * 64u + ln, hi = ln >> 5, r = ln & 31u, ch = wid >> 1, dh = wid & 1u, j = idx & 15u;
    asm volatile("s_waitcnt vmcnt(0)" : "+v"(s.xw[0]), "+v"(s.xw[1]) :: "memory");
    if (b < 224u && !__all((((unsigned long long)s.f0[1] << 32) | s.f0[0]) == c.tag)) {
      wait_flags((gu64*)(P.ws + WS_FL) + FL_S0 + (7u * r + (s.c0 >> 6)) * 16u, c.tag);
      sfor<2>([&](auto C) AI { s.xw[C] = __builtin_amdgcn_raw_buffer_load_b128(mk_srd(P.ws + WS_XT, SEQ * DM * 2), (((tid >> 3) * DM + s.c0 + C) * 64u + 8u * (tid & 7u)) * 2u, 0, 16); });
    }
    sfor<2>([&](auto C) AI { *(LAS u32x4*)((LAS char*)lds + XOFF + C * XCH + (32u + (tid >> 2)) * XROW + (tid & 3u) * 16u) = s.xw[C]; });
    const lds_cptr rp = (lds_cptr)lds + WO_LDS + wid * 4096u + (ln >> 2) * 64u + 16u * (ln & 3u);
    sfor<4>([&](auto J) AI { s.wf[J] = cat(lds_tr(rp + (J >> 1) * 2048 + (J & 1) * 8), lds_tr(rp + (J >> 1) * 2048 + (J & 1) * 8 + 1024)); });
    s.sw = mk_srd(P.ws + WS_W + (size_t)h * (DH * DM * 2), DH * DM * 2); s.wo = ((7u * (j & 1u) + wid) * 16u + (j & 14u)) * 1024u;
    s.sq = mk_srd(P.ws + ((size_t)(h * 8u + (j >> 1)) * 4u + wid) * 16384u, (j & 1u) ? 0u : 16384u);
    s.ha = (lds_cptr)lds + SSM_LDS + ch * HCH + (r & 3u) * HCOPY + (24u - 16u * dh - 8u * hi + 4u * (r >> 2)) * 2u; PINV(s.ha);
    s.xa = (lds_cptr)lds + XOFF + ch * XCH + (r + 1u) * XROW + (2u * dh + hi) * 16u; PINV(s.xa);
    asm volatile("s_waitcnt lgkmcnt(0)\n\ts_barrier" ::: "memory");
    sfor<6>([&](auto I) AI { ssm_rd<I / 3, I % 3>(s); });
    c.qs = (f32x2){QSCALE, QSCALE}; PINV(c.qs);
    SBAR();
    sfor<96>([&](auto Y) AI { ssm_gap<Y>(c, s, qo); });
    sfor<20 - y_st(96)>([&](auto I) AI { k1_late<y_st(96) + I>(c, s, qo); });
    asm volatile("s_waitcnt vmcnt(20)\n\ts_barrier" ::: "memory");
    if (wid == 0) flag_store((gu64*)(P.ws + WS_FL) + FL_S1 + b * 16u, c.tag);
    MFMA_DRAIN(); PINV(s.y0); PINV(s.y1);
  }
  return c.tag;
}

constexpr float THR = 8.0f;
constexpr int AQ = 128, AQ2 = 192;
#define NA_OF(pi) (pi == 7 ? 17 : 18)
template <int R> DI void pv_mfma(const bf16x8& a, const u32x4& b) { asm volatile("v_mfma_f32_16x16x32_bf16 a[%c2:%c3], %0, %1, a[%c2:%c3]" :: "v"(a), "v"(b), "n"(R), "n"(R + 3)); }
template <int R> DI void pv_mfmaz(const bf16x8& a, const u32x4& b) { asm volatile("v_mfma_f32_16x16x32_bf16 a[%c2:%c3], %0, %1, 0" :: "v"(a), "v"(b), "n"(R), "n"(R + 3)); }
template <int R> DI void qk_mfma(f32x4& c, const bf16x8& a) { asm volatile("v_mfma_f32_16x16x32_bf16 %0, %1, a[%c2:%c3], %0" : "+v"(c) : "v"(a), "n"(R), "n"(R + 3)); }
template <int R> DI void qk_init(f32x4& d, const bf16x8& a, const f32x4& c) { asm volatile("v_mfma_f32_16x16x32_bf16 %0, %1, a[%c3:%c4], %2" : "=&v"(d) : "v"(a), "v"(c), "n"(R), "n"(R + 3)); }
DI void mfma_vv(f32x4& c, const bf16x8& a, const u32x4& b) { asm volatile("v_mfma_f32_16x16x32_bf16 %0, %1, %2, %0" : "+v"(c) : "v"(a), "v"(b)); }
DI void mfma_vz(f32x4& d, const bf16x8& a, const u32x4& b) { asm volatile("v_mfma_f32_16x16x32_bf16 %0, %1, %2, 0" : "=&v"(d) : "v"(a), "v"(b)); }
DI void use4(const bf16x8& w, const bf16x8& x, const bf16x8& y, const bf16x8& z) { asm volatile("" :: "v"(w), "v"(x), "v"(y), "v"(z)); }
DI float max2f(float a, float b) { float r; asm("v_max_f32_e32 %0, %1, %2" : "=v"(r) : "v"(a), "v"(b)); return r; }
DI void max4(float& m, const f32x4& v) { asm volatile("v_max3_f32 %0, %0, %1, %2\n\tv_max3_f32 %0, %0, %3, %4" : "+v"(m) : "v"(v[0]), "v"(v[1]), "v"(v[2]), "v"(v[3])); }
DI float max4i(const f32x4& v) { float m; asm volatile("v_max_f32_e32 %0, %1, %2\n\tv_max3_f32 %0, %0, %3, %4" : "=&v"(m) : "v"(v[0]), "v"(v[1]), "v"(v[2]), "v"(v[3])); return m; }
DI float row_max(float x) {
  asm volatile("s_nop 1" : "+v"(x));
  auto a = __builtin_amdgcn_permlane32_swap(__float_as_uint(x), __float_as_uint(x), false, false); x = max2f(__uint_as_float(a[0]), __uint_as_float(a[1]));
  asm volatile("s_nop 1" : "+v"(x));
  auto b = __builtin_amdgcn_permlane16_swap(__float_as_uint(x), __float_as_uint(x), false, false); return max2f(__uint_as_float(b[0]), __uint_as_float(b[1]));
}
struct K2C {
  unsigned ldsb, vlane, vlane4;
  srd_t sK;
  lds_cptr kr, vr;
  unsigned kslot_w, vslot_w, vslot_r;
  unsigned ksoff, vsoff, dt;
  f32x4 s[2][4], ci[2][4]; u32x4 p[4]; bf16x8 fr[16];
  f32x4 L[2]; bf16x8 ones;
  float m[2], fct[2], mS[2], mx, thr; bool pend, first;
  unsigned spec, t0off, wbase; int pos, kn1, kend; lds_cptr vbase;
  f32x4 ml, ob[32];
  srd_t sP;
  f32x4 tri[4];
};
template <int J> DI void k2_dma(unsigned& t, unsigned m0v, unsigned voff, srd_t srd, unsigned soff) {
  if constexpr (J == 0) asm volatile("s_mov_b32 m0, %1\n\ts_mov_b32 %0, %4\n\tbuffer_load_dwordx4 %2, %3, %0 offen sc1 lds" : "=&s"(t) : "s"(m0v), "v"(voff), "s"(srd), "s"(soff) : "memory");
  else if constexpr (J == 4) asm volatile("s_add_u32 m0, m0, 4096\n\ts_add_u32 %0, %0, 4096\n\tbuffer_load_dwordx4 %1, %2, %0 offen sc1 lds" : "+s"(t) : "v"(voff), "s"(srd) : "memory", "scc");
  else asm volatile("buffer_load_dwordx4 %0, %1, %2 offen offset:%3 sc1 lds" :: "v"(voff), "s"(srd), "s"(t), "n"((J & 3) * 1024) : "memory");
}
DI void k2_dma8(unsigned m0v, unsigned voff, srd_t srd, unsigned soff) { unsigned t; sfor<8>([&](auto J) AI { k2_dma<J>(t, m0v, voff, srd, soff); }); }
DI void k2_cinit(K2C& c, float r0, float r1, int rel) {
  sfor<2>([&](auto KB) AI {
    const int rb = rel + 32 * KB;
    const float cap = rb > 0 ? -__builtin_inff() : __builtin_inff(), lift = rb < 0 ? __builtin_inff() : -__builtin_inff();
    const float a0 = __builtin_fminf(r0, cap), a1 = __builtin_fminf(r1, cap), b0 = __builtin_fminf(a0, lift), b1 = __builtin_fminf(a1, lift);
    sfor<16>([&](auto E) AI { constexpr int t = E >> 2, i = E & 3; c.ci[KB][t][i] = __builtin_amdgcn_fmed3f(c.tri[t][i], t >> 1 ? a1 : a0, t >> 1 ? b1 : b0); });
    sfor<4>([&](auto T) AI { PINV(c.ci[KB][T]); });
  });
}
DI void s_seleq(unsigned& x, int a, int b, unsigned y) { asm volatile("s_cmp_eq_u32 %1, %2\n\ts_cselect_b32 %0, %3, %0" : "+s"(x) : "s"(a), "s"(b), "s"(y) : "scc"); }
template <int kb, int e> DI void k2_exp(K2C& c) { c.s[kb][e >> 2][e & 3] = __builtin_amdgcn_exp2f(c.s[kb][e >> 2][e & 3]); PINV(c.s[kb][e >> 2]); }
template <int kb, int w> DI void k2_pack(K2C& c) { c.p[2 * kb + (w >> 2)][w & 3] = cvtpk(c.s[kb][w >> 1][2 * (w & 1)], c.s[kb][w >> 1][2 * (w & 1) + 1]); PINV(c.p[2 * kb + (w >> 2)]); }
DI void m0_set(unsigned v) { asm volatile("s_mov_b32 m0, %0" :: "s"(v)); }
DI void m0_bump() { asm volatile("s_add_u32 m0, m0, 4096" ::: "scc"); }
template <int J> DI void k2_ld(const K2C& c, unsigned soff) { asm volatile("buffer_load_dwordx4 %0, %1, %2 offen offset:%3 sc1 lds" :: "v"(J < 4 ? c.vlane : c.vlane4), "s"(c.sK), "s"(soff), "n"((J & 3) * 1024) : "memory"); }
DI void s_inc(unsigned& x) { asm volatile("s_add_u32 %0, %0, 0x8000" : "+s"(x) :: "scc"); }
template <int g> DI void k2_use(K2C& c) { use4(c.fr[g & 15], c.fr[(g + 1) & 15], c.fr[(g + 2) & 15], c.fr[(g + 3) & 15]); }
template <bool FIN, bool QK, bool FL, int g> DI void k2_gapA(K2C& c) {
  constexpr int kb = g >> 4, db = (g >> 1) & 7, m = g & 1;
  if constexpr (QK) {
    if constexpr (db == 0) qk_init<AQ>(c.s[kb][m], c.fr[g & 15], c.ci[kb][m]); else qk_mfma<AQ + 8 * db>(c.s[kb][m], c.fr[g & 15]);
    SBAR();
    if constexpr (g < 24) c.fr[(g + 8) & 15] = lds_rd128(c.kr + (g + 8) * 1024);
  }
  if constexpr (FIN && (QK ? g >= 24 : g < 8)) c.fr[g & 7] = lds_rd128(c.vr + (g & 7) * 1024);
  if constexpr (FIN && g < 10 && (g & 3) != 3) k2_pack<1, g - g / 4>(c);
  if constexpr (QK) {
    if constexpr (FIN && g == 12) m0_bump();
    if constexpr (g == 22) m0_set(c.vslot_w);
    if constexpr (g == 25) s_inc(c.vslot_w);
    if constexpr (g == 20) asm volatile("s_add_u32 %0, %1, %2" : "=s"(c.vsoff) : "s"(c.ksoff), "n"(MB16) : "scc");
    if constexpr (g == 21) s_inc(c.ksoff);
    if constexpr ((g & 3) == 3 && (FIN || g < 31)) { SBAR(); k2_use<g + 1>(c); }
    SBAR();
    if constexpr (db == 0) qk_init<AQ + 4>(c.s[kb][2 + m], c.fr[g & 15], c.ci[kb][2 + m]); else qk_mfma<AQ + 8 * db + 4>(c.s[kb][2 + m], c.fr[g & 15]);
  } else if constexpr (g < 6) { if constexpr (g == 2) { m0_bump(); asm volatile("s_nop 0"); } k2_ld<g + 2>(c, c.ksoff); }
  SBAR();
  if constexpr (FIN && g < 8) k2_exp<1, 8 + g>(c);
  if constexpr (!QK && g < 8) asm volatile("s_nop 0");
  if constexpr (FL) {
    acc_store4<4 * g, 1024 * g>(c.sP, c.vlane);
    if constexpr (g == 13) { float a = c.mS[0], b = c.mS[1], d = c.L[0][0], e = c.L[1][0]; PINV(a); PINV(b); PINV(d); PINV(e);
      st16w<32 * 1024>((u32x4){__float_as_uint(a), __float_as_uint(b), __float_as_uint(d), __float_as_uint(e)}, c.sP, c.vlane, 0u); }
  }
  if constexpr (QK) {
    if constexpr (FIN && g >= 8 && g < 20 && !(g & 1)) k2_ld<(g - 8) / 2 + 2>(c, c.ksoff);
    if constexpr (g >= 24 && !(g & 1)) k2_ld<(g - 24) / 2>(c, c.vsoff);
    if constexpr (g == 19) c.mx = max4i(c.s[0][0]);
    if constexpr (g >= 20 && g < 23) max4(c.mx, c.s[0][g - 19]);
    if constexpr (g == 23) s_seleq(c.vsoff, c.pos, c.kend - 1, c.wbase + 65536u);
    if constexpr (g == 25) s_seleq(c.vslot_w, (int)c.vslot_w, (int)(c.ldsb + 163840u), c.ldsb + 65536u);
    if constexpr (g == 27) s_seleq(c.ksoff, c.pos, c.kn1 - 2, c.t0off);
    if constexpr (g == 29) s_seleq(c.ksoff, c.pos, c.kend - 2, c.wbase);
    if constexpr (g == 31) m0_bump();
  }
  SBAR();
}
template <bool FIN, bool QK, bool FL = false> DI void k2_phaseA(K2C& c) {
  if constexpr (QK && !FIN) sfor<8>([&](auto I) AI { c.fr[I] = lds_rd128(c.kr + I * 1024); });
  sfor<(QK ? 32 : 10)>([&](auto G) AI { k2_gapA<FIN, QK, FL, G>(c); });
}
DI void k2_rebase(K2C& c, bool first) {
  sfor<2>([&](auto Q) AI {
    float x = max4i(c.s[0][2 * Q]); max4(x, c.s[0][2 * Q + 1]); max4(x, c.s[1][2 * Q]); max4(x, c.s[1][2 * Q + 1]);
    const float rm = row_max(x), dl = max2f(rm, first ? -1e30f : 0.f);
    float om = c.m[Q], oms = c.mS[Q]; PINV(om); PINV(oms);
    c.mS[Q] = first ? om : oms; c.fct[Q] = __builtin_amdgcn_exp2f(-dl); c.m[Q] = (first ? 0.f : om) + dl;
    sfor<4>([&](auto E) AI { c.s[E >> 1][2 * Q + (E & 1)] -= dl; });
  });
  c.pend = !first; c.thr = THR; c.first = false; c.spec |= 2u << c.pos;
}
template <bool PV, bool SM, bool FL, int g> DI void k2_gapB(K2C& c) {
  constexpr int kb = g >> 4, nb = g & 15;
  if constexpr (SM && g == 24) {
    asm volatile("s_waitcnt vmcnt(%0) lgkmcnt(0)\n\ts_barrier" :: "n"(FL ? 4 : 8) : "memory");
    SBAR();
  }
  if constexpr (PV) {
    if constexpr (SM && FL && kb == 0) pv_mfmaz<8 * nb>(c.fr[g & 15], c.p[0]); else pv_mfma<8 * nb>(c.fr[g & 15], c.p[2 * kb]);
    SBAR();
    constexpr int f = g < 3 ? 8 + 2 * g : g < 5 ? 11 + g : 12 + g;
    if constexpr (g < 20) c.fr[f & 15] = lds_rd128(c.vr + f * 1024);
    if constexpr (g < 3 || g == 4) c.fr[(f + 1) & 15] = lds_rd128(c.vr + (f + 1) * 1024);
  }
  if constexpr (SM) {
    if constexpr (g >= 24) c.fr[(g + 8) & 15] = lds_rd128(c.kr + (g - 24) * 1024);
    if constexpr (g >= 16 && g < 19) k2_pack<0, g - 16>(c);
    if constexpr (g == 20) { k2_pack<0, 3>(c); k2_pack<0, 4>(c); }
    if constexpr (g == 21) k2_pack<0, 5>(c);
    if constexpr (g == 22) k2_pack<0, 6>(c);
    if constexpr (g == 23) k2_pack<0, 7>(c);
    if constexpr (g == 21) { c.vr = c.vbase + c.vslot_r; PINV(c.vr); }
    if constexpr (g == 22) s_inc(c.vslot_r);
    if constexpr (g == 23) { c.kr = (lds_cptr)(size_t)((unsigned)(size_t)c.kr ^ 32768u); PINV(c.kr); }
    if constexpr (g == 24) m0_set(c.kslot_w);
    if constexpr (g == 26) asm volatile("s_xor_b32 %0, %0, 0x8000" : "+s"(c.kslot_w) :: "scc");
  }
  if constexpr ((g & 3) == 3 && (PV ? (SM || g < 31) : g == 31)) { SBAR(); k2_use<g + 1>(c); }
  SBAR();
  if constexpr (PV) {
    if constexpr (SM && FL && kb == 0) pv_mfmaz<8 * nb + 4>(c.fr[g & 15], c.p[1]); else pv_mfma<8 * nb + 4>(c.fr[g & 15], c.p[2 * kb + 1]);
    SBAR();
  }
  if constexpr (nb == 9 || nb == 13) {
    if constexpr (SM) { if constexpr (kb == 0) k2_ld<(nb == 9 ? 6 : 7)>(c, c.vsoff); else k2_ld<(nb == 9 ? 0 : 1)>(c, c.ksoff); SBAR(); }
    if constexpr (PV) { constexpr int q = nb == 13; if constexpr (SM && FL && kb == 0) mfma_vz(c.L[q], c.ones, c.p[q]); else mfma_vv(c.L[q], c.ones, c.p[2 * kb + q]); SBAR(); }
  }
  if constexpr (!SM && FL) c.ob[g] = __builtin_bit_cast(f32x4, __builtin_amdgcn_raw_buffer_load_b128(c.sP, c.vlane, g * 1024, 16));
  if constexpr (SM) {
    if constexpr (g == 0) k2_ld<4>(c, c.vsoff);
    if constexpr (g == 5) k2_ld<5>(c, c.vsoff);
    if constexpr (PV && g >= 1 && g < 5) max4(c.mx, c.s[1][g - 1]);
    if constexpr (g == 6) {
      if constexpr (!PV) k2_rebase(c, true);
      else if (__builtin_expect(__any(c.mx > c.thr), 0)) k2_rebase(c, c.first);
    }
    if constexpr (g == 23) s_seleq(c.vslot_r, (int)c.vslot_r, 98304, 0u);
    if constexpr (g >= 7 && g < 23) k2_exp<0, g - 7>(c);
    if constexpr (g >= 24) k2_exp<1, g - 24>(c);
    if constexpr (!PV && g >= 7) asm volatile("s_nop 0");
  }
  SBAR();
}
template <bool PV, bool SM, bool FL = false> DI void k2_phaseB(K2C& c) {
  if constexpr (!PV) { MFMA_DRAIN(); SBAR(); }
  if constexpr (!SM) { PINV(c.L[0]); PINV(c.L[1]); }
  sfor<32>([&](auto G) AI { k2_gapB<PV, SM, FL, G>(c); });
}
DI void k2_scaleO(K2C& c) { MFMA_DRAIN(); SBAR(); sfor<64>([&](auto I) AI { acc_scale2<2 * I>(c.fct[(I >> 1) & 1]); });
  PINV(c.L[0]); PINV(c.L[1]); c.L[0] *= c.fct[0]; c.L[1] *= c.fct[1]; PINV(c.L[0]); PINV(c.L[1]); c.pend = false; }

struct K2E { u32x4 cx[16]; lds_cptr rc, rn; unsigned slotw, wsoff, so; srd_t sP; u32x4 pk[2]; };
template <int CH, int g> DI void k2_gapC(K2C& c, K2E& e) {
  constexpr int kb = g >> 4, j = (g >> 1) & 7, m = g & 1;
  if constexpr (CH == 3 && g == 24) { asm volatile("s_waitcnt vmcnt(4)\n\ts_barrier" ::: "memory"); SBAR(); }
  if constexpr (j == 0) mfma_vz(c.s[kb][m], c.fr[g & 15], e.cx[0]); else mfma_vv(c.s[kb][m], c.fr[g & 15], e.cx[2 * j]);
  SBAR();
  if constexpr (CH < 2 && g == 28) { asm volatile("s_barrier" ::: "memory"); SBAR(); }
  if constexpr (g < 24) c.fr[(g + 8) & 15] = lds_rd128(e.rc + (g + 8) * 1024);
  else if constexpr (CH < 6) c.fr[(g + 8) & 15] = lds_rd128(e.rn + (g - 24) * 1024);
  constexpr int u = (g & 15) - 2; constexpr bool EP = g >= 16 || CH > 0;
  constexpr int n = 2 * CH + (g < 16 ? -1 : 0);
  if constexpr (EP && u == 0) asm volatile("s_mov_b32 %0, %1" : "=s"(e.so) : "n"(n * 2048));
  if constexpr (CH < 3 && g == 8) m0_set(e.slotw);
  if constexpr (CH < 3 && g == 24) m0_bump();
  if constexpr ((g & 3) == 3 && (CH < 6 || g < 31)) { SBAR(); k2_use<g + 1>(c); }
  SBAR();
  if constexpr (j == 0) mfma_vz(c.s[kb][2 + m], c.fr[g & 15], e.cx[1]); else mfma_vv(c.s[kb][2 + m], c.fr[g & 15], e.cx[2 * j + 1]);
  SBAR();
  if constexpr (EP && u >= 0 && u < 6) {
    constexpr int a = u / 3, i = u % 3;
    if constexpr (i < 2) { const f32x4& t = c.s[kb ^ 1][2 * a + i]; e.pk[a][2 * i] = cvtpk(t[0], t[1]); e.pk[a][2 * i + 1] = cvtpk(t[2], t[3]); PINV(e.pk[a]); }
    else asm volatile("buffer_store_dwordx4 %0, %1, %2, %3 offen offset:%4 sc1" :: "v"(e.pk[a]), "v"(c.vlane), "s"(e.sP), "s"(e.so), "n"(a * 1024) : "memory");
  }
  if constexpr (CH < 3 && (g & 15) >= 8 && !(g & 1)) k2_ld<4 * kb + ((g & 15) - 8) / 2>(c, e.wsoff);
  SBAR();
}
template <int CH> DI void k2_phaseC(K2C& c, K2E& e) { sfor<32>([&](auto G) AI { k2_gapC<CH, G>(c, e); }); }

template <class F> DI void phase_attn(const Params& P, char* lds, unsigned long long tag, const unsigned wid, u32x4 (&qo)[16], F&& filler) {
  const char *KVW = P.ws + WS_K; char *Pf = P.ws + WS_PF, *part = P.ws + WS_PART; gu64* flags = (gu64*)(P.ws + WS_FL);
  const unsigned b = blockIdx.x, xcd = b & 7u, idx = b >> 3, h = 2u * xcd + (idx >> 4), pi = (idx >> 1) & 7u, roleB = idx & 1u;
  const unsigned pair = h * 8u + pi;
  const int NA = NA_OF(pi);
  const int n1 = roleB ? 32 - 2 * (int)pi - NA : NA, tb1 = roleB ? NA : 0, ntot = roleB ? 34 - NA : NA;
  const unsigned wbase = 2u * MB16 + h * (DH * DM * 2) + wid * 8192u;
  const unsigned t0off = h * (SEQ * DH * 2) + wid * 8192u, k0off = t0off + (unsigned)tb1 * 32768u, k1off = n1 > 1 ? k0off + 32768u : t0off;
  K2C c;
  c.ldsb = (unsigned)(size_t)lds + wid * 8192u; c.vlane = lane_id() * 16u;
  c.sK = mk_srd(KVW, 2u * MB16 + NH * DH * DM * 2);
  unsigned qb = 15u - pi;
  const unsigned grp = (xcd + 8u * ((idx & 16u) + (lane_id() & 15u))) * 16u;
  filler(0);
  if (roleB) sfor<16>([&](auto I) AI { acc_write4<AQ2 + 4 * I>(qo[I]); }); else sfor<16>([&](auto I) AI { acc_write4<AQ + 4 * I>(qo[I]); });
  sfor<32>([&](auto I) AI { acc_zero4<4 * I>(); });
  wait_flags(flags + FL_S1 + grp, tag);
  asm volatile("s_waitcnt vmcnt(0)" ::: "memory");
  k2_dma8(c.ldsb, c.vlane, c.sK, k0off);
  asm volatile("s_waitcnt lgkmcnt(0)\n\ts_barrier" ::: "memory");
  if (wid == 0) flag_store(flags + FL_SQ + b * 16u, tag);
  k2_dma8(c.ldsb + 32768u, c.vlane, c.sK, k1off); k2_dma8(c.ldsb + 65536u, c.vlane, c.sK, k0off + MB16);
  filler(1);
  const unsigned lane = lane_id();
  { const int dq = (int)(lane & 15u) - 16 * (int)(lane >> 5) - 4 * (int)((lane >> 4) & 1u);
    sfor<16>([&](auto E) AI { constexpr int t = E >> 2, i = E & 3; c.tri[t][i] = (8 * (t & 1) + i - 16 * (t >> 1) > dq) ? -__builtin_inff() : __builtin_inff(); });
    sfor<4>([&](auto T) AI { PINV(c.tri[T]); }); }
  c.kr = (lds_cptr)lds + c.vlane; c.kslot_w = c.ldsb; c.vslot_w = c.ldsb + 98304u; c.vslot_r = 0u; c.vlane4 = c.vlane + 4096u; PINV(c.vlane4);
  c.sP = mk_srd(part + ((size_t)pair * 4u + wid) * 33792u, 33792u);
  c.pos = 0; c.kn1 = roleB ? n1 : 99; c.kend = ntot; c.t0off = t0off; c.wbase = wbase; c.ksoff = k1off; c.vbase = (lds_cptr)lds + 65536u + c.vlane;
  c.spec = (15u << (ntot - 3)) | (roleB ? (n1 > 1 ? 15u << (n1 - 2) : 7u) : 0u);
  int relb = 64 * tb1 - (int)(qb * 128u + wid * 32u);
  u32x2 fw = {0u, 0u};
  auto prelude = [&]() AI {
    if (__builtin_expect((c.spec >> c.pos) & 1u, 0)) {
      if (c.pend) k2_scaleO(c);
      float r0 = -c.m[0], r1 = -c.m[1];
      if (c.pos == c.kn1) {
        sfor<16>([&](auto I) AI { acc_mov4<AQ + 4 * I, AQ2 + 4 * I>(); });
        r0 = 0.f; r1 = 0.f; c.thr = -__builtin_inff(); c.first = true; relb = -64 * n1 - (int)(pi * 128u + wid * 32u);
      }
      k2_cinit(c, r0, r1, relb + 64 * c.pos);
      if (!roleB && c.pos == ntot - 1) asm volatile("s_nop 4\n\tbuffer_load_dwordx2 a[192:193], off, %0, 0 sc1" :: "s"(mk_srd(P.ws + WS_FL + (FL_S2 + pair * 16u) * 8u, 8u)) : "memory");
      if (c.pos == ntot - 2) { PINV(fw); if (!__all((((unsigned long long)fw[1] << 32) | fw[0]) == tag)) wait_flags(flags + FL_SQ + grp, tag); }
      if (c.pos == ntot - 3) asm volatile("buffer_load_dwordx2 %0, %1, %2, 0 offen sc1" : "+v"(fw) : "v"((FL_SQ + grp) * 8u), "s"(mk_srd(P.ws + WS_FL, 1u << 20)) : "memory");
      asm volatile("s_nop 1");
    }
    SBAR();
  };
  sfor<2>([&](auto Q) AI { c.L[Q] = f32x4{}; PINV(c.L[Q]); c.m[Q] = 0.f; c.fct[Q] = 1.f; c.mS[Q] = 0.f; });
  c.ones = (bf16x8){0x3f80, 0x3f80, 0x3f80, 0x3f80, 0x3f80, 0x3f80, 0x3f80, 0x3f80}; PINV(c.ones);
  c.pend = false; c.thr = THR; c.first = false;
  k2_cinit(c, 0.f, 0.f, relb);
  SBAR();
  if (roleB) {
    wait_flags(flags + FL_SQ + (b - 8u) * 16u, tag);
    const srd_t sq = mk_srd(P.ws + ((size_t)pair * 4u + wid) * 16384u, 16384u);
    sfor<16>([&](auto I) AI { acc_load4<AQ + 4 * I, I * 1024>(sq, c.vlane); });
    asm volatile("s_waitcnt vmcnt(0)" ::: "memory");
  }
  asm volatile("s_waitcnt vmcnt(16) lgkmcnt(0)\n\ts_barrier" ::: "memory");
  k2_phaseA<false, true>(c); k2_phaseB<false, true>(c);
  if (wid == 0) flag_store(flags + FL_S4 + b * 16u, tag);
  ++c.pos;
  auto run = [&](int stop) AI {
#pragma nounroll
    while (c.pos < stop) {
      prelude();
#pragma nounroll
      do { k2_phaseA<true, true>(c); k2_phaseB<true, true>(c); ++c.pos; } while (!((c.spec >> c.pos) & 1u));
    }
  };
  run(roleB ? n1 + 1 : ntot);
  if (roleB) {
    prelude();
    k2_phaseA<true, true, true>(c); k2_phaseB<true, true, true>(c);
    if (wid == 0) flag_store(flags + FL_S2 + pair * 16u, tag);
    ++c.pos;
    run(ntot);
  }
  if (c.pend) k2_scaleO(c);
  qb = roleB ? pi : qb;
  const u32x4 f2 = __builtin_bit_cast(u32x4, acc_read4<AQ2>());
  const bool early = !roleB && rfl(f2[0]) == (unsigned)tag && rfl(f2[1]) == (unsigned)(tag >> 32);
  SBAR();
  k2_phaseA<true, false>(c);
  k2_dma8(c.vslot_w, c.vlane, c.sK, wbase + 3u * 32768u);
  if (early) { c.ml = __builtin_bit_cast(f32x4, __builtin_amdgcn_raw_buffer_load_b128(c.sP, c.vlane, 32 * 1024, 16)); SBAR(); k2_phaseB<true, false, true>(c); } else k2_phaseB<true, false>(c);
  MFMA_DRAIN(); SBAR();
  PINV(c.L[0]); PINV(c.L[1]);
  const float l[2] = {c.L[0][0], c.L[1][0]};
  K2E e;
  if (roleB) {
    const float rl[2] = {1.0f / l[0], 1.0f / l[1]};
    sfor<16>([&](auto I) AI { constexpr int r = 16 * (I >> 1) + 4 * (I & 1); e.cx[I] = pack8(acc_read4<r>(), acc_read4<r + 8>(), rl[I & 1]); PINV(e.cx[I]); });
  } else {
    if (!early) {
      wait_flags(flags + FL_S2 + pair * 16u, tag);
      c.ml = __builtin_bit_cast(f32x4, __builtin_amdgcn_raw_buffer_load_b128(c.sP, c.vlane, 32 * 1024, 16));
      sfor<32>([&](auto K) AI { c.ob[K] = __builtin_bit_cast(f32x4, __builtin_amdgcn_raw_buffer_load_b128(c.sP, c.vlane, K * 1024, 16)); });
    }
    const f32x4 ml = c.ml;
    SBAR();
    float fa[2], fb[2];
    sfor<2>([&](auto Q) AI { const float mm = fmaxf(c.m[Q], ml[0 + Q]), wa = __builtin_amdgcn_exp2f(c.m[Q] - mm), wb = __builtin_amdgcn_exp2f(ml[0 + Q] - mm), rl = 1.0f / (wa * l[Q] + wb * ml[2 + Q]); fa[Q] = wa * rl; fb[Q] = wb * rl; });
    sfor<16>([&](auto I) AI { constexpr int r = 16 * (I >> 1) + 4 * (I & 1), q = I & 1;
      e.cx[I] = pack8(acc_read4<r>() * fa[q] + c.ob[r / 4] * fb[q], acc_read4<r + 8>() * fa[q] + c.ob[r / 4 + 2] * fb[q], 1.0f); PINV(e.cx[I]); });
  }
  e.sP = mk_srd(Pf + ((size_t)h * 64u + qb * 4u + wid) * 28672u, 28672u);
  const unsigned kA = c.kslot_w - c.ldsb, kB = kA ^ 32768u;
  const unsigned v2 = 65536u + c.vslot_r, v3 = c.vslot_w - c.ldsb, v4 = 294912u - v2 - v3;
  const lds_cptr lb = (lds_cptr)lds + c.vlane;
  asm volatile("s_waitcnt vmcnt(0)\n\ts_barrier" ::: "memory");
  e.rn = lb + kA; PINV(e.rn);
  sfor<8>([&](auto I) AI { c.fr[I] = lds_rd128(e.rn + I * 1024); });
  auto next = [&](unsigned rn, unsigned slot, unsigned chunk) AI { e.rc = e.rn; e.rn = lb + rn; PINV(e.rn); e.slotw = c.ldsb + slot; e.wsoff = wbase + chunk * 32768u; SBAR(); };
  next(kB, v4, 4u); k2_phaseC<0>(c, e);
  next(v2, kA, 5u); k2_phaseC<1>(c, e);
  next(v3, kB, 6u); k2_phaseC<2>(c, e);
  next(v4, 0u, 0u); k2_phaseC<3>(c, e);
  next(kA, 0u, 0u); k2_phaseC<4>(c, e);
  next(kB, 0u, 0u); k2_phaseC<5>(c, e);
  next(kB, 0u, 0u); k2_phaseC<6>(c, e);
  MFMA_DRAIN(); sfor<4>([&](auto I) AI { PINV(c.s[1][I]); }); SBAR();
  st16w<26 * 1024>(pack8(c.s[1][0], c.s[1][1], 1.0f), e.sP, c.vlane, 0u); st16w<27 * 1024>(pack8(c.s[1][2], c.s[1][3], 1.0f), e.sP, c.vlane, 0u);
}
constexpr int OUTW = 2 * DM;
template <int R> DI void reduce_sum(unsigned task, unsigned lane, const bf16x8& sel0, const bf16x8& sel1, unsigned short* out) {
  const unsigned rb = task / 28u, f = task % 28u;
  f32x4 lo = {}, hi = {}; PINV(lo); PINV(hi); asm volatile("s_nop 1");
  sfor<16>([&](auto H) AI { qk_mfma<R + 4 * H>(lo, sel0); qk_mfma<R + 4 * H>(hi, sel1); });
  MFMA_DRAIN(); PINV(lo); PINV(hi);
  *(u32x4*)(out + (size_t)(32u * rb + 16u * (f & 1u) + (lane & 15u)) * OUTW + 32u * (f >> 1) + 8u * (lane >> 4)) = pack8(lo, hi, 1.0f);
}

DI void ssm_finish(SsmS& s, const Params& P, char* lds, const unsigned wid, int part) {
  const unsigned lane = lane_id(), tid = wid * 64u + lane, hi = lane >> 5, r = lane & 31u;
  LAS char* ys = (LAS char*)lds + SSM_LDS + wid * 8192u;
  if (part == 0) {
#pragma unroll
  for (int k = 0; k < 4; ++k) {
    const unsigned o = r * 128u + (((2u * k + hi) ^ (r & 7u)) << 4);
    *(LAS f32x4*)(ys + o) = (f32x4){s.y0[4 * k], s.y0[4 * k + 1], s.y0[4 * k + 2], s.y0[4 * k + 3]};
    *(LAS f32x4*)(ys + 4096u + o) = (f32x4){s.y1[4 * k], s.y1[4 * k + 1], s.y1[4 * k + 2], s.y1[4 * k + 3]};
  }
  return; }
  const unsigned row = tid >> 2, yo0 = row * 128u + (((2u * (tid & 3u)) ^ (row & 7u)) << 4), yo1 = row * 128u + (((2u * (tid & 3u) + 1u) ^ (row & 7u)) << 4);
  const srd_t sz = mk_srd(P.ws + WS_ZT, SEQ * DM * 2);
  sfor<2>([&](auto C) AI {
    const LAS char* yp = (const LAS char*)lds + SSM_LDS + C * 16384;
    const f32x4 a = *(const LAS f32x4*)(yp + yo0) + *(const LAS f32x4*)(yp + 8192 + yo0), bb = *(const LAS f32x4*)(yp + yo1) + *(const LAS f32x4*)(yp + 8192 + yo1);
    const float dd = bf2f(C ? s.dw >> 16 : s.dw & 0xffffu);
    u32x4 z;
#pragma unroll
    for (int i = 0; i < 4; ++i) {
      const f32x2 kv = up2(s.xw[C][3 - i]), y = rb2(i < 2 ? (f32x2){a[2 * i], a[2 * i + 1]} : (f32x2){bb[2 * i - 4], bb[2 * i - 3]}), kd = rb2((f32x2){kv[1] * dd, kv[0] * dd});
      z[i] = cvtpk(y[0] + kd[0], y[1] + kd[1]);
    }
    st16w<C * 128>(z, sz, (((tid >> 3) * DM + s.c0) * 64u + 8u * (tid & 7u)) * 2u, 0u);
  });
}
struct GateS { unsigned qw[2][8]; u32x4 z[2][2]; unsigned long long f; };
DI gu64* gate_flag(const Params& P, unsigned g0) {
  const unsigned lane = lane_id(), pr = 32u * ((g0 + (lane >> 5)) % 7u) + (lane & 31u);
  return (gu64*)(P.ws + WS_FL) + FL_S4 + (pr / 28u + 8u * (pr % 28u)) * 16u;
}
DI void gate_load1(GateS& s, const Params& P, unsigned g0, const unsigned wid) {
  const unsigned tid = wid * 64u + lane_id();
  s.f = __hip_atomic_load(gate_flag(P, g0), RLX_AGENT);
  sfor<16>([&](auto I) AI { const unsigned blk = g0 + (I >> 3); s.qw[I >> 3][I & 7] = *(const unsigned*)(P.bq + (size_t)(64u * (blk / 7u) + 8u * (tid >> 5) + (I & 7)) * DM + 64u * (blk % 7u) + 2u * (tid & 31u)); });
}
DI void gate_load2(GateS& s, const Params& P, unsigned g0, unsigned long long tag, const unsigned wid) {
  const unsigned tid = wid * 64u + lane_id();
  if (!__all(s.f == tag)) wait_flags(gate_flag(P, g0), tag);
  const srd_t sz = mk_srd(P.ws + WS_ZT, SEQ * DM * 2);
  sfor<4>([&](auto I) AI { const unsigned blk = g0 + (I >> 1);
    s.z[I >> 1][I & 1] = __builtin_amdgcn_raw_buffer_load_b128(sz, (((blk / 7u) * DM + 64u * (blk % 7u) + 2u * (tid & 31u) + (I & 1)) * 64u + 8u * (tid >> 5)) * 2u, 0, 16); });
}
DI void gate_store(GateS& s, const Params& P, unsigned g0, const unsigned wid) {
  const unsigned tid = wid * 64u + lane_id();
  sfor<16>([&](auto I) AI { constexpr int u = I >> 3, i = I & 7; const unsigned blk = g0 + u;
    const f32x2 q = up2(s.qw[u][i]), a = up2(s.z[u][0][i >> 1]), bb = up2(s.z[u][1][i >> 1]);
    *(unsigned*)(P.out + (size_t)(64u * (blk / 7u) + 8u * (tid >> 5) + i) * OUTW + DM + 64u * (blk % 7u) + 2u * (tid & 31u)) = cvtpk(q[0] * a[i & 1], q[1] * bb[i & 1]); });
}

__global__ __launch_bounds__(256, 1) void k1_fused(Params P) {
#ifdef __HIP_DEVICE_COMPILE__
  extern __shared__ __attribute__((aligned(16))) char lds[];
  const unsigned wid = rfl(threadIdx.x >> 6);
  const unsigned b = blockIdx.x, xcd = b & 7u, idx = b >> 3, h = 2u * xcd + (idx >> 4), j = idx & 15u;
  ACC_CLAIM();
  srd_t scode;
  { unsigned long long pc; unsigned len;
    asm volatile("s_getpc_b64 %0\n.Lcode_begin:\n\ts_mov_b32 %1, .Lcode_end-.Lcode_begin" : "=s"(pc), "=s"(len));
    scode = mk_srd((const void*)pc, len); }
  if (wid == 3) touch_lds((unsigned)(size_t)lds, scode, code_off());
  gu64* flags = (gu64*)(P.ws + WS_FL);
  const unsigned qb = (j & 1u) ? (j >> 1) : 15u - (j >> 1);
  const bool ssm = b < 224u;
  u32x4 qo[16]; SsmS s;
  const unsigned long long tag = phase_proj(P, lds, wid, qb, qo, s, scode);
  phase_attn(P, lds, tag, wid, qo, [&](int part) AI { if (ssm) ssm_finish(s, P, lds, wid, part); });
  if (!(j & 1u) && j >= 2u) {
    const unsigned g0 = 2u * (7u * h + (j >> 1) - 1u);
    GateS gs;
    gate_load1(gs, P, g0, wid);
    publish(flags + FL_S3 + b * 16u, tag, wid);
    gate_load2(gs, P, g0, tag, wid);
    gate_store(gs, P, g0, wid);
  } else publish(flags + FL_S3 + b * 16u, tag, wid);
  const unsigned lane = lane_id();
  const unsigned task = qb * 112u + h * 7u + wid;
  const srd_t sPa = mk_srd(P.ws + WS_PF + (size_t)task * 1024u, NH * 64u * 28672u - task * 1024u), sPb = mk_srd(P.ws + WS_PF + (size_t)(task + 4u) * 1024u, wid < 3u ? NH * 64u * 28672u - (task + 4u) * 1024u : 0u);
  gu64* fl = flags + FL_S3 + (((lane & 15u) >> 1) + 8u * (16u * (lane & 1u) + j)) * 16u;
  const unsigned one = (lane >> 4) == ((lane & 15u) >> 2) ? 0x3f80u << (16u * (lane & 1u)) : 0u, w0 = (lane & 2u) ? 0u : one, w1 = (lane & 2u) ? one : 0u;
  bf16x8 sel0 = __builtin_bit_cast(bf16x8, (u32x4){w0, w1, 0u, 0u}), sel1 = __builtin_bit_cast(bf16x8, (u32x4){0u, 0u, w0, w1}); PINV(sel0); PINV(sel1);
  unsigned done = 0u;
  for (int spin = 0; spin < (1 << 20) && done != 0xffffu; ++spin) {
    const unsigned go = (unsigned)__ballot(__hip_atomic_load(fl, RLX_AGENT) == tag) & 0xffffu & ~done;
    sfor<16>([&](auto H) AI { if ((go >> H) & 1u) { acc_load4<4 * H, H * (64u * 28672u)>(sPa, lane * 16u); acc_load4<64 + 4 * H, H * (64u * 28672u)>(sPb, lane * 16u); } });
    done |= go;
    if (!go) __builtin_amdgcn_s_sleep(1);
  }
  if (b == 0 && wid == 0) flag_store(flags + FL_EPOCH, tag);
  asm volatile("s_waitcnt vmcnt(0)" ::: "memory");
  reduce_sum<0>(task, lane, sel0, sel1, P.out);
  if (wid < 3u) reduce_sum<64>(task + 4u, lane, sel0, sel1, P.out);
  asm volatile(".Lcode_end:");
#endif
}

extern "C" void kernel_launch(void* const* d_in, const int* in_sizes, int n_in, void* d_out, int out_size, void* d_ws, size_t ws_size, hipStream_t stream) {
  typedef const unsigned short* cu16;
  Params P = {(cu16)d_in[0], (cu16)d_in[1], (cu16)d_in[2], (cu16)d_in[3], (cu16)d_in[4], (cu16)d_in[5], (cu16)d_in[6], (cu16)d_in[7], (cu16)d_in[8], (cu16)d_in[9], (cu16)d_in[10], (cu16)d_in[11],
              (unsigned short*)d_out, (char*)d_ws};
  (void)hipFuncSetAttribute((const void*)k1_fused, hipFuncAttributeMaxDynamicSharedMemorySize, 163840);
  k1_fused<<<256, 256, 163840, stream>>>(P);
}
```
